```python
import torch

def _softmax(x, axis=-1):
    return torch.softmax(x, dim=axis)
def _gelu(x):
    return torch.nn.functional.gelu(x, approximate="tanh")
def _silu(x):
    return torch.nn.functional.silu(x)
def _sigmoid(x):
    return torch.sigmoid(x)
def _layernorm(x, axis=-1, eps=1e-5):
    m = x.mean(dim=axis, keepdim=True)
    v = x.var(dim=axis, keepdim=True, unbiased=False)
    return (x - m) / torch.sqrt(v + eps)
def _scatter_add(tgt, idx, src, axis=0):
    return tgt.clone().index_add_(axis, idx, src)
def _segment_sum(src, seg, n):
    out = torch.zeros((n,) + src.shape[1:], dtype=src.dtype)
    return out.index_add_(0, seg, src)
def _pad(x, pw):
    flat = tuple(v for pair in reversed(pw) for v in pair)
    return torch.nn.functional.pad(x, flat)

kernel_name = 'gated_mix__ssm_conv_h3__glu_gated_geglu__prompt'

def setup_inputs():

    _w = (lambda a: (lambda t: t.to(torch.bfloat16) if t.is_floating_point() else t)(torch.as_tensor(a)))
    def _A_setup():
        B, S, D = 1, 2048, 512
        ks = 4
        return {
            "q": _w((lambda *s: torch.randn(*s, dtype=torch.bfloat16) * 0.05)(B, S, D)),
            "k": _w((lambda *s: torch.randn(*s, dtype=torch.bfloat16) * 0.05)(B, S, D)),
            "v": _w((lambda *s: torch.randn(*s, dtype=torch.bfloat16) * 0.05)(B, S, D)),
            "shift_k": _w((lambda *s: torch.randn(*s, dtype=torch.bfloat16) * 0.05)(D, ks) * 0.5),
            "ssm_k": _w((lambda *s: torch.randn(*s, dtype=torch.bfloat16) * 0.05)(D, S) * (1.0 / S ** 0.5)),
            "ssm_D": _w((lambda *s: torch.randn(*s, dtype=torch.bfloat16) * 0.05)(D)),
        }
    _A_in = _A_setup()

    def _B_setup():
        B, S, Dm, Df = 1, 2048, 512, 1792
        return {
            "x": _w((lambda *s: torch.randn(*s, dtype=torch.bfloat16) * 0.05)(B, S, Dm)),
            "w_gate": _w((lambda *s: torch.randn(*s, dtype=torch.bfloat16) * 0.1)(Dm, Df) * (Dm ** -0.5)),
            "w_up": _w((lambda *s: torch.randn(*s, dtype=torch.bfloat16) * 0.1)(Dm, Df) * (Dm ** -0.5)),
            "w_down": _w((lambda *s: torch.randn(*s, dtype=torch.bfloat16) * 0.1)(Df, Dm) * (Df ** -0.5)),
        }
    _B_in = _B_setup()

    return {
        "A_q": _A_in["q"],
        "A_k": _A_in["k"],
        "A_v": _A_in["v"],
        "A_shift_k": _A_in["shift_k"],
        "A_ssm_k": _A_in["ssm_k"],
        "A_ssm_D": _A_in["ssm_D"],
        "B_x": _B_in["x"],
        "B_w_gate": _B_in["w_gate"],
        "B_w_up": _B_in["w_up"],
        "B_w_down": _B_in["w_down"],
        "gate_w": _w((lambda *s: torch.randn(*s, dtype=torch.bfloat16) * 0.05)(512) * 16.0),
    }

def reference(A_q, A_k, A_v, A_shift_k, A_ssm_k, A_ssm_D, B_x, B_w_gate, B_w_up, B_w_down, gate_w):
    def _A_ref(q, k, v, shift_k, ssm_k, ssm_D):
        B, S, D = q.shape
        ks = shift_k.shape[-1]
        k_pad = _pad(k, ((0, 0), (ks - 1, 0), (0, 0)))
        k_shift = torch.zeros_like(k)
        for t in range(ks):
            k_shift = k_shift + k_pad[:, t:t + S, :] * shift_k[:, ks - 1 - t][None, None, :]
        kv = k_shift * v
        L = 2 * S
        kv_pad = _pad(kv, ((0, 0), (0, S), (0, 0)))
        kf_pad = _pad(ssm_k, ((0, 0), (0, S)))
        kv_f = (lambda x, axes=(-2,-1): torch.fft.rfft2(x.float(), dim=axes))(kv_pad[:, None, :, :], axes=(1, 2))
        kf_f = (lambda x, axes=(-2,-1): torch.fft.rfft2(x.float(), dim=axes))(kf_pad[:, None, :], axes=(1, 2))
        kf_f = (lambda x, *d: x.permute(*d))(kf_f, 1, 2, 0)
        y_f = kv_f * kf_f[None, :, :, :]
        y = (lambda x, s, axes=(-2,-1): torch.fft.irfft2(x, s=s, dim=axes).to(torch.bfloat16))(y_f, (1, L), axes=(1, 2))[:, 0, :S, :]
        y = y + kv * ssm_D[None, None, :]
        return q * y

    def _B_ref(x, w_gate, w_up, w_down):
        g = torch.einsum("bsd,df->bsf", x, w_gate)
        u = torch.einsum("bsd,df->bsf", x, w_up)
        h = _gelu(g) * u
        return torch.einsum("bsf,fd->bsd", h, w_down)

    x = A_q
    g = _sigmoid((x * gate_w).sum(-1, keepdims=True))
    a = _A_ref(x, A_k, A_v, A_shift_k, A_ssm_k, A_ssm_D)
    b = _B_ref(x, B_w_gate, B_w_up, B_w_down)
    return g * a + (1 - g) * b
```

```cpp
#include <hip/hip_runtime.h>
#include <hip/hip_bf16.h>
#include <utility>
#pragma clang diagnostic ignored "-Winline-asm"
#pragma clang diagnostic ignored "-Wint-to-pointer-cast"

#define DI __device__ __forceinline__
#define LAS __attribute__((address_space(3)))
#define GAS __attribute__((address_space(1)))
typedef unsigned short u16;
typedef float f32x16 __attribute__((ext_vector_type(16)));
typedef float f32x4 __attribute__((ext_vector_type(4)));
typedef float f32x2 __attribute__((ext_vector_type(2)));
typedef __bf16 bf16x8 __attribute__((ext_vector_type(8)));
typedef __bf16 bf16x2 __attribute__((ext_vector_type(2)));
typedef short s16x4 __attribute__((ext_vector_type(4)));
typedef short s16x8 __attribute__((ext_vector_type(8)));
typedef unsigned u32x4 __attribute__((ext_vector_type(4)));
typedef unsigned u32x2 __attribute__((ext_vector_type(2)));
typedef __amdgpu_buffer_rsrc_t rsrc_t;
template <int... Is> using iseq = std::integer_sequence<int, Is...>;
template <int N> using make_iseq = std::make_integer_sequence<int, N>;

constexpr int S_ = 2048, D_ = 512, F_ = 1792;
constexpr size_t WS_KVT = 0;
constexpr size_t WS_Y2T = WS_KVT + 2u * 1024 * 1024;
constexpr size_t WS_H   = WS_Y2T + 2u * 1024 * 1024;
constexpr size_t WS_G   = WS_H + 64u * 112 * 1024;
constexpr size_t WS_END = WS_G + 8192;

#define SB() __builtin_amdgcn_sched_barrier(0)
DI unsigned rfl(unsigned x) { return __builtin_amdgcn_readfirstlane(x); }
DI rsrc_t mk_rsrc(const void* p, unsigned bytes) { return __builtin_amdgcn_make_buffer_rsrc((void*)p, 0, bytes, 0x00020000); }
DI unsigned cvtpk(float lo, float hi) { f32x2 v = {lo, hi}; bf16x2 b = __builtin_convertvector(v, bf16x2); return __builtin_bit_cast(unsigned, b); }
DI float bflo(unsigned x) { return __uint_as_float(x << 16); }
DI float bfhi(unsigned x) { return __uint_as_float(x & 0xffff0000u); }
DI void dma16(rsrc_t r, unsigned voff, unsigned soff, unsigned ldsaddr) {
  asm volatile("s_mov_b32 m0, %0\n\ts_nop 0\n\tbuffer_load_dwordx4 %1, %2, %3 offen lds" :: "s"(ldsaddr), "v"(voff), "s"(r), "s"(soff) : "m0", "memory");
}
template <int N> DI void wait_vm() { asm volatile("s_waitcnt vmcnt(%0)" :: "i"(N) : "memory"); }
DI void wait_lgkm0() { asm volatile("s_waitcnt lgkmcnt(0)" ::: "memory"); }
DI void barrier() { asm volatile("s_barrier" ::: "memory"); }
DI bf16x8 lds_b128(unsigned a) { return *(const LAS bf16x8*)a; }
DI bf16x8 lds_tr2(unsigned a0, unsigned a1) {
  s16x4 lo = __builtin_amdgcn_ds_read_tr16_b64_v4i16((LAS s16x4*)a0);
  s16x4 hi = __builtin_amdgcn_ds_read_tr16_b64_v4i16((LAS s16x4*)a1);
  s16x8 v = __builtin_shufflevector(lo, hi, 0, 1, 2, 3, 4, 5, 6, 7);
  return __builtin_bit_cast(bf16x8, v);
}
#define MFMA32(a, b, c) __builtin_amdgcn_mfma_f32_32x32x16_bf16((a), (b), (c), 0, 0, 0)
#define MFMA16(a, b, c) __builtin_amdgcn_mfma_f32_16x16x32_bf16((a), (b), (c), 0, 0, 0)

DI f32x4 selv(unsigned m, f32x4 a, f32x4 b) {
  f32x4 r;
#pragma unroll
  for (int e = 0; e < 4; ++e) r[e] = __uint_as_float((__float_as_uint(a[e]) & m) | (__float_as_uint(b[e]) & ~m));
  return r;
}
__global__ __launch_bounds__(256) void k_pre(const u16* __restrict__ q, const u16* __restrict__ k, const u16* __restrict__ v,
                                             const u16* __restrict__ shk, const u16* __restrict__ gw, char* __restrict__ ws) {
  __shared__ __attribute__((aligned(16))) unsigned T[64 * 32];
  const int t = threadIdx.x, sb = blockIdx.x >> 3, db = blockIdx.x & 7;
  const int s0 = sb * 64, d0 = db * 64;
  const int c = t & 7, rg = t >> 3;
  const int s = s0 + 2 * rg;
  u32x4 kr[5], vr[2], w4[4];
#pragma unroll
  for (int i = 0; i < 5; ++i) { int ss = s - 3 + i; kr[i] = ss >= 0 ? *(const u32x4*)(k + ss * D_ + d0 + c * 8) : u32x4{0, 0, 0, 0}; }
#pragma unroll
  for (int i = 0; i < 2; ++i) vr[i] = *(const u32x4*)(v + (s + i) * D_ + d0 + c * 8);
#pragma unroll
  for (int i = 0; i < 4; ++i) w4[i] = *(const u32x4*)(shk + (d0 + c * 8) * 4 + i * 8);
#pragma unroll
  for (int j = 0; j < 8; ++j) {
    unsigned wa = w4[j >> 1][(j & 1) * 2], wb = w4[j >> 1][(j & 1) * 2 + 1];
    float w0 = bflo(wa), w1 = bfhi(wa), w2 = bflo(wb), w3 = bfhi(wb);
    float kk[5];
#pragma unroll
    for (int i = 0; i < 5; ++i) kk[i] = (j & 1) ? bfhi(kr[i][j >> 1]) : bflo(kr[i][j >> 1]);
    float a0 = kk[0] * w3 + kk[1] * w2 + kk[2] * w1 + kk[3] * w0;
    float a1 = kk[1] * w3 + kk[2] * w2 + kk[3] * w1 + kk[4] * w0;
    float v0 = (j & 1) ? bfhi(vr[0][j >> 1]) : bflo(vr[0][j >> 1]);
    float v1 = (j & 1) ? bfhi(vr[1][j >> 1]) : bflo(vr[1][j >> 1]);
    unsigned pk = cvtpk(a1 * v1, a0 * v0);
    int w = 31 - rg, dl = 8 * c + j;
    T[dl * 32 + ((((w >> 2) ^ c) & 7) << 2) + (w & 3)] = pk;
  }
  {
    const int wv = t >> 6, lane = t & 63;
    u32x4 g4 = *(const u32x4*)(gw + lane * 8);
#pragma unroll
    for (int r = 0; r < 2; ++r) {
      int row = s0 + 8 * db + wv * 2 + r;
      u32x4 x4 = *(const u32x4*)(q + row * D_ + lane * 8);
      float a = 0.f;
#pragma unroll
      for (int j = 0; j < 4; ++j) a += bflo(x4[j]) * bflo(g4[j]) + bfhi(x4[j]) * bfhi(g4[j]);
#pragma unroll
      for (int m = 32; m >= 1; m >>= 1) a += __shfl_xor(a, m);
      if (lane == 0) ((float*)(ws + WS_G))[row] = 1.f / (1.f + __expf(-a));
    }
  }
  __syncthreads();
  {
    const int wv = t >> 6, lane = t & 63;
#pragma unroll
    for (int i = 0; i < 2; ++i) {
      int dl = (wv * 2 + i) * 8 + (lane >> 3), ch = lane & 7;
      u32x4 val = *(const u32x4*)&T[dl * 32 + ((ch ^ (dl >> 3)) & 7) * 4];
      *(u32x4*)(ws + WS_KVT + (size_t)(d0 + dl) * 4096 + (1984 - s0) * 2 + ch * 16) = val;
    }
  }
}

constexpr unsigned G1_XS = 16384, G1_WIMG = 14336 + 128, G1_WS = 14336 * 2 + 128;
constexpr unsigned G1_XOFF = 0, G1_WOFF = 3 * G1_XS, G1_LDS = G1_WOFF + 3 * G1_WS;

struct G1 {
  rsrc_t xs, wsr;
  unsigned xv[2], wv[7];
  unsigned xsoff, wsoff, xm0, wm0;
  unsigned xa[4], xb[4], wb[3];
  f32x16 acc[3][2], acc6;
  bf16x8 XA[2], XB[2], W[2][4];
};
template <int ST, int N> DI void g1_dma(G1& c) {
  constexpr int buf = ST % 3;
  if constexpr (N < 4) dma16(c.xs, c.xv[N & 1], c.xsoff + N * 8192 + ST * 128, c.xm0 + buf * G1_XS + N * 1024);
  else dma16(c.wsr, c.wv[N - 4], c.wsoff + ST * 64 * 3584, c.wm0 + buf * G1_WS + (N - 4) * 1024);
}
template <int ST, int... Ns> DI void g1_dma_all(G1& c, iseq<Ns...>) { (g1_dma<ST, Ns>(c), ...); }
template <int I, int R> DI void g1_read(G1& c, unsigned t3off) {
  constexpr int st = I >> 2, kk = I & 3, buf = st % 3, set = I & 1;
  if constexpr (R == 0) c.XA[set] = lds_b128(c.xa[kk] + buf * G1_XS);
  else if constexpr (R == 2) c.XB[set] = lds_b128(c.xb[kk] + buf * G1_XS);
  else {
    constexpr int p = R == 1 ? 0 : R - 2;
    constexpr unsigned o = kk * 16 * 224;
    if constexpr (p < 3) c.W[set][p] = lds_tr2(c.wb[buf] + t3off + o + p * 32, c.wb[buf] + t3off + o + p * 32 + 4 * 224);
    else c.W[set][p] = lds_tr2(c.wb[buf] + o + 6 * 32, c.wb[buf] + o + 6 * 32 + 4 * 224);
  }
}
template <int I, int M> DI void g1_mfma(G1& c) {
  constexpr int set = I & 1;
  if constexpr (M < 6) {
    constexpr int p = M >> 1, b = M & 1;
    if constexpr (I == 0) { f32x16 z = {}; c.acc[p][b] = MFMA32(c.W[set][p], b ? c.XB[set] : c.XA[set], z); }
    else c.acc[p][b] = MFMA32(c.W[set][p], b ? c.XB[set] : c.XA[set], c.acc[p][b]);
  } else {
    if constexpr (I == 0) { f32x16 z = {}; c.acc6 = MFMA32(c.W[set][3], c.XA[set], z); }
    else c.acc6 = MFMA32(c.W[set][3], c.XA[set], c.acc6);
  }
}
template <int I, int M> DI void g1_gap(G1& c, unsigned t3off) {
  constexpr int st = I >> 2, kk = I & 3;
  g1_mfma<I, M>(c);
  constexpr bool last = (I == 31);
  if constexpr (!last) {
    if constexpr (kk != 3) {
      if constexpr (M < 6) g1_read<I + 1, M>(c, t3off);
    } else {
      if constexpr (M == 1) {
        SB();
        if constexpr (st <= 5) asm volatile("s_waitcnt vmcnt(11) lgkmcnt(0)\n\ts_barrier" ::: "memory");
        else asm volatile("s_waitcnt vmcnt(0) lgkmcnt(0)\n\ts_barrier" ::: "memory");
      }
      if constexpr (M == 2) { g1_read<I + 1, 0>(c, t3off); g1_read<I + 1, 1>(c, t3off); }
      if constexpr (M >= 3) g1_read<I + 1, M - 1>(c, t3off);
    }
  }
  if constexpr (kk == 3) {
    if constexpr (st + 3 < 8) { if constexpr (M == 4) g1_dma<st + 3, 0>(c); if constexpr (M == 6) g1_dma<st + 3, 1>(c); }
  } else if constexpr (st >= 1 && st + 2 < 8) {
    if constexpr (M == 1 || M == 3 || M == 5) g1_dma<st + 2, 2 + 3 * kk + (M >> 1)>(c);
  }
  SB();
}
template <int I, int... Ms> DI void g1_step(G1& c, unsigned t3off, iseq<Ms...>) { (g1_gap<I, Ms>(c, t3off), ...); }
template <int... Is> DI void g1_loop(G1& c, unsigned t3off, iseq<Is...>) { (g1_step<Is>(c, t3off, make_iseq<7>{}), ...); }

DI float gelu_mul(float g, float u) {
  constexpr float c1 = -2.302208198f, c2 = -0.1029432397f;
  float e = __builtin_amdgcn_exp2f(g * __builtin_fmaf(g * g, c2, c1));
  return g * u * __builtin_amdgcn_rcpf(1.f + e);
}
DI void g1_store_tile(const f32x16& a, char* dst, int lane) {
  u32x4 o;
#pragma unroll
  for (int r = 0; r < 4; ++r) o[r] = cvtpk(gelu_mul(a[2 * r], a[8 + 2 * r]), gelu_mul(a[2 * r + 1], a[8 + 2 * r + 1]));
  *(u32x4*)(dst + lane * 16) = o;
}

__global__ __launch_bounds__(256, 1) void k_g1(const u16* __restrict__ x, const u16* __restrict__ wg, const u16* __restrict__ wu, char* __restrict__ ws) {
  __shared__ __attribute__((aligned(16))) char smem[G1_LDS];
  const unsigned lds0 = (unsigned)(size_t)(LAS char*)smem;
  const int lane = threadIdx.x & 63;
  const unsigned w = rfl(threadIdx.x >> 6);
  const int xcd = blockIdx.x & 7, j = blockIdx.x >> 3;
  const int rb = 8 * (xcd & 1) + (j & 7), fc = 4 * (xcd >> 1) + (j >> 3);
  const unsigned mi = w >> 1, ni = w & 1;
  G1 c;
  c.xs = mk_rsrc(x, S_ * D_ * 2);
  c.wsr = mk_rsrc((w >> 1) ? wu : wg, D_ * F_ * 2);
#pragma unroll
  for (int e = 0; e < 2; ++e) c.xv[e] = (lane >> 3) * 1024 + (((lane & 7) ^ ((4 * e + (lane >> 4)) & 7)) << 4);
#pragma unroll
  for (int jj = 0; jj < 7; ++jj) { unsigned ci = 64 * (7 * (w & 1) + jj) + lane; unsigned k = ci / 14, p = ci - k * 14; c.wv[jj] = k * 3584 + p * 16; }
  c.xsoff = (rb * 128 + 32 * w) * 1024;
  c.wsoff = fc * 224;
  c.xm0 = lds0 + G1_XOFF + w * 4096;
  c.wm0 = lds0 + G1_WOFF + (w >> 1) * G1_WIMG + (w & 1) * 7 * 1024;
  {
    const int r = lane & 31, hh = lane >> 5;
    unsigned base = r * 128 + ((hh ^ ((r >> 1) & 7)) << 4);
#pragma unroll
    for (int kk = 0; kk < 4; ++kk) {
      unsigned b = base ^ (kk << 5);
      c.xa[kk] = lds0 + G1_XOFF + (2 * mi + ni) * 4096 + b;
      c.xb[kk] = lds0 + G1_XOFF + (2 * mi + (1 - ni)) * 4096 + b;
    }
    const int img = (lane >> 4) & 1, q = (lane & 15) >> 2, p = lane & 3;
    unsigned wbv = lds0 + G1_WOFF + img * G1_WIMG + (8 * hh + q) * 224 + p * 8;
#pragma unroll
    for (int b = 0; b < 3; ++b) c.wb[b] = wbv + b * G1_WS;
  }
  const unsigned t3off = ni * 96;
  g1_dma_all<0>(c, make_iseq<11>{});
  g1_dma_all<1>(c, make_iseq<11>{});
  g1_dma_all<2>(c, make_iseq<11>{});
  asm volatile("s_waitcnt vmcnt(22)\n\ts_barrier" ::: "memory");
  g1_read<0, 0>(c, t3off); g1_read<0, 1>(c, t3off); g1_read<0, 2>(c, t3off); g1_read<0, 3>(c, t3off); g1_read<0, 4>(c, t3off); g1_read<0, 5>(c, t3off);
  SB();
  g1_loop(c, t3off, make_iseq<32>{});
  char* hb = ws + WS_H;
  const int stA = rb * 4 + 2 * mi + ni, stB = rb * 4 + 2 * mi + (1 - ni);
#pragma unroll
  for (int p = 0; p < 3; ++p) {
    g1_store_tile(c.acc[p][0], hb + ((size_t)stA * 112 + fc * 7 + 3 * ni + p) * 1024, lane);
    g1_store_tile(c.acc[p][1], hb + ((size_t)stB * 112 + fc * 7 + 3 * ni + p) * 1024, lane);
  }
  g1_store_tile(c.acc6, hb + ((size_t)stA * 112 + fc * 7 + 6) * 1024, lane);
}

constexpr unsigned CV_HS = 4160, CV_H = 4 * CV_HS, CV_X = 5120, CV_CH = CV_H + CV_X;
constexpr unsigned CV_RED = 2 * CV_CH, CV_LDS = CV_RED + 4 * 4096;
DI void ds_rd64(u32x2& d, unsigned a, int off) { asm volatile("ds_read_b64 %0, %1 offset:%2" : "=v"(d) : "v"(a), "i"(off) : "memory"); }

__global__ __launch_bounds__(256, 1) void k_conv(const u16* __restrict__ hk, const u16* __restrict__ Dv, char* __restrict__ ws) {
  __shared__ __attribute__((aligned(16))) char smem[CV_LDS];
  const unsigned lds0 = (unsigned)(size_t)(LAS char*)smem;
  const int lane = threadIdx.x & 63;
  const unsigned w = rfl(threadIdx.x >> 6);
  const unsigned ch = w >> 1, hf = w & 1;
  const unsigned d = blockIdx.x * 2 + ch;
  const unsigned cb = lds0 + ch * CV_CH;
  {
    rsrc_t hs = mk_rsrc(hk + (size_t)d * S_, (D_ - d) * S_ * 2);
    rsrc_t xs = mk_rsrc(ws + WS_KVT + (size_t)d * 4096, 4096);
#pragma unroll
    for (int tt = 0; tt < 2; ++tt) {
      unsigned t = 2 * hf + tt;
#pragma unroll
      for (int i = 0; i < 4; ++i) dma16(hs, lane * 16 + (t + 17) * 2, i * 1024, cb + t * CV_HS + i * 1024);
    }
    if (hf == 0) { for (int i = 0; i < 3; ++i) dma16(xs, lane * 16, i * 1024, cb + CV_H + i * 1024); }
    else { for (int i = 3; i < 5; ++i) dma16(xs, lane * 16, i * 1024, cb + CV_H + i * 1024); }
  }
  const int li = lane & 15, gq = lane >> 4;
  u32x4 a0;
  {
    const u16* hrow = hk + (size_t)d * S_;
#pragma unroll
    for (int e = 0; e < 4; ++e) {
      int i0 = li + 8 * gq + 2 * e - 15, i1 = i0 + 1;
      unsigned lo = i0 >= 0 ? hrow[i0] : 0, hi = i1 >= 0 ? hrow[i1] : 0;
      a0[e] = lo | (hi << 16);
    }
  }
  const float Dd = __uint_as_float((unsigned)Dv[d] << 16);
  asm volatile("s_waitcnt vmcnt(0)\n\ts_barrier" ::: "memory");
  const unsigned abase = cb + (li & 3) * CV_HS + (li >> 2) * 8 + gq * 16 + hf * 4 * 64 - 64;
  const unsigned bbase = cb + CV_H + (254 - 2 * li + gq) * 16 - 64 * 56 + hf * 4 * 64;
  f32x4 acc[8];
#pragma unroll
  for (int g = 0; g < 8; ++g) acc[g] = f32x4{0, 0, 0, 0};
#pragma unroll
  for (int rr = 0; rr < 4; ++rr) {
    u32x2 alo[8], ahi[8];
    bf16x8 B[8];
#pragma unroll
    for (int al = 0; al < 8; ++al) { ds_rd64(alo[al], abase, rr * 64 + al * 512); ds_rd64(ahi[al], abase, rr * 64 + al * 512 + 8); }
#pragma unroll
    for (int be = 0; be < 8; ++be) B[be] = lds_b128(bbase + 64 * (56 - 8 * be) + 64 * rr);
    asm volatile("s_waitcnt lgkmcnt(0)" : "+v"(alo[0]), "+v"(alo[1]), "+v"(alo[2]), "+v"(alo[3]), "+v"(alo[4]), "+v"(alo[5]), "+v"(alo[6]), "+v"(alo[7]),
                 "+v"(ahi[0]), "+v"(ahi[1]), "+v"(ahi[2]), "+v"(ahi[3]), "+v"(ahi[4]), "+v"(ahi[5]), "+v"(ahi[6]), "+v"(ahi[7]) :: "memory");
#pragma unroll
    for (int al = 0; al < 8; ++al) {
      u32x4 av = {alo[al][0], alo[al][1], ahi[al][0], ahi[al][1]};
      if (rr == 0 && al == 0) { const unsigned hm0 = 0u - hf; for (int e = 0; e < 4; ++e) av[e] = (av[e] & hm0) | (a0[e] & ~hm0); }
      bf16x8 A = __builtin_bit_cast(bf16x8, av);
#pragma unroll
      for (int be = 0; be + al < 8; ++be) acc[al + be] = MFMA16(A, B[be], acc[al + be]);
    }
  }
  const unsigned red = lds0 + CV_RED;
  const unsigned hm = 0u - hf;
#pragma unroll
  for (int g = 0; g < 4; ++g) *(LAS f32x4*)(red + w * 4096 + g * 1024 + lane * 16) = selv(hm, acc[g], acc[4 + g]);
  __syncthreads();
  char* yrow = ws + WS_Y2T + (size_t)d * 4096;
#pragma unroll
  for (int g = 0; g < 4; ++g) {
    f32x4 o = *(const LAS f32x4*)(red + (w ^ 1) * 4096 + g * 1024 + lane * 16);
    f32x4 y = selv(hm, acc[4 + g], acc[g]);
    y += o;
    const int n0 = 256 * (g + 4 * hf) + 16 * li + 4 * gq;
    u32x2 kvr = *(const LAS u32x2*)(cb + CV_H + (2044 - n0) * 2);
    float r0 = y[0] + bfhi(kvr[1]) * Dd, r1 = y[1] + bflo(kvr[1]) * Dd, r2 = y[2] + bfhi(kvr[0]) * Dd, r3 = y[3] + bflo(kvr[0]) * Dd;
    u32x2 pk = {cvtpk(r0, r1), cvtpk(r2, r3)};
    *(u32x2*)(yrow + n0 * 2) = pk;
  }
}

constexpr unsigned G2_SLOT = 4096, G2_NS = 8, G2_RING = G2_SLOT * G2_NS, G2_Y = 4 * G2_RING, G2_LDS = G2_Y + 8192;
struct G2 {
  rsrc_t hs, wds;
  unsigned hv, wv[2], hso0, hso1, wso, ring;
  unsigned hrd, wrd;
  f32x16 acc[2][2];
  bf16x8 H[2][2], W[2][2];
};
template <int I, int N> DI void g2_dma(G2& c) {
  constexpr unsigned slot = (I % G2_NS) * G2_SLOT;
  if constexpr (N == 0) dma16(c.hs, c.hv, c.hso0 + I * 1024, c.ring + slot);
  else if constexpr (N == 1) dma16(c.hs, c.hv, c.hso1 + I * 1024, c.ring + slot + 1024);
  else dma16(c.wds, c.wv[0], c.wso + I * 16 * 1024 + (N - 2) * 8 * 1024, c.ring + slot + 2048 + (N - 2) * 1024);
}
template <int I, int R> DI void g2_read(G2& c) {
  constexpr unsigned slot = (I % G2_NS) * G2_SLOT;
  constexpr int set = I & 1;
  if constexpr (R < 2) c.H[set][R] = lds_b128(c.hrd + slot + R * 1024);
  else c.W[set][R - 2] = lds_tr2((c.wrd ^ ((R - 2) * 64)) + slot, (c.wrd ^ ((R - 2) * 64)) + slot + 8 * 128);
}
template <int I, int M> DI void g2_gap(G2& c) {
  constexpr int set = I & 1, dt = M >> 1, st = M & 1;
  if constexpr (I == 0) { f32x16 z = {}; c.acc[dt][st] = MFMA32(c.W[set][dt], c.H[set][st], z); }
  else c.acc[dt][st] = MFMA32(c.W[set][dt], c.H[set][st], c.acc[dt][st]);
  if constexpr (I + 1 < 28) {
    if constexpr (M == 0) {
      SB();
      constexpr int rem = 27 - (I + 1);
      constexpr int inflight = rem < 5 ? rem : 5;
      wait_vm<4 * inflight>();
      g2_read<I + 1, 0>(c); g2_read<I + 1, 2>(c);
    }
    if constexpr (M == 1) { g2_read<I + 1, 1>(c); g2_read<I + 1, 3>(c); }
  }
  if constexpr (I + 7 < 28) g2_dma<I + 7, M>(c);
  SB();
}
template <int I, int... Ms> DI void g2_step(G2& c, iseq<Ms...>) { (g2_gap<I, Ms>(c), ...); }
template <int... Is> DI void g2_loop(G2& c, iseq<Is...>) { (g2_step<Is>(c, make_iseq<4>{}), ...); }
template <int I, int... Ns> DI void g2_dma_step(G2& c, iseq<Ns...>) { (g2_dma<I, Ns>(c), ...); }
template <int... Is> DI void g2_pro(G2& c, iseq<Is...>) { (g2_dma_step<Is>(c, make_iseq<4>{}), ...); }

__global__ __launch_bounds__(256, 1) void k_g2(const u16* __restrict__ q, const u16* __restrict__ wd, char* __restrict__ ws, u16* __restrict__ out) {
  __shared__ __attribute__((aligned(16))) char smem[G2_LDS];
  const unsigned lds0 = (unsigned)(size_t)(LAS char*)smem;
  const int lane = threadIdx.x & 63;
  const unsigned w = rfl(threadIdx.x >> 6);
  const int xcd = blockIdx.x & 7, j = blockIdx.x >> 3;
  const int rb = 4 * xcd + (j & 3), cb = j >> 2;
  const int s0 = rb * 64, d0 = cb * 64;
  G2 c;
  c.hs = mk_rsrc(ws + WS_H, 64 * 112 * 1024);
  c.wds = mk_rsrc(wd, F_ * D_ * 2);
  c.hv = lane * 16;
  { int row = lane >> 3; c.wv[0] = row * 1024 + ((((lane & 7) ^ (((row >> 1) & 1) << 2))) << 4); c.wv[1] = 0; }
  c.hso0 = ((2 * rb) * 112 + 28 * w) * 1024;
  c.hso1 = ((2 * rb + 1) * 112 + 28 * w) * 1024;
  c.wso = (28 * w * 16) * 1024 + d0 * 2;
  c.ring = lds0 + w * G2_RING;
  c.hrd = c.ring + lane * 16;
  {
    const int blk = (lane >> 4) & 1, hh = lane >> 5, q = (lane & 15) >> 2, p = lane & 3;
    const int row = 4 * hh + q;
    unsigned chunk = (blk * 2 + (p >> 1)) ^ (((row >> 1) & 1) << 2);
    c.wrd = c.ring + 2048 + row * 128 + chunk * 16 + (p & 1) * 8;
  }
  {
    rsrc_t ys = mk_rsrc(ws + WS_Y2T, D_ * S_ * 2);
#pragma unroll
    for (int i = 0; i < 2; ++i) {
      int rowl = (2 * w + i) * 8 + (lane >> 3);
      unsigned voff = (lane >> 3) * 4096 + (((lane & 7) ^ ((2 * w + i) & 7)) << 4);
      dma16(ys, voff, (d0 + (2 * w + i) * 8) * 4096 + s0 * 2, lds0 + G2_Y + (2 * w + i) * 1024);
      (void)rowl;
    }
  }
  g2_pro(c, make_iseq<7>{});
  wait_vm<24>();
  g2_read<0, 0>(c); g2_read<0, 2>(c); g2_read<0, 1>(c); g2_read<0, 3>(c);
  SB();
  g2_loop(c, make_iseq<28>{});
  asm volatile("s_waitcnt lgkmcnt(0)\n\ts_barrier" ::: "memory");
  {
    const int sl = lane & 31, hh = lane >> 5;
#pragma unroll
    for (int dt = 0; dt < 2; ++dt)
#pragma unroll
      for (int st = 0; st < 2; ++st)
#pragma unroll
        for (int qd = 0; qd < 4; ++qd) {
          const int s = 32 * st + sl, chunk = 8 * dt + 2 * qd + hh;
          f32x4 v = {c.acc[dt][st][4 * qd], c.acc[dt][st][4 * qd + 1], c.acc[dt][st][4 * qd + 2], c.acc[dt][st][4 * qd + 3]};
          *(LAS f32x4*)(lds0 + w * 16384 + s * 256 + ((chunk ^ (s & 15)) << 4)) = v;
        }
  }
  __syncthreads();
  const int t = threadIdx.x;
  const float* gate = (const float*)(ws + WS_G);
#pragma unroll
  for (int ps = 0; ps < 2; ++ps) {
    const int s = 32 * ps + (t >> 3), cc = t & 7;
    f32x4 b0 = {0, 0, 0, 0}, b1 = {0, 0, 0, 0};
#pragma unroll
    for (int ww = 0; ww < 4; ++ww) {
      b0 += *(const LAS f32x4*)(lds0 + ww * 16384 + s * 256 + (((2 * cc) ^ (s & 15)) << 4));
      b1 += *(const LAS f32x4*)(lds0 + ww * 16384 + s * 256 + (((2 * cc + 1) ^ (s & 15)) << 4));
    }
    const float g = gate[s0 + s];
    u32x4 qv = *(const u32x4*)(q + (size_t)(s0 + s) * D_ + d0 + cc * 8);
    float y[8];
#pragma unroll
    for (int e = 0; e < 8; ++e) {
      u16 yv = *(const LAS u16*)(lds0 + G2_Y + (8 * cc + e) * 128 + ((((s >> 3) ^ cc) & 7) << 4) + (s & 7) * 2);
      y[e] = __uint_as_float((unsigned)yv << 16);
    }
    u32x4 o;
#pragma unroll
    for (int e = 0; e < 4; ++e) {
      float bl = e < 2 ? b0[2 * e] : b1[2 * e - 4], bh = e < 2 ? b0[2 * e + 1] : b1[2 * e - 3];
      float al = bflo(qv[e]) * y[2 * e], ah = bfhi(qv[e]) * y[2 * e + 1];
      o[e] = cvtpk(g * al + (1.f - g) * bl, g * ah + (1.f - g) * bh);
    }
    *(u32x4*)(out + (size_t)(s0 + s) * D_ + d0 + cc * 8) = o;
  }
}

extern "C" void kernel_launch(void* const* d_in, const int* in_sizes, int n_in, void* d_out, int out_size, void* d_ws, size_t ws_size, hipStream_t stream) {
  const u16* q = (const u16*)d_in[0]; const u16* k = (const u16*)d_in[1]; const u16* v = (const u16*)d_in[2];
  const u16* shk = (const u16*)d_in[3]; const u16* hk = (const u16*)d_in[4]; const u16* Dv = (const u16*)d_in[5];
  const u16* wg = (const u16*)d_in[7]; const u16* wu = (const u16*)d_in[8]; const u16* wd = (const u16*)d_in[9]; const u16* gw = (const u16*)d_in[10];
  char* ws = (char*)d_ws;
  k_pre<<<256, 256, 0, stream>>>(q, k, v, shk, gw, ws);
  k_g1<<<256, 256, 0, stream>>>(q, wg, wu, ws);
  k_conv<<<256, 256, 0, stream>>>(hk, Dv, ws);
  k_g2<<<256, 256, 0, stream>>>(q, wd, ws, (u16*)d_out);
}
```

```cpp
#include <hip/hip_runtime.h>
#include <hip/hip_bf16.h>
#include <utility>
#pragma clang diagnostic ignored "-Winline-asm"
#pragma clang diagnostic ignored "-Wint-to-pointer-cast"

#define DI __device__ __forceinline__
#define LAS __attribute__((address_space(3)))
#define GAS __attribute__((address_space(1)))
typedef unsigned short u16;
typedef unsigned long long u64;
typedef float f32x4 __attribute__((ext_vector_type(4)));
typedef float f32x2 __attribute__((ext_vector_type(2)));
typedef __bf16 bf16x8 __attribute__((ext_vector_type(8)));
typedef __bf16 bf16x2 __attribute__((ext_vector_type(2)));
typedef short s16x4 __attribute__((ext_vector_type(4)));
typedef short s16x8 __attribute__((ext_vector_type(8)));
typedef unsigned u32x4 __attribute__((ext_vector_type(4)));
typedef unsigned u32x2 __attribute__((ext_vector_type(2)));
typedef __amdgpu_buffer_rsrc_t rsrc_t;
template <int... Is> using iseq = std::integer_sequence<int, Is...>;
template <int N> using make_iseq = std::make_integer_sequence<int, N>;

constexpr int S_ = 2048, D_ = 512, F_ = 1792;
constexpr unsigned WS_E = 0, WS_FS = 256, WS_FH = 4096, WS_FY = 8192, WS_TMO = 12288;
constexpr unsigned WS_G   = 16384;
constexpr unsigned WS_KVT = 1u << 20;
constexpr unsigned WS_Y2T = WS_KVT + (2u << 20);
constexpr unsigned WS_WDT = WS_Y2T + (2u << 20);
constexpr unsigned WS_H   = WS_WDT + (2u << 20);
constexpr unsigned WS_END = WS_H + 128u * 56 * 1024;

constexpr unsigned G1_XS = 16384, G1_WS = 28672, G1_XOFF = 0, G1_WOFF = 3 * G1_XS;
constexpr unsigned ST_K = 106496, ST_V = ST_K + 9216, ST_SH = ST_V + 8192, ST_GX = ST_SH + 1024, ST_GW = ST_GX + 8192, ST_WD = ST_GW + 1024, ST_T = ST_WD + 8192;
constexpr unsigned CV_HS = 4160, CV_H = 4 * CV_HS, CV_X = 5120, CV_CH = CV_H + CV_X, CV_BASE = 106496, CV_RED = 0;
constexpr unsigned LDS_BYTES = ST_T + 8192;
static_assert(CV_BASE + 2 * CV_CH <= LDS_BYTES, "lds");

#define SB() __builtin_amdgcn_sched_barrier(0)
#define STAMP_DECL
#define STAMP(i)
#define STAMP_FLUSH(n)

DI unsigned rfl(unsigned x) { return __builtin_amdgcn_readfirstlane(x); }
DI rsrc_t mk_rsrc(const void* p, unsigned bytes) { return __builtin_amdgcn_make_buffer_rsrc((void*)p, 0, bytes, 0x00020000); }
DI unsigned cvtpk(float lo, float hi) { f32x2 v = {lo, hi}; bf16x2 b = __builtin_convertvector(v, bf16x2); return __builtin_bit_cast(unsigned, b); }
DI float bflo(unsigned x) { return __uint_as_float(x << 16); }
DI float bfhi(unsigned x) { return __uint_as_float(x & 0xffff0000u); }
DI void dma16(rsrc_t r, unsigned voff, unsigned soff, unsigned ldsaddr) {
  asm volatile("s_mov_b32 m0, %0\n\ts_nop 0\n\tbuffer_load_dwordx4 %1, %2, %3 offen lds" :: "s"(ldsaddr), "v"(voff), "s"(r), "s"(soff) : "m0", "memory");
}
#define WAIT_VM(n) asm volatile("s_waitcnt vmcnt(" #n ")" ::: "memory")
#define WAIT_VM_BAR(n) asm volatile("s_waitcnt vmcnt(" #n ") lgkmcnt(0)\n\ts_barrier" ::: "memory")
#define LGKM_BAR() asm volatile("s_waitcnt lgkmcnt(0)\n\ts_barrier" ::: "memory")
DI bf16x8 lds_b128(unsigned a) { return *(const LAS bf16x8*)a; }
DI u32x4 lds_u128(unsigned a) { return *(const LAS u32x4*)a; }
DI bf16x8 lds_tr2(unsigned a0, unsigned a1) {
  s16x4 lo = __builtin_amdgcn_ds_read_tr16_b64_v4i16((LAS s16x4*)a0);
  s16x4 hi = __builtin_amdgcn_ds_read_tr16_b64_v4i16((LAS s16x4*)a1);
  s16x8 v = __builtin_shufflevector(lo, hi, 0, 1, 2, 3, 4, 5, 6, 7);
  return __builtin_bit_cast(bf16x8, v);
}
DI void mfma_vvv(f32x4& c, const bf16x8& a, const bf16x8& b) { asm volatile("v_mfma_f32_16x16x32_bf16 %0, %1, %2, %0" : "+v"(c) : "v"(a), "v"(b)); }
DI void mfma_vvv0(f32x4& c, const bf16x8& a, const bf16x8& b) { asm volatile("v_mfma_f32_16x16x32_bf16 %0, %1, %2, 0" : "=&v"(c) : "v"(a), "v"(b)); }
DI void mfma_vav(f32x4& c, const u32x4& a, const u32x4& b) { asm volatile("v_mfma_f32_16x16x32_bf16 %0, %1, %2, %0" : "+v"(c) : "a"(a), "v"(b)); }
DI void mfma_vav0(f32x4& c, const u32x4& a, const u32x4& b) { asm volatile("v_mfma_f32_16x16x32_bf16 %0, %1, %2, 0" : "=&v"(c) : "a"(a), "v"(b)); }
#define MFMA_DRAIN() do { SB(); asm volatile("s_nop 7\n\ts_nop 3" ::: "memory"); SB(); } while (0)
constexpr int SC1 = 16;
DI u32x4 gld128(rsrc_t r, unsigned voff, unsigned soff) { return __builtin_amdgcn_raw_buffer_load_b128(r, voff, soff, SC1); }
DI unsigned gld32(rsrc_t r, unsigned voff, unsigned soff) { return __builtin_amdgcn_raw_buffer_load_b32(r, voff, soff, SC1); }
DI void gst128(u32x4 v, rsrc_t r, unsigned voff, unsigned soff) { __builtin_amdgcn_raw_buffer_store_b128(v, r, voff, soff, SC1); }
DI void gst64(u32x2 v, rsrc_t r, unsigned voff, unsigned soff) { __builtin_amdgcn_raw_buffer_store_b64(v, r, voff, soff, SC1); }
DI void gst32(unsigned v, rsrc_t r, unsigned voff, unsigned soff) { __builtin_amdgcn_raw_buffer_store_b32(v, r, voff, soff, SC1); }
#define RLX_AGENT __ATOMIC_RELAXED, __HIP_MEMORY_SCOPE_AGENT
DI u64 flag_ld(const char* a) { return __hip_atomic_load((const GAS u64*)a, RLX_AGENT); }
DI void flag_st(char* a, u64 v) { __hip_atomic_store((GAS u64*)a, v, RLX_AGENT); }
template <int N> DI void wait_flags(const char* base, u64 epoch, char* tmo, int lane) {
  for (unsigned spins = 0;; ++spins) {
    bool ok = true;
#pragma unroll
    for (int i = 0; i < (N + 63) / 64; ++i) { const int idx = i * 64 + lane; const u64 v = flag_ld(base + (idx < N ? idx : 0) * 8); ok &= (v == epoch); }
    if (__all(ok)) break;
    if (spins > 30000u) { if (lane == 0) flag_st(tmo, 1); break; }
    __builtin_amdgcn_s_sleep(1);
  }
  __builtin_amdgcn_fence(__ATOMIC_ACQUIRE, "wavefront");
}
DI f32x4 selv(unsigned m, f32x4 a, f32x4 b) {
  f32x4 r;
#pragma unroll
  for (int e = 0; e < 4; ++e) r[e] = __uint_as_float((__float_as_uint(a[e]) & m) | (__float_as_uint(b[e]) & ~m));
  return r;
}
DI float gelu_mul(float g, float u) {
  constexpr float c1 = -2.302208198f, c2 = -0.1029432397f;
  float e = __builtin_amdgcn_exp2f(g * __builtin_fmaf(g * g, c2, c1));
  return g * u * __builtin_amdgcn_rcpf(1.f + e);
}

struct Params { const u16 *q, *k, *v, *shk, *hk, *Dv, *wg, *wu, *wd, *gw; u16* out; char* ws; };

struct G1 {
  rsrc_t xs, wsr;
  unsigned xv[2], wv[7];
  unsigned xsoff, wsoff, xm0, wm0;
  unsigned xa[2], xb[2], wt, w6;
  f32x4 acc[3][2][4], acc6[2][2];
  bf16x8 X[2][4], W[2][8];
};
template <int N> DI void g1_dma(G1& c, unsigned st, unsigned xo, unsigned wo) {
  if constexpr (N < 4) dma16(c.xs, c.xv[N & 1], c.xsoff + N * 8192 + st * 128, c.xm0 + xo + N * 1024);
  else dma16(c.wsr, c.wv[N - 4], c.wsoff + st * (64 * 3584), c.wm0 + wo + (N - 4) * 1024);
}
template <int... Ns> DI void g1_dma_all(G1& c, unsigned st, unsigned xo, unsigned wo, iseq<Ns...>) { (g1_dma<Ns>(c, st, xo, wo), ...); }
template <int KK, int R> DI void g1_read(G1& c) {
  constexpr int xi[12] = {0, -1, 1, 2, 3, -1, -1, -1, -1, -1, -1, -1};
  constexpr int wi[12] = {-1, 0, -1, -1, -1, 1, 2, 3, 4, 5, 6, 7};
  if constexpr (xi[R] >= 0) { constexpr int i = xi[R]; c.X[KK][i] = lds_b128((i < 2 ? c.xa[KK] : c.xb[KK]) + (i & 1) * 2048); }
  else {
    constexpr int i = wi[R], pr = i >> 1, gu = i & 1;
    constexpr unsigned o = KK * 32 * 224 + gu * 14336;
    if constexpr (pr < 3) c.W[KK][i] = lds_tr2(c.wt + o + pr * 32, c.wt + o + pr * 32 + 8 * 224);
    else c.W[KK][i] = lds_tr2(c.w6 + o, c.w6 + o + 8 * 224);
  }
}
template <int KK, int M, bool FIRST> DI void g1_mfma(G1& c) {
  if constexpr (M < 24) {
    constexpr int pr = M >> 3, gu = (M >> 2) & 1, st = M & 3;
    mfma_vvv(c.acc[pr][gu][st], c.W[KK][2 * pr + gu], c.X[KK][st]);
  } else {
    constexpr int gu = (M - 24) >> 1, st = M & 1;
    mfma_vvv(c.acc6[gu][st], c.W[KK][6 + gu], c.X[KK][st]);
  }
}
struct G1Stage { unsigned s, xo2, wo2, xo3, wo3, dx, dw; };
struct CvDma { rsrc_t hs; unsigned voff, m0; };
template <int MODE, int M> DI void g1_gap0(G1& c, const G1Stage& g, const CvDma& cv) {
  g1_mfma<0, M, false>(c);
  if constexpr (M >= 2 && M < 26 && (M & 1) == 0) g1_read<1, (M - 2) / 2>(c);
  if constexpr (MODE <= 5) { if constexpr (M % 4 == 3 && M / 4 < 6) g1_dma<5 + M / 4>(c, g.s + 2, g.xo2, g.wo2); }
  if constexpr (MODE == 6) { if constexpr (M % 4 == 3 && M / 4 < 4) dma16(cv.hs, cv.voff + 2, (M / 4) * 1024, cv.m0 + CV_HS + (M / 4) * 1024); }
  SB();
}
template <int MODE, int M> DI void g1_gap1(G1& c, const G1Stage& g, const CvDma& cv) {
  g1_mfma<1, M, false>(c);
  if constexpr (MODE != 7) {
    if constexpr (M == 2) {
      SB();
      if constexpr (MODE == 6) WAIT_VM_BAR(8); else WAIT_VM_BAR(11);
      c.xa[0] += g.dx; c.xa[1] += g.dx; c.xb[0] += g.dx; c.xb[1] += g.dx; c.wt += g.dw; c.w6 += g.dw;
    }
    if constexpr (M >= 3 && M < 27 && (M & 1) == 1) g1_read<0, (M - 3) / 2>(c);
    if constexpr (MODE == 0) { if constexpr (M >= 4 && M % 4 == 0 && M / 4 - 1 < 5) g1_dma<M / 4 - 1>(c, g.s + 3, g.xo3, g.wo3); }
    if constexpr (MODE == 5) { if constexpr (M >= 4 && M % 4 == 0 && M / 4 - 1 < 4) dma16(cv.hs, cv.voff, (M / 4 - 1) * 1024, cv.m0 + (M / 4 - 1) * 1024); }
  }
  SB();
}
template <int MODE, int... Ms> DI void g1_stage(G1& c, const G1Stage& g, const CvDma& cv, iseq<Ms...>) { (g1_gap0<MODE, Ms>(c, g, cv), ...); (g1_gap1<MODE, Ms>(c, g, cv), ...); }
template <int... Rs> DI void g1_read0(G1& c, iseq<Rs...>) { (g1_read<0, Rs>(c), ...); }

__global__ __launch_bounds__(256, 1) void k_fused(Params p) {
  __shared__ __attribute__((aligned(16))) char smem[LDS_BYTES];
  const unsigned lds0 = (unsigned)(size_t)(LAS char*)smem;
  const int lane = threadIdx.x & 63;
  const unsigned w = rfl(threadIdx.x >> 6);
  const unsigned b = blockIdx.x, xcd = b & 7, jb = b >> 3;
  char* const ws = p.ws;
  STAMP_DECL; STAMP(0);
  const rsrc_t wsr = mk_rsrc(ws, WS_END);
  const u64 epoch = *(const u64*)(ws + WS_E) * 6364136223846793005ull + 1442695040888963407ull;

  const unsigned sb = b >> 3, db = b & 7, s0p = sb * 64, d0p = db * 64;
  const unsigned kg_w = b >> 2, cq_w = b & 3;
  {
    const unsigned r8 = lane >> 3, c8 = lane & 7;
    if (w == 0) {
      rsrc_t r = mk_rsrc(p.k, S_ * D_ * 2);
#pragma unroll
      for (int i = 0; i < 9; ++i) { int row = (int)s0p - 3 + 8 * i + (int)r8; unsigned vo = row >= 0 ? (unsigned)row * 1024 + d0p * 2 + c8 * 16 : 0x80000000u; dma16(r, vo, 0, lds0 + ST_K + i * 1024); }
    } else if (w == 1) {
      rsrc_t r = mk_rsrc(p.v, S_ * D_ * 2);
#pragma unroll
      for (int i = 0; i < 8; ++i) dma16(r, r8 * 1024 + c8 * 16, (s0p + 8 * i) * 1024 + d0p * 2, lds0 + ST_V + i * 1024);
      dma16(mk_rsrc(p.shk, D_ * 8), lane * 16, d0p * 8, lds0 + ST_SH);
    } else if (w == 2) {
      rsrc_t r = mk_rsrc(p.q, S_ * D_ * 2);
#pragma unroll
      for (int i = 0; i < 8; ++i) dma16(r, lane * 16, (b * 8 + i) * 1024, lds0 + ST_GX + i * 1024);
      dma16(mk_rsrc(p.gw, D_ * 2), lane * 16, 0, lds0 + ST_GW);
    } else {
      rsrc_t r = mk_rsrc(p.wd, F_ * D_ * 2);
#pragma unroll
      for (int i = 0; i < 8; ++i) dma16(r, (lane >> 4) * 1024 + (lane & 15) * 16, (kg_w * 32 + 4 * i) * 1024 + cq_w * 256, lds0 + ST_WD + i * 1024);
      dma16(r, lane * 16, 0x80000000u >> 1, lds0 + ST_T);
    }
  }
  const unsigned rb = 8 * (xcd & 1) + (jb & 7), fc = 4 * (xcd >> 1) + (jb >> 3);
  const unsigned mi = w >> 1, ni = w & 1;
  G1 c;
  c.xs = mk_rsrc(p.q, S_ * D_ * 2);
  c.wsr = mk_rsrc((w >> 1) ? p.wu : p.wg, D_ * F_ * 2);
#pragma unroll
  for (int e = 0; e < 2; ++e) c.xv[e] = (lane >> 3) * 1024 + (((lane & 7) ^ ((4 * e + (lane >> 4)) & 7)) << 4);
#pragma unroll
  for (int jj = 0; jj < 7; ++jj) {
    unsigned ci = 64 * (7 * (w & 1) + jj) + lane, rho = ci / 14, pc = ci - rho * 14;
    unsigned k = (rho & ~12u) | ((rho & 4) << 1) | ((rho & 8) >> 1);
    c.wv[jj] = k * 3584 + pc * 16;
  }
  c.xsoff = (rb * 128 + 32 * w) * 1024;
  c.wsoff = fc * 224;
  c.xm0 = lds0 + G1_XOFF + w * 4096;
  c.wm0 = lds0 + G1_WOFF + (w >> 1) * 14336 + (w & 1) * 7 * 1024;
  {
    const int li = lane & 15, gq = lane >> 4;
#pragma unroll
    for (int kk = 0; kk < 2; ++kk) {
      unsigned bb = li * 128 + (((4 * kk + gq) ^ ((li >> 1) & 7)) << 4);
      c.xa[kk] = lds0 + G1_XOFF + (4 * mi + 2 * ni) * 2048 + bb;
      c.xb[kk] = lds0 + G1_XOFF + (4 * mi + 2 * (1 - ni)) * 2048 + bb;
    }
    const int q = li >> 2, pp = lane & 3;
    unsigned wb = lds0 + G1_WOFF + (16 * (gq >> 1) + 4 * (gq & 1) + q) * 224 + pp * 8;
    c.wt = wb + ni * 96; c.w6 = wb + 6 * 32;
  }
  g1_dma_all(c, 0, 0, 0, make_iseq<11>{});
  g1_dma_all(c, 1, G1_XS, G1_WS, make_iseq<11>{});
  STAMP(1);
  WAIT_VM_BAR(22);
  STAMP(2);

  {
    const int t = threadIdx.x, cc = t & 7, rg = t >> 3;
    u32x4 kr[5], vr[2], w4[4];
#pragma unroll
    for (int i = 0; i < 5; ++i) kr[i] = lds_u128(lds0 + ST_K + (2 * rg + i) * 128 + cc * 16);
#pragma unroll
    for (int i = 0; i < 2; ++i) vr[i] = lds_u128(lds0 + ST_V + (2 * rg + i) * 128 + cc * 16);
#pragma unroll
    for (int i = 0; i < 4; ++i) w4[i] = lds_u128(lds0 + ST_SH + cc * 64 + i * 16);
#pragma unroll
    for (int j = 0; j < 8; ++j) {
      unsigned wa = w4[j >> 1][(j & 1) * 2], wb = w4[j >> 1][(j & 1) * 2 + 1];
      float w0 = bflo(wa), w1 = bfhi(wa), w2 = bflo(wb), w3 = bfhi(wb);
      float kk[5];
#pragma unroll
      for (int i = 0; i < 5; ++i) kk[i] = (j & 1) ? bfhi(kr[i][j >> 1]) : bflo(kr[i][j >> 1]);
      float a0 = kk[0] * w3 + kk[1] * w2 + kk[2] * w1 + kk[3] * w0;
      float a1 = kk[1] * w3 + kk[2] * w2 + kk[3] * w1 + kk[4] * w0;
      float v0 = (j & 1) ? bfhi(vr[0][j >> 1]) : bflo(vr[0][j >> 1]);
      float v1 = (j & 1) ? bfhi(vr[1][j >> 1]) : bflo(vr[1][j >> 1]);
      const int wd = 31 - rg, dl = 8 * cc + j;
      *(LAS unsigned*)(lds0 + ST_T + (dl * 32 + ((((wd >> 2) ^ cc) & 7) << 2) + (wd & 3)) * 4) = cvtpk(a1 * v1, a0 * v0);
    }
    u32x4 g4 = lds_u128(lds0 + ST_GW + lane * 16);
    float gsum[2];
#pragma unroll
    for (int r = 0; r < 2; ++r) {
      u32x4 x4 = lds_u128(lds0 + ST_GX + (2 * w + r) * 1024 + lane * 16);
      float a = 0.f;
#pragma unroll
      for (int j = 0; j < 4; ++j) a += bflo(x4[j]) * bflo(g4[j]) + bfhi(x4[j]) * bfhi(g4[j]);
#pragma unroll
      for (int m = 32; m >= 1; m >>= 1) a += __shfl_xor(a, m);
      gsum[r] = __builtin_amdgcn_rcpf(1.f + __builtin_amdgcn_exp2f(-1.4426950409f * a));
    }
    gst32(__float_as_uint(lane & 1 ? gsum[1] : gsum[0]), wsr, lane < 2 ? WS_G + (b * 8 + 2 * w + lane) * 4 : 0x80000000u, 0);
    const int li = lane & 15, gq = lane >> 4;
#pragma unroll
    for (int i = 0; i < 2; ++i) {
      const unsigned dt8 = 2 * w + i, a = lds0 + ST_WD + (8 * gq + (li >> 2)) * 256 + dt8 * 32 + (lane & 3) * 8;
      bf16x8 f = lds_tr2(a, a + 4 * 256);
      const unsigned D16 = 8 * cq_w + dt8;
      gst128(__builtin_bit_cast(u32x4, f), wsr, kg_w < 56 ? WS_WDT + (((D16 >> 2) * 56 + kg_w) * 4 + (D16 & 3)) * 1024 + lane * 16 : 0x80000000u, 0);
    }
    LGKM_BAR();
#pragma unroll
    for (int i = 0; i < 2; ++i) {
      const int dl = (w * 2 + i) * 8 + (lane >> 3), ch = lane & 7;
      u32x4 val = lds_u128(lds0 + ST_T + dl * 128 + ((ch ^ (dl >> 3)) & 7) * 16);
      gst128(val, wsr, WS_KVT + (d0p + dl) * 4096 + (1984 - s0p) * 2 + ch * 16, 0);
    }
  }
  STAMP(3);
  WAIT_VM_BAR(16);
  g1_dma<0>(c, 2, 2 * G1_XS, 2 * G1_WS); g1_dma<1>(c, 2, 2 * G1_XS, 2 * G1_WS); g1_dma<2>(c, 2, 2 * G1_XS, 2 * G1_WS); g1_dma<3>(c, 2, 2 * G1_XS, 2 * G1_WS); g1_dma<4>(c, 2, 2 * G1_XS, 2 * G1_WS);
  g1_read0(c, make_iseq<12>{});
#pragma unroll
  for (int i = 0; i < 3; ++i)
#pragma unroll
    for (int j = 0; j < 2; ++j)
#pragma unroll
      for (int k = 0; k < 4; ++k) c.acc[i][j][k] = f32x4{0, 0, 0, 0};
#pragma unroll
  for (int j = 0; j < 2; ++j) { c.acc6[j][0] = f32x4{0, 0, 0, 0}; c.acc6[j][1] = f32x4{0, 0, 0, 0}; }
  SB();
  asm volatile("s_nop 1" ::: "memory");
  SB();
  STAMP(4);
  const unsigned cch = w >> 1, hf = w & 1, dch = b * 2 + cch, cvb = lds0 + CV_BASE + cch * CV_CH;
  CvDma cv;
  cv.hs = mk_rsrc(p.hk + (size_t)dch * S_, (D_ - dch) * S_ * 2);
  cv.voff = lane * 16 + (2 * hf + 17) * 2; cv.m0 = cvb + 2 * hf * CV_HS;
  {
    G1Stage g;
    unsigned slot = 0;
#pragma unroll 1
    for (unsigned s = 0; s < 5; ++s) {
      const unsigned n1 = slot == 2 ? 0 : slot + 1, n2 = n1 == 2 ? 0 : n1 + 1;
      g.s = s; g.xo2 = n2 * G1_XS; g.wo2 = n2 * G1_WS; g.xo3 = slot * G1_XS; g.wo3 = slot * G1_WS;
      g.dx = (n1 - slot) * G1_XS; g.dw = (n1 - slot) * G1_WS;
      g1_stage<0>(c, g, cv, make_iseq<28>{});
      if (s == 0 && threadIdx.x == 0) flag_st(ws + WS_FS + b * 8, epoch);
      slot = n1;
    }
    g.s = 5; g.xo2 = 1 * G1_XS; g.wo2 = 1 * G1_WS; g.xo3 = 0; g.wo3 = 0; g.dx = 0u - 2 * G1_XS; g.dw = 0u - 2 * G1_WS;
    g1_stage<5>(c, g, cv, make_iseq<28>{});
    g.s = 6; g.dx = G1_XS; g.dw = G1_WS;
    g1_stage<6>(c, g, cv, make_iseq<28>{});
    g1_stage<7>(c, g, cv, make_iseq<28>{});
  }
  MFMA_DRAIN();
  STAMP(5);
  {
    const int li = lane & 15, gq = lane >> 4;
    const unsigned lo = ((gq >> 1) * 16 + li) * 16 + (gq & 1) * 8;
    const unsigned stA = rb * 8 + 4 * mi + 2 * ni, stB = rb * 8 + 4 * mi + 2 * (1 - ni);
    auto put = [&](const f32x4& g, const f32x4& u, unsigned stg, unsigned ft) {
      u32x2 o = {cvtpk(gelu_mul(g[0], u[0]), gelu_mul(g[1], u[1])), cvtpk(gelu_mul(g[2], u[2]), gelu_mul(g[3], u[3]))};
      gst64(o, wsr, lo, WS_H + (stg * 56 + (ft >> 1)) * 1024 + (ft & 1) * 512);
    };
#pragma unroll
    for (int pr = 0; pr < 3; ++pr)
#pragma unroll
      for (int st = 0; st < 4; ++st) put(c.acc[pr][0][st], c.acc[pr][1][st], (st < 2 ? stA : stB) + (st & 1), fc * 7 + 3 * ni + pr);
#pragma unroll
    for (int st = 0; st < 2; ++st) put(c.acc6[0][st], c.acc6[1][st], stA + st, fc * 7 + 6);
  }
  STAMP(6);
  if (w == 0) wait_flags<256>(ws + WS_FS, epoch, ws + WS_TMO, lane);
  WAIT_VM_BAR(0);
  if (threadIdx.x == 0) { flag_st(ws + WS_FH + rb * 128 + fc * 8, epoch); if (b == 0) flag_st(ws + WS_E, epoch); }

  const unsigned rb2 = 4 * xcd + (jb & 3), cb = jb >> 2;
  u32x4 Wd[14][4];
  {
    const int li = lane & 15, gq = lane >> 4;
    rsrc_t xr = mk_rsrc(ws + WS_KVT + dch * 4096, 4096);
    u32x4 xv[3];
#pragma unroll
    for (int i = 0; i < 3; ++i) xv[i] = gld128(xr, (hf * 128 + i * 64 + lane) * 16, 0);
    u32x4 a0;
    {
      rsrc_t hr = mk_rsrc(p.hk + (size_t)dch * S_, 128);
#pragma unroll
      for (int e = 0; e < 4; ++e) {
        const unsigned o = (unsigned)(li + 8 * gq + 2 * e - 15) * 2;
        unsigned lo = (u16)__builtin_amdgcn_raw_buffer_load_b16(hr, o, 0, 0), hi = (u16)__builtin_amdgcn_raw_buffer_load_b16(hr, o + 2, 0, 0);
        a0[e] = lo | (hi << 16);
      }
    }
    const float Dd = __uint_as_float((unsigned)p.Dv[dch] << 16);
    SB();
#pragma unroll
    for (int i = 0; i < 14; ++i)
#pragma unroll
      for (int dt = 0; dt < 4; ++dt) Wd[i][dt] = gld128(wsr, lane * 16 + dt * 1024, WS_WDT + (cb * 56 + 14 * w + i) * 4096);
    SB();
#pragma unroll
    for (int i = 0; i < 3; ++i) *(LAS u32x4*)(cvb + CV_H + (hf * 128 + i * 64 + lane) * 16) = xv[i];
    LGKM_BAR();
    STAMP(7);
    const unsigned abase = cvb + (li & 3) * CV_HS + (li >> 2) * 8 + gq * 16 + hf * 4 * 64 - 64;
    const unsigned bbase = cvb + CV_H + (254 - 2 * li + gq) * 16 - 64 * 56 + hf * 4 * 64;
    f32x4 acc[8];
    u32x2 alo[2][8], ahi[2][8];
    bf16x8 B[2][8];
    auto rdA = [&](int set, int rr, int al) {
      alo[set][al] = *(const volatile LAS u32x2*)(abase + rr * 64 + al * 512);
      ahi[set][al] = *(const volatile LAS u32x2*)(abase + rr * 64 + al * 512 + 8);
    };
    auto rdB = [&](int set, int rr, int be) { B[set][be] = lds_b128(bbase + 64 * (56 - 8 * be) + 64 * rr); };
#pragma unroll
    for (int i = 0; i < 8; ++i) { rdA(0, 0, i); rdB(0, 0, i); }
    SB();
#pragma unroll
    for (int rr = 0; rr < 4; ++rr) {
      const int set = rr & 1;
      int m = 0;
#pragma unroll
      for (int al = 0; al < 8; ++al) {
        u32x4 av = {alo[set][al][0], alo[set][al][1], ahi[set][al][0], ahi[set][al][1]};
        if (rr == 0 && al == 0) { const unsigned hm0 = 0u - hf; for (int e = 0; e < 4; ++e) av[e] = (av[e] & hm0) | (a0[e] & ~hm0); }
        bf16x8 A = __builtin_bit_cast(bf16x8, av);
        if (rr == 0 && al == 0) asm volatile("s_nop 1" : "+v"(A));
#pragma unroll
        for (int be = 0; be + al < 8; ++be) {
          if (rr == 0 && al == 0) mfma_vvv0(acc[be], A, B[set][be]); else mfma_vvv(acc[al + be], A, B[set][be]);
          if (rr < 3 && m >= 2 && m < 34 && (m & 1) == 0) { const int i = (m - 2) >> 2; if (((m - 2) >> 1) & 1) rdB(set ^ 1, rr + 1, i); else rdA(set ^ 1, rr + 1, i); }
          ++m;
          SB();
        }
      }
    }
    MFMA_DRAIN();
    STAMP(8);
    const unsigned red = lds0 + CV_RED, hm = 0u - hf;
#pragma unroll
    for (int g = 0; g < 4; ++g) *(LAS f32x4*)(red + w * 4096 + g * 1024 + lane * 16) = selv(hm, acc[g], acc[4 + g]);
    LGKM_BAR();
#pragma unroll
    for (int g = 0; g < 4; ++g) {
      f32x4 o = *(const LAS f32x4*)(red + (w ^ 1) * 4096 + g * 1024 + lane * 16);
      f32x4 y = selv(hm, acc[4 + g], acc[g]);
      y += o;
      const int n0 = 256 * (g + 4 * hf) + 16 * li + 4 * gq;
      u32x2 kvr = *(const LAS u32x2*)(cvb + CV_H + (2044 - n0) * 2);
      float r0 = y[0] + bfhi(kvr[1]) * Dd, r1 = y[1] + bflo(kvr[1]) * Dd, r2 = y[2] + bfhi(kvr[0]) * Dd, r3 = y[3] + bflo(kvr[0]) * Dd;
      gst64(u32x2{cvtpk(r0, r1), cvtpk(r2, r3)}, wsr, n0 * 2, WS_Y2T + dch * 4096);
    }
  }
  if (w == 0) wait_flags<16>(ws + WS_FH + (rb2 >> 1) * 128, epoch, ws + WS_TMO, lane);
  WAIT_VM_BAR(0);
  if (threadIdx.x == 0) flag_st(ws + WS_FY + (b >> 5) * 256 + (b & 31) * 8, epoch);
  STAMP(9);

  {
    constexpr int PD = 3;
    f32x4 acc[4][4];
    u32x4 H[PD + 1][4];
    const unsigned hbase = WS_H + ((4 * rb2) * 56 + 14 * w) * 1024;
    auto ldH = [&](int i) {
#pragma unroll
      for (int st = 0; st < 4; ++st) H[i % (PD + 1)][st] = gld128(wsr, lane * 16, hbase + (st * 56 + i) * 1024);
    };
#pragma unroll
    for (int i = 0; i < PD; ++i) ldH(i);
    SB();
#pragma unroll
    for (int i = 0; i < 14; ++i) {
      if (i + PD < 14) ldH(i + PD);
#pragma unroll
      for (int dt = 0; dt < 4; ++dt)
#pragma unroll
        for (int st = 0; st < 4; ++st) {
          if (i == 0) mfma_vav0(acc[dt][st], Wd[i][dt], H[i % (PD + 1)][st]); else mfma_vav(acc[dt][st], Wd[i][dt], H[i % (PD + 1)][st]);
        }
      SB();
    }
    MFMA_DRAIN();
    STAMP(10);
    const int li = lane & 15, gq = lane >> 4;
#pragma unroll
    for (int dt = 0; dt < 4; ++dt)
#pragma unroll
      for (int st = 0; st < 4; ++st) {
        const int s = 16 * st + li, chunk = 4 * dt + gq;
        *(LAS f32x4*)(lds0 + w * 16384 + s * 256 + ((chunk ^ li) << 4)) = acc[dt][st];
      }
  }
  if (w == 0) wait_flags<32>(ws + WS_FY + cb * 256, epoch, ws + WS_TMO, lane);
  LGKM_BAR();
  {
    const int t = threadIdx.x, cc = t & 7, sp = t >> 3;
    const unsigned s0 = rb2 * 64, d0 = cb * 64;
    unsigned yv[8];
#pragma unroll
    for (int e = 0; e < 8; ++e) yv[e] = gld32(wsr, (8 * cc + e) * 4096 + sp * 4, WS_Y2T + d0 * 4096 + s0 * 2);
    u32x2 gt = __builtin_amdgcn_raw_buffer_load_b64(wsr, sp * 8, WS_G + s0 * 4, SC1);
    u32x4 qv[2];
#pragma unroll
    for (int r = 0; r < 2; ++r) qv[r] = *(const u32x4*)(p.q + (size_t)(s0 + 2 * sp + r) * D_ + d0 + cc * 8);
#pragma unroll
    for (int r = 0; r < 2; ++r) {
      const int s = 2 * sp + r;
      f32x4 b0 = {0, 0, 0, 0}, b1 = {0, 0, 0, 0};
#pragma unroll
      for (int ww = 0; ww < 4; ++ww) {
        b0 += *(const LAS f32x4*)(lds0 + ww * 16384 + s * 256 + (((2 * cc) ^ (s & 15)) << 4));
        b1 += *(const LAS f32x4*)(lds0 + ww * 16384 + s * 256 + (((2 * cc + 1) ^ (s & 15)) << 4));
      }
      const float g = __uint_as_float(gt[r]);
      u32x4 o;
#pragma unroll
      for (int e = 0; e < 4; ++e) {
        float bl = e < 2 ? b0[2 * e] : b1[2 * e - 4], bh = e < 2 ? b0[2 * e + 1] : b1[2 * e - 3];
        float yl = r ? bfhi(yv[2 * e]) : bflo(yv[2 * e]), yh = r ? bfhi(yv[2 * e + 1]) : bflo(yv[2 * e + 1]);
        float al = bflo(qv[r][e]) * yl, ah = bfhi(qv[r][e]) * yh;
        o[e] = cvtpk(g * al + (1.f - g) * bl, g * ah + (1.f - g) * bh);
      }
      *(u32x4*)(p.out + (size_t)(s0 + s) * D_ + d0 + cc * 8) = o;
    }
  }
  STAMP(11);
  STAMP_FLUSH(12);
}

extern "C" void kernel_launch(void* const* d_in, const int* in_sizes, int n_in, void* d_out, int out_size, void* d_ws, size_t ws_size, hipStream_t stream) {
  Params p;
  p.q = (const u16*)d_in[0]; p.k = (const u16*)d_in[1]; p.v = (const u16*)d_in[2];
  p.shk = (const u16*)d_in[3]; p.hk = (const u16*)d_in[4]; p.Dv = (const u16*)d_in[5];
  p.wg = (const u16*)d_in[7]; p.wu = (const u16*)d_in[8]; p.wd = (const u16*)d_in[9]; p.gw = (const u16*)d_in[10];
  p.out = (u16*)d_out; p.ws = (char*)d_ws;
  k_fused<<<256, 256, 0, stream>>>(p);
}
```

```cpp
#include <hip/hip_runtime.h>
#include <hip/hip_bf16.h>
#include <utility>
#pragma clang diagnostic ignored "-Winline-asm"
#pragma clang diagnostic ignored "-Wint-to-pointer-cast"

#define DI __device__ __forceinline__
#define LAS __attribute__((address_space(3)))
#define GAS __attribute__((address_space(1)))
typedef unsigned short u16;
typedef unsigned long long u64;
typedef float f32x4 __attribute__((ext_vector_type(4)));
typedef float f32x2 __attribute__((ext_vector_type(2)));
typedef __bf16 bf16x8 __attribute__((ext_vector_type(8)));
typedef __bf16 bf16x2 __attribute__((ext_vector_type(2)));
typedef short s16x4 __attribute__((ext_vector_type(4)));
typedef short s16x8 __attribute__((ext_vector_type(8)));
typedef unsigned u32x4 __attribute__((ext_vector_type(4)));
typedef unsigned u32x2 __attribute__((ext_vector_type(2)));
typedef __amdgpu_buffer_rsrc_t rsrc_t;
template <int... Is> using iseq = std::integer_sequence<int, Is...>;
template <int N> using make_iseq = std::make_integer_sequence<int, N>;

constexpr int S_ = 2048, D_ = 512, F_ = 1792;
constexpr unsigned WS_E = 0, WS_FS = 256, WS_FH = 4096, WS_FY = 8192, WS_TMO = 12288;
constexpr unsigned WS_G   = 16384;
constexpr unsigned WS_KVT = 1u << 20;
constexpr unsigned WS_Y2T = WS_KVT + (2u << 20);
constexpr unsigned WS_WDT = WS_Y2T + (2u << 20);
constexpr unsigned WS_H   = WS_WDT + (2u << 20);
constexpr unsigned WS_END = WS_H + 128u * 56 * 1024;

constexpr unsigned G1_XS = 16384, G1_WS = 28672, G1_XOFF = 0, G1_WOFF = 3 * G1_XS;
constexpr unsigned ST_K = 106496, ST_V = ST_K + 9216, ST_SH = ST_V + 8192, ST_GX = ST_SH + 1024, ST_GW = ST_GX + 8192, ST_WD = ST_GW + 1024, ST_T = ST_WD + 8192;
constexpr unsigned CV_HS = 4160, CV_H = 4 * CV_HS, CV_X = 5120, CV_CH = CV_H + CV_X, CV_BASE = 106496, CV_RED = 0;
constexpr unsigned CV_Z = CV_BASE + 2 * CV_CH;
constexpr unsigned LDS_BYTES = CV_Z + 2 * 1088;
static_assert(ST_T + 8192 <= LDS_BYTES && LDS_BYTES <= 163840, "lds");

#define SB() __builtin_amdgcn_sched_barrier(0)
#define STAMP_DECL
#define STAMP(i)
#define DSTAMP(i)
#define STAMP_FLUSH(n)

DI unsigned rfl(unsigned x) { return __builtin_amdgcn_readfirstlane(x); }
DI rsrc_t mk_rsrc(const void* p, unsigned bytes) { return __builtin_amdgcn_make_buffer_rsrc((void*)p, 0, bytes, 0x00020000); }
DI unsigned cvtpk(float lo, float hi) { f32x2 v = {lo, hi}; bf16x2 b = __builtin_convertvector(v, bf16x2); return __builtin_bit_cast(unsigned, b); }
DI float bflo(unsigned x) { return __uint_as_float(x << 16); }
DI float bfhi(unsigned x) { return __uint_as_float(x & 0xffff0000u); }
DI void dma16(rsrc_t r, unsigned voff, unsigned soff, unsigned ldsaddr) {
  asm volatile("s_mov_b32 m0, %0\n\ts_nop 0\n\tbuffer_load_dwordx4 %1, %2, %3 offen lds" :: "s"(ldsaddr), "v"(voff), "s"(r), "s"(soff) : "m0", "memory");
}
#define WAIT_VM(n) asm volatile("s_waitcnt vmcnt(" #n ")" ::: "memory")
#define WAIT_VM_BAR(n) asm volatile("s_waitcnt vmcnt(" #n ") lgkmcnt(0)\n\ts_barrier" ::: "memory")
#define LGKM_BAR() asm volatile("s_waitcnt lgkmcnt(0)\n\ts_barrier" ::: "memory")
DI bf16x8 lds_b128(unsigned a) { return *(const LAS bf16x8*)a; }
DI u32x4 lds_u128(unsigned a) { return *(const LAS u32x4*)a; }
DI bf16x8 lds_tr2(unsigned a0, unsigned a1) {
  s16x4 lo = __builtin_amdgcn_ds_read_tr16_b64_v4i16((LAS s16x4*)a0);
  s16x4 hi = __builtin_amdgcn_ds_read_tr16_b64_v4i16((LAS s16x4*)a1);
  s16x8 v = __builtin_shufflevector(lo, hi, 0, 1, 2, 3, 4, 5, 6, 7);
  return __builtin_bit_cast(bf16x8, v);
}
DI void mfma_vvv(f32x4& c, const bf16x8& a, const bf16x8& b) { asm volatile("v_mfma_f32_16x16x32_bf16 %0, %1, %2, %0" : "+v"(c) : "v"(a), "v"(b)); }
DI void mfma_vvv0(f32x4& c, const bf16x8& a, const bf16x8& b) { asm volatile("v_mfma_f32_16x16x32_bf16 %0, %1, %2, 0" : "=&v"(c) : "v"(a), "v"(b)); }
DI void mfma_vav(f32x4& c, const u32x4& a, const u32x4& b) { asm volatile("v_mfma_f32_16x16x32_bf16 %0, %1, %2, %0" : "+v"(c) : "a"(a), "v"(b)); }
DI void mfma_vav0(f32x4& c, const u32x4& a, const u32x4& b) { asm volatile("v_mfma_f32_16x16x32_bf16 %0, %1, %2, 0" : "=&v"(c) : "a"(a), "v"(b)); }
#define MFMA_DRAIN() do { SB(); asm volatile("s_nop 7\n\ts_nop 3" ::: "memory"); SB(); } while (0)
constexpr int SC1 = 16;
DI u32x4 gld128(rsrc_t r, unsigned voff, unsigned soff) { return __builtin_amdgcn_raw_buffer_load_b128(r, voff, soff, SC1); }
DI unsigned gld32(rsrc_t r, unsigned voff, unsigned soff) { return __builtin_amdgcn_raw_buffer_load_b32(r, voff, soff, SC1); }
DI void gst128(u32x4 v, rsrc_t r, unsigned voff, unsigned soff) { __builtin_amdgcn_raw_buffer_store_b128(v, r, voff, soff, SC1); }
DI void gst64(u32x2 v, rsrc_t r, unsigned voff, unsigned soff) { __builtin_amdgcn_raw_buffer_store_b64(v, r, voff, soff, SC1); }
DI void gst32(unsigned v, rsrc_t r, unsigned voff, unsigned soff) { __builtin_amdgcn_raw_buffer_store_b32(v, r, voff, soff, SC1); }
#define RLX_AGENT __ATOMIC_RELAXED, __HIP_MEMORY_SCOPE_AGENT
DI u64 flag_ld(const char* a) { return __hip_atomic_load((const GAS u64*)a, RLX_AGENT); }
DI void flag_st(char* a, u64 v) { __hip_atomic_store((GAS u64*)a, v, RLX_AGENT); }
template <int N> DI void wait_flags(const char* base, u64 epoch, char* tmo, int lane) {
  for (unsigned spins = 0;; ++spins) {
    bool ok = true;
#pragma unroll
    for (int i = 0; i < (N + 63) / 64; ++i) { const int idx = i * 64 + lane; const u64 v = flag_ld(base + (idx < N ? idx : 0) * 8); ok &= (v == epoch); }
    if (__all(ok)) break;
    if (spins > 30000u) { if (lane == 0) flag_st(tmo, 1); break; }
    __builtin_amdgcn_s_sleep(1);
  }
  __builtin_amdgcn_fence(__ATOMIC_ACQUIRE, "wavefront");
}
DI f32x4 selv(unsigned m, f32x4 a, f32x4 b) {
  f32x4 r;
#pragma unroll
  for (int e = 0; e < 4; ++e) r[e] = __uint_as_float((__float_as_uint(a[e]) & m) | (__float_as_uint(b[e]) & ~m));
  return r;
}
DI unsigned gelu_mul2(f32x2 g, f32x2 u) {
  const f32x2 c1 = {-2.302208198f, -2.302208198f}, c2 = {-0.1029432397f, -0.1029432397f}, one = {1.f, 1.f};
  f32x2 a = g * __builtin_elementwise_fma(g * g, c2, c1);
  f32x2 e = {__builtin_amdgcn_exp2f(a[0]), __builtin_amdgcn_exp2f(a[1])};
  f32x2 d = e + one;
  f32x2 r = {__builtin_amdgcn_rcpf(d[0]), __builtin_amdgcn_rcpf(d[1])};
  f32x2 h = (g * u) * r;
  return cvtpk(h[0], h[1]);
}

DI float wave_sum(float a) {
  a += __builtin_bit_cast(float, __builtin_amdgcn_update_dpp(0, __builtin_bit_cast(int, a), 0xB1, 0xf, 0xf, true));
  a += __builtin_bit_cast(float, __builtin_amdgcn_update_dpp(0, __builtin_bit_cast(int, a), 0x4E, 0xf, 0xf, true));
  a += __builtin_bit_cast(float, __builtin_amdgcn_update_dpp(0, __builtin_bit_cast(int, a), 0x141, 0xf, 0xf, true));
  a += __builtin_bit_cast(float, __builtin_amdgcn_update_dpp(0, __builtin_bit_cast(int, a), 0x140, 0xf, 0xf, true));
  const int x = __builtin_bit_cast(int, a);
  return (__builtin_bit_cast(float, __builtin_amdgcn_readlane(x, 0)) + __builtin_bit_cast(float, __builtin_amdgcn_readlane(x, 16))) +
         (__builtin_bit_cast(float, __builtin_amdgcn_readlane(x, 32)) + __builtin_bit_cast(float, __builtin_amdgcn_readlane(x, 48)));
}
struct Params { const u16 *q, *k, *v, *shk, *hk, *Dv, *wg, *wu, *wd, *gw; u16* out; char* ws; };

struct G1 {
  rsrc_t xs, wsr;
  unsigned xv[2], wv[7];
  unsigned xsoff, wsoff, xm0, wm0;
  unsigned xa[2], xb[2], wt, w6;
  f32x4 acc[3][2][4], acc6[2][2];
  bf16x8 X[2][4], W[2][8];
};
template <int N> DI void g1_dma(G1& c, unsigned st, unsigned xo, unsigned wo) {
  if constexpr (N < 4) dma16(c.xs, c.xv[N & 1], c.xsoff + N * 8192 + st * 128, c.xm0 + xo + N * 1024);
  else dma16(c.wsr, c.wv[N - 4], c.wsoff + st * (64 * 3584), c.wm0 + wo + (N - 4) * 1024);
}
template <int... Ns> DI void g1_dma_all(G1& c, unsigned st, unsigned xo, unsigned wo, iseq<Ns...>) { (g1_dma<Ns>(c, st, xo, wo), ...); }
template <int KK, int R> DI void g1_read(G1& c) {
  constexpr int xi[12] = {0, -1, 1, 2, 3, -1, -1, -1, -1, -1, -1, -1};
  constexpr int wi[12] = {-1, 0, -1, -1, -1, 1, 2, 3, 4, 5, 6, 7};
  if constexpr (xi[R] >= 0) { constexpr int i = xi[R]; c.X[KK][i] = lds_b128((i < 2 ? c.xa[KK] : c.xb[KK]) + (i & 1) * 2048); }
  else {
    constexpr int i = wi[R], pr = i >> 1, gu = i & 1;
    constexpr unsigned o = KK * 32 * 224 + gu * 14336;
    if constexpr (pr < 3) c.W[KK][i] = lds_tr2(c.wt + o + pr * 32, c.wt + o + pr * 32 + 8 * 224);
    else c.W[KK][i] = lds_tr2(c.w6 + o, c.w6 + o + 8 * 224);
  }
}
template <int KK, int M, bool FIRST> DI void g1_mfma(G1& c) {
  if constexpr (M < 24) {
    constexpr int pr = M >> 3, gu = (M >> 2) & 1, st = M & 3;
    mfma_vvv(c.acc[pr][gu][st], c.W[KK][2 * pr + gu], c.X[KK][st]);
  } else {
    constexpr int gu = (M - 24) >> 1, st = M & 1;
    mfma_vvv(c.acc6[gu][st], c.W[KK][6 + gu], c.X[KK][st]);
  }
}
struct G1Stage { unsigned s, xo2, wo2, xo3, wo3, dx, dw; };
struct Tail {
  rsrc_t hs, xr, wsr; unsigned hvoff, hm0, zm0, xvoff, wdso, xlds; const char* sflags; u64 epoch; char* tmo;
  u64 pf[4]; u32x4 xv[3];
};
DI void poll_issue(u64& v, const char* a) { asm volatile("global_load_dwordx2 %0, %1, off sc1" : "=v"(v) : "v"(a) : "memory"); }
template <int I> DI void tail_op(Tail& t, u32x4 (&Wd)[14][4], int lane) {
  if constexpr (I < 4) dma16(t.hs, t.hvoff, I * 1024, t.hm0 + I * 1024);
  else if constexpr (I == 4) dma16(t.hs, lane * 16, 0, t.zm0);
  else if constexpr (I < 9) dma16(t.hs, t.hvoff + 2, (I - 5) * 1024, t.hm0 + CV_HS + (I - 5) * 1024);
  else if constexpr (I < 12) t.xv[I - 9] = gld128(t.xr, t.xvoff + (I - 9) * 1024, 0);
  else { constexpr int n = I - 12; Wd[n >> 2][n & 3] = gld128(t.wsr, lane * 16 + (n & 3) * 1024, t.wdso + (n >> 2) * 4096); }
}
constexpr int TAIL_WD = 16;
template <int MODE, int M> DI void g1_gap0(G1& c, G1Stage& g, Tail& t, u32x4 (&Wd)[14][4], int lane) {
  if constexpr (MODE == 5 && M == 0) {
#pragma unroll
    for (int i = 0; i < 4; ++i) poll_issue(t.pf[i], t.sflags + (i * 64 + lane) * 8);
  }
  g1_mfma<0, M, false>(c);
  if constexpr (M >= 2 && M < 26 && (M & 1) == 0) g1_read<1, (M - 2) / 2>(c);
  if constexpr (M % 5 == 0) {
    if constexpr (MODE <= 5) g1_dma<5 + M / 5>(c, g.s + 2, g.xo2, g.wo2);
    if constexpr (MODE == 6) tail_op<5 + M / 5>(t, Wd, lane);
    if constexpr (MODE == 7) tail_op<16 + M / 5>(t, Wd, lane);
  }
  SB();
}
template <int MODE, int M> DI void g1_gap1(G1& c, G1Stage& g, Tail& t, u32x4 (&Wd)[14][4], int lane) {
  g1_mfma<1, M, false>(c);
  if constexpr (MODE != 7 && M == 2) {
    SB();
    if constexpr (MODE == 5) {
      asm volatile("s_waitcnt vmcnt(6)" : "+v"(t.pf[0]), "+v"(t.pf[1]), "+v"(t.pf[2]), "+v"(t.pf[3]) :: "memory");
      const bool ok = t.pf[0] == t.epoch && t.pf[1] == t.epoch && t.pf[2] == t.epoch && t.pf[3] == t.epoch;
      if (!__all(ok)) wait_flags<256>(t.sflags, t.epoch, t.tmo, lane);
    }
    WAIT_VM_BAR(11);
    c.xa[0] += g.dx; c.xa[1] += g.dx; c.xb[0] += g.dx; c.xb[1] += g.dx; c.wt += g.dw; c.w6 += g.dw;
  }
  if constexpr (MODE != 7 && M >= 3 && M < 27 && (M & 1) == 1) g1_read<0, (M - 3) / 2>(c);
  if constexpr (M % 5 == 3 && M < 28) {
    if constexpr (MODE == 0 && M < 25) g1_dma<M / 5>(c, g.s + 3, g.xo3, g.wo3);
    if constexpr (MODE == 5 && M < 25) tail_op<M / 5>(t, Wd, lane);
    if constexpr (MODE == 6 && M < 25) tail_op<11 + M / 5>(t, Wd, lane);
    if constexpr (MODE == 7) tail_op<22 + M / 5>(t, Wd, lane);
  }
  if constexpr (MODE == 7 && M == 27) tail_op<27>(t, Wd, lane);
  if constexpr (MODE == 7 && M >= 10 && M < 13) *(LAS u32x4*)(t.xlds + (M - 10) * 1024) = t.xv[M - 10];
  if constexpr (MODE == 0 && M == 26) {
    const unsigned slot = g.xo2 == 0 ? 2 : (g.xo2 == G1_XS ? 0 : 1);
    const unsigned n1 = slot == 2 ? 0 : slot + 1, n2 = n1 == 2 ? 0 : n1 + 1;
    g.s += 1; g.xo3 = slot * G1_XS; g.wo3 = slot * G1_WS; g.xo2 = n2 * G1_XS; g.wo2 = n2 * G1_WS;
    g.dx = (n1 - slot) * G1_XS; g.dw = (n1 - slot) * G1_WS;
    asm volatile("" : "+s"(g.s), "+s"(g.xo3), "+s"(g.wo3), "+s"(g.xo2), "+s"(g.wo2), "+s"(g.dx), "+s"(g.dw));
  }
  SB();
}
template <int MODE, int... Ms> DI void g1_stage(G1& c, G1Stage& g, Tail& t, u32x4 (&Wd)[14][4], int lane, iseq<Ms...>) { (g1_gap0<MODE, Ms>(c, g, t, Wd, lane), ...); (g1_gap1<MODE, Ms>(c, g, t, Wd, lane), ...); }
template <int... Rs> DI void g1_read0(G1& c, iseq<Rs...>) { (g1_read<0, Rs>(c), ...); }

__global__ __launch_bounds__(256, 1) void k_fused(Params p) {
  __shared__ __attribute__((aligned(16))) char smem[LDS_BYTES];
  const unsigned lds0 = (unsigned)(size_t)(LAS char*)smem;
  const int lane = threadIdx.x & 63;
  const unsigned w = rfl(threadIdx.x >> 6);
  const unsigned b = blockIdx.x, xcd = b & 7, jb = b >> 3;
  char* const ws = p.ws;
  STAMP_DECL; STAMP(0);
  const rsrc_t wsr = mk_rsrc(ws, WS_END);
  const u64 epoch = *(const u64*)(ws + WS_E) * 6364136223846793005ull + 1442695040888963407ull;

  const unsigned sb = b >> 3, db = b & 7, s0p = sb * 64, d0p = db * 64;
  const unsigned kg_w = b >> 2, cq_w = b & 3;
  {
    const unsigned r8 = lane >> 3, c8 = lane & 7;
    if (w == 0) {
      rsrc_t r = mk_rsrc(p.k, S_ * D_ * 2);
#pragma unroll
      for (int i = 0; i < 9; ++i) { int row = (int)s0p - 3 + 8 * i + (int)r8; unsigned vo = row >= 0 ? (unsigned)row * 1024 + d0p * 2 + c8 * 16 : 0x80000000u; dma16(r, vo, 0, lds0 + ST_K + i * 1024); }
    } else if (w == 1) {
      rsrc_t r = mk_rsrc(p.v, S_ * D_ * 2);
#pragma unroll
      for (int i = 0; i < 8; ++i) dma16(r, r8 * 1024 + c8 * 16, (s0p + 8 * i) * 1024 + d0p * 2, lds0 + ST_V + i * 1024);
      dma16(mk_rsrc(p.shk, D_ * 8), lane * 16, d0p * 8, lds0 + ST_SH);
    } else if (w == 2) {
      rsrc_t r = mk_rsrc(p.q, S_ * D_ * 2);
#pragma unroll
      for (int i = 0; i < 8; ++i) dma16(r, lane * 16, (b * 8 + i) * 1024, lds0 + ST_GX + i * 1024);
      dma16(mk_rsrc(p.gw, D_ * 2), lane * 16, 0, lds0 + ST_GW);
    } else {
      rsrc_t r = mk_rsrc(p.wd, F_ * D_ * 2);
#pragma unroll
      for (int i = 0; i < 8; ++i) dma16(r, (lane >> 4) * 1024 + (lane & 15) * 16, (kg_w * 32 + 4 * i) * 1024 + cq_w * 256, lds0 + ST_WD + i * 1024);
      dma16(r, lane * 16, 0x80000000u >> 1, lds0 + ST_T);
    }
  }
  const unsigned rb = 8 * (xcd & 1) + (jb & 7), fc = 4 * (xcd >> 1) + (jb >> 3);
  const unsigned mi = w >> 1, ni = w & 1;
  G1 c;
  c.xs = mk_rsrc(p.q, S_ * D_ * 2);
  c.wsr = mk_rsrc((w >> 1) ? p.wu : p.wg, D_ * F_ * 2);
#pragma unroll
  for (int e = 0; e < 2; ++e) c.xv[e] = (lane >> 3) * 1024 + (((lane & 7) ^ ((4 * e + (lane >> 4)) & 7)) << 4);
#pragma unroll
  for (int jj = 0; jj < 7; ++jj) {
    unsigned ci = 64 * (7 * (w & 1) + jj) + lane, rho = ci / 14, pc = ci - rho * 14;
    unsigned k = (rho & ~12u) | ((rho & 4) << 1) | ((rho & 8) >> 1);
    c.wv[jj] = k * 3584 + pc * 16;
  }
  c.xsoff = (rb * 128 + 32 * w) * 1024;
  c.wsoff = fc * 224;
  c.xm0 = lds0 + G1_XOFF + w * 4096;
  c.wm0 = lds0 + G1_WOFF + (w >> 1) * 14336 + (w & 1) * 7 * 1024;
  {
    const int li = lane & 15, gq = lane >> 4;
#pragma unroll
    for (int kk = 0; kk < 2; ++kk) {
      unsigned bb = li * 128 + (((4 * kk + gq) ^ ((li >> 1) & 7)) << 4);
      c.xa[kk] = lds0 + G1_XOFF + (4 * mi + 2 * ni) * 2048 + bb;
      c.xb[kk] = lds0 + G1_XOFF + (4 * mi + 2 * (1 - ni)) * 2048 + bb;
    }
    const int q = li >> 2, pp = lane & 3;
    unsigned wb = lds0 + G1_WOFF + (16 * (gq >> 1) + 4 * (gq & 1) + q) * 224 + pp * 8;
    c.wt = wb + ni * 96; c.w6 = wb + 6 * 32;
  }
  g1_dma_all(c, 0, 0, 0, make_iseq<11>{});
  STAMP(1);
  WAIT_VM_BAR(11);
  STAMP(2);

  {
    const int t = threadIdx.x, cc = t & 7, rg = t >> 3;
    u32x4 kr[5], vr[2], w4[4];
#pragma unroll
    for (int i = 0; i < 5; ++i) kr[i] = lds_u128(lds0 + ST_K + (2 * rg + i) * 128 + cc * 16);
#pragma unroll
    for (int i = 0; i < 2; ++i) vr[i] = lds_u128(lds0 + ST_V + (2 * rg + i) * 128 + cc * 16);
#pragma unroll
    for (int i = 0; i < 4; ++i) w4[i] = lds_u128(lds0 + ST_SH + cc * 64 + i * 16);
#pragma unroll
    for (int j = 0; j < 8; ++j) {
      unsigned wa = w4[j >> 1][(j & 1) * 2], wb = w4[j >> 1][(j & 1) * 2 + 1];
      float w0 = bflo(wa), w1 = bfhi(wa), w2 = bflo(wb), w3 = bfhi(wb);
      float kk[5];
#pragma unroll
      for (int i = 0; i < 5; ++i) kk[i] = (j & 1) ? bfhi(kr[i][j >> 1]) : bflo(kr[i][j >> 1]);
      float a0 = kk[0] * w3 + kk[1] * w2 + kk[2] * w1 + kk[3] * w0;
      float a1 = kk[1] * w3 + kk[2] * w2 + kk[3] * w1 + kk[4] * w0;
      float v0 = (j & 1) ? bfhi(vr[0][j >> 1]) : bflo(vr[0][j >> 1]);
      float v1 = (j & 1) ? bfhi(vr[1][j >> 1]) : bflo(vr[1][j >> 1]);
      const int wd = 31 - rg, dl = 8 * cc + j;
      *(LAS unsigned*)(lds0 + ST_T + (dl * 32 + ((((wd >> 2) ^ cc) & 7) << 2) + (wd & 3)) * 4) = cvtpk(a1 * v1, a0 * v0);
      if (j == 0) { g1_dma<0>(c, 1, G1_XS, G1_WS); } if (j == 1) { g1_dma<1>(c, 1, G1_XS, G1_WS); g1_dma<2>(c, 1, G1_XS, G1_WS); }
      if (j == 2) { g1_dma<3>(c, 1, G1_XS, G1_WS); } if (j == 3) { g1_dma<4>(c, 1, G1_XS, G1_WS); g1_dma<5>(c, 1, G1_XS, G1_WS); }
      if (j == 4) { g1_dma<6>(c, 1, G1_XS, G1_WS); } if (j == 5) { g1_dma<7>(c, 1, G1_XS, G1_WS); g1_dma<8>(c, 1, G1_XS, G1_WS); }
      if (j == 6) { g1_dma<9>(c, 1, G1_XS, G1_WS); } if (j == 7) { g1_dma<10>(c, 1, G1_XS, G1_WS); }
      SB();
    }
    u32x4 g4 = lds_u128(lds0 + ST_GW + lane * 16);
    float gsum[2];
#pragma unroll
    for (int r = 0; r < 2; ++r) {
      u32x4 x4 = lds_u128(lds0 + ST_GX + (2 * w + r) * 1024 + lane * 16);
      float a = 0.f;
#pragma unroll
      for (int j = 0; j < 4; ++j) a += bflo(x4[j]) * bflo(g4[j]) + bfhi(x4[j]) * bfhi(g4[j]);
      a = wave_sum(a);
      gsum[r] = __builtin_amdgcn_rcpf(1.f + __builtin_amdgcn_exp2f(-1.4426950409f * a));
    }
    gst32(__float_as_uint(lane & 1 ? gsum[1] : gsum[0]), wsr, lane < 2 ? WS_G + (b * 8 + 2 * w + lane) * 4 : 0x80000000u, 0);
    const int li = lane & 15, gq = lane >> 4;
#pragma unroll
    for (int i = 0; i < 2; ++i) {
      const unsigned dt8 = 2 * w + i, a = lds0 + ST_WD + (8 * gq + (li >> 2)) * 256 + dt8 * 32 + (lane & 3) * 8;
      bf16x8 f = lds_tr2(a, a + 4 * 256);
      const unsigned D16 = 8 * cq_w + dt8;
      gst128(__builtin_bit_cast(u32x4, f), wsr, kg_w < 56 ? WS_WDT + (((D16 >> 2) * 56 + kg_w) * 4 + (D16 & 3)) * 1024 + lane * 16 : 0x80000000u, 0);
    }
    LGKM_BAR();
#pragma unroll
    for (int i = 0; i < 2; ++i) {
      const int dl = (w * 2 + i) * 8 + (lane >> 3), ch = lane & 7;
      u32x4 val = lds_u128(lds0 + ST_T + dl * 128 + ((ch ^ (dl >> 3)) & 7) * 16);
      gst128(val, wsr, WS_KVT + (d0p + dl) * 4096 + (1984 - s0p) * 2 + ch * 16, 0);
    }
  }
  STAMP(3);
  WAIT_VM_BAR(16);
  g1_dma<0>(c, 2, 2 * G1_XS, 2 * G1_WS); g1_dma<1>(c, 2, 2 * G1_XS, 2 * G1_WS); g1_dma<2>(c, 2, 2 * G1_XS, 2 * G1_WS); g1_dma<3>(c, 2, 2 * G1_XS, 2 * G1_WS); g1_dma<4>(c, 2, 2 * G1_XS, 2 * G1_WS);
  g1_read0(c, make_iseq<12>{});
#pragma unroll
  for (int i = 0; i < 3; ++i)
#pragma unroll
    for (int j = 0; j < 2; ++j)
#pragma unroll
      for (int k = 0; k < 4; ++k) c.acc[i][j][k] = f32x4{0, 0, 0, 0};
#pragma unroll
  for (int j = 0; j < 2; ++j) { c.acc6[j][0] = f32x4{0, 0, 0, 0}; c.acc6[j][1] = f32x4{0, 0, 0, 0}; }
  SB();
  asm volatile("s_nop 1" ::: "memory");
  SB();
  STAMP(4);
  const unsigned cch = w >> 1, hf = w & 1, dch = b * 2 + cch, cvb = lds0 + CV_BASE + cch * CV_CH, cvz = lds0 + CV_Z + cch * 1088;
  const unsigned rb2 = 4 * xcd + (jb & 3), cb = jb >> 2;
  u32x4 Wd[14][4];
  Tail t;
  t.hs = mk_rsrc(p.hk + (size_t)dch * S_, (D_ - dch) * S_ * 2);
  t.hvoff = lane * 16 + (2 * hf + 17) * 2; t.hm0 = cvb + 2 * hf * CV_HS; t.zm0 = cvz + 32;
  t.xr = mk_rsrc(ws + WS_KVT + dch * 4096, 4096); t.xvoff = (hf * 128 + lane) * 16; t.xlds = cvb + CV_H + (hf * 128 + lane) * 16;
  t.wsr = wsr; t.wdso = WS_WDT + (cb * 56 + 14 * w) * 4096;
  t.sflags = ws + WS_FS; t.epoch = epoch; t.tmo = ws + WS_TMO;
  {
    G1Stage g;
    g.s = 0; g.xo2 = 2 * G1_XS; g.wo2 = 2 * G1_WS; g.xo3 = 0; g.wo3 = 0; g.dx = G1_XS; g.dw = G1_WS;
#pragma unroll 1
    for (unsigned s = 0; s < 5; ++s) {
      DSTAMP(s);
      g1_stage<0>(c, g, t, Wd, lane, make_iseq<28>{});
      if (s == 0 && threadIdx.x == 0) flag_st(ws + WS_FS + b * 8, epoch);
    }
    g.s = 5; g.xo2 = 1 * G1_XS; g.wo2 = 1 * G1_WS; g.xo3 = 0; g.wo3 = 0; g.dx = 0u - 2 * G1_XS; g.dw = 0u - 2 * G1_WS;
    DSTAMP(5);
    g1_stage<5>(c, g, t, Wd, lane, make_iseq<28>{});
    if (b == 0 && threadIdx.x == 0) flag_st(ws + WS_E, epoch);
    if (lane < 8) *(LAS unsigned*)(cvz + lane * 4) = 0;
    g.s = 6; g.dx = G1_XS; g.dw = G1_WS;
    DSTAMP(6);
    g1_stage<6>(c, g, t, Wd, lane, make_iseq<28>{});
    DSTAMP(7);
    g1_stage<7>(c, g, t, Wd, lane, make_iseq<28>{});
    DSTAMP(8);
  }
  MFMA_DRAIN();
  STAMP(5);
  LGKM_BAR();
  auto ldWd = [&](int n) { Wd[n >> 2][n & 3] = gld128(wsr, lane * 16 + (n & 3) * 1024, t.wdso + (n >> 2) * 4096); };
  {
    const int li = lane & 15, gq = lane >> 4;
    const unsigned lo = ((gq >> 1) * 16 + li) * 16 + (gq & 1) * 8;
    const unsigned stA = rb * 8 + 4 * mi + 2 * ni, stB = rb * 8 + 4 * mi + 2 * (1 - ni);
    auto put = [&](int u, const f32x4& g, const f32x4& uu, unsigned stg, unsigned ft) {
      ldWd(TAIL_WD + u);
      u32x2 o = {gelu_mul2(f32x2{g[0], g[1]}, f32x2{uu[0], uu[1]}), gelu_mul2(f32x2{g[2], g[3]}, f32x2{uu[2], uu[3]})};
      gst64(o, wsr, lo, WS_H + (stg * 56 + (ft >> 1)) * 1024 + (ft & 1) * 512);
      SB();
    };
#pragma unroll
    for (int pr = 0; pr < 3; ++pr)
#pragma unroll
      for (int st = 0; st < 4; ++st) put(pr * 4 + st, c.acc[pr][0][st], c.acc[pr][1][st], (st < 2 ? stA : stB) + (st & 1), fc * 7 + 3 * ni + pr);
#pragma unroll
    for (int st = 0; st < 2; ++st) put(12 + st, c.acc6[0][st], c.acc6[1][st], stA + st, fc * 7 + 6);
  }
  STAMP(6);

  constexpr int PD = 3;
  u32x4 H[PD + 1][4];
  const unsigned hbase = WS_H + ((4 * rb2) * 56 + 14 * w) * 1024;
  auto ldH = [&](int i) {
#pragma unroll
    for (int st = 0; st < 4; ++st) H[i % (PD + 1)][st] = gld128(wsr, lane * 16, hbase + (st * 56 + i) * 1024);
  };
  {
    const int li = lane & 15, gq = lane >> 4;
    u32x4 a0;
#pragma unroll
    for (int e = 0; e < 4; ++e) {
      const unsigned o = cvz + (li + 8 * gq + 2 * e + 1) * 2;
      a0[e] = (unsigned)*(const LAS u16*)o | ((unsigned)*(const LAS u16*)(o + 2) << 16);
    }
    const float Dd = __uint_as_float((unsigned)p.Dv[dch] << 16);
    const unsigned abase = cvb + (li & 3) * CV_HS + (li >> 2) * 8 + gq * 16 + hf * 4 * 64 - 64;
    const unsigned bbase = cvb + CV_H + (254 - 2 * li + gq) * 16 - 64 * 56 + hf * 4 * 64;
    f32x4 acc[8];
    u32x2 alo[2][8], ahi[2][8];
    bf16x8 B[2][8];
    auto rdA = [&](int set, int rr, int al) {
      alo[set][al] = *(const volatile LAS u32x2*)(abase + rr * 64 + al * 512);
      ahi[set][al] = *(const volatile LAS u32x2*)(abase + rr * 64 + al * 512 + 8);
    };
    auto rdB = [&](int set, int rr, int be) { B[set][be] = lds_b128(bbase + 64 * (56 - 8 * be) + 64 * rr); };
#pragma unroll
    for (int i = 0; i < 8; ++i) { rdA(0, 0, i); rdB(0, 0, i); }
    SB();
    STAMP(7);
    u64 hflag = 0;
#pragma unroll
    for (int rr = 0; rr < 4; ++rr) {
      const int set = rr & 1;
      int m = 0;
#pragma unroll
      for (int al = 0; al < 8; ++al) {
        u32x4 av = {alo[set][al][0], alo[set][al][1], ahi[set][al][0], ahi[set][al][1]};
        if (rr == 0 && al == 0) { const unsigned hm0 = 0u - hf; for (int e = 0; e < 4; ++e) av[e] = (av[e] & hm0) | (a0[e] & ~hm0); }
        bf16x8 A = __builtin_bit_cast(bf16x8, av);
        if (rr == 0 && al == 0) asm volatile("s_nop 1" : "+v"(A));
#pragma unroll
        for (int be = 0; be + al < 8; ++be) {
          if (rr == 0 && al == 0) mfma_vvv0(acc[be], A, B[set][be]); else mfma_vvv(acc[al + be], A, B[set][be]);
          if (rr < 3 && m >= 2 && m < 34 && (m & 1) == 0) { const int i = (m - 2) >> 2; if (((m - 2) >> 1) & 1) rdB(set ^ 1, rr + 1, i); else rdA(set ^ 1, rr + 1, i); }
          if (m % 5 == 3 && rr * 7 + m / 5 < 26) ldWd(TAIL_WD + 14 + rr * 7 + m / 5);
          if (rr == 3 && m == 30) hflag = flag_ld(ws + WS_FH + (rb2 >> 1) * 128 + li * 8);
          ++m;
          SB();
        }
      }
      if (rr == 0) {
        WAIT_VM(7); asm volatile("s_barrier" ::: "memory");
        if (threadIdx.x == 0) flag_st(ws + WS_FH + rb * 128 + fc * 8, epoch);
        SB();
      }
    }
    MFMA_DRAIN();
    STAMP(8);
    if (!__all(hflag == epoch)) wait_flags<16>(ws + WS_FH + (rb2 >> 1) * 128, epoch, ws + WS_TMO, lane);
    __builtin_amdgcn_fence(__ATOMIC_ACQUIRE, "wavefront");
#pragma unroll
    for (int i = 0; i < PD; ++i) ldH(i);
    SB();
    const unsigned red = lds0 + CV_RED, hm = 0u - hf;
#pragma unroll
    for (int g = 0; g < 4; ++g) *(LAS f32x4*)(red + w * 4096 + g * 1024 + lane * 16) = selv(hm, acc[g], acc[4 + g]);
    LGKM_BAR();
#pragma unroll
    for (int g = 0; g < 4; ++g) {
      f32x4 o = *(const LAS f32x4*)(red + (w ^ 1) * 4096 + g * 1024 + lane * 16);
      f32x4 y = selv(hm, acc[4 + g], acc[g]);
      y += o;
      const int n0 = 256 * (g + 4 * hf) + 16 * li + 4 * gq;
      u32x2 kvr = *(const LAS u32x2*)(cvb + CV_H + (2044 - n0) * 2);
      float r0 = y[0] + bfhi(kvr[1]) * Dd, r1 = y[1] + bflo(kvr[1]) * Dd, r2 = y[2] + bfhi(kvr[0]) * Dd, r3 = y[3] + bflo(kvr[0]) * Dd;
      gst64(u32x2{cvtpk(r0, r1), cvtpk(r2, r3)}, wsr, n0 * 2, WS_Y2T + dch * 4096);
    }
    SB();
  }
  STAMP(9);

  const int tt = threadIdx.x, cc = tt & 7, sp = tt >> 3;
  const unsigned s0 = rb2 * 64, d0 = cb * 64;
  unsigned yv[8]; u32x2 gt; u32x4 qv[2];
  {
    f32x4 acc[4][4];
    u64 yflag = 0;
#pragma unroll
    for (int i = 0; i < 14; ++i) {
      if (i + PD < 14) ldH(i + PD);
      if (i == 9) yflag = flag_ld(ws + WS_FY + cb * 256 + (lane & 31) * 8);
      if (i == 11) {
        if (!__all(yflag == epoch)) wait_flags<32>(ws + WS_FY + cb * 256, epoch, ws + WS_TMO, lane);
        __builtin_amdgcn_fence(__ATOMIC_ACQUIRE, "wavefront");
#pragma unroll
        for (int e = 0; e < 8; ++e) yv[e] = gld32(wsr, (8 * cc + e) * 4096 + sp * 4, WS_Y2T + d0 * 4096 + s0 * 2);
      }
      if (i == 12) {
        gt = __builtin_amdgcn_raw_buffer_load_b64(wsr, sp * 8, WS_G + s0 * 4, SC1);
#pragma unroll
        for (int r = 0; r < 2; ++r) qv[r] = *(const u32x4*)(p.q + (size_t)(s0 + 2 * sp + r) * D_ + d0 + cc * 8);
      }
      SB();
#pragma unroll
      for (int dt = 0; dt < 4; ++dt)
#pragma unroll
        for (int st = 0; st < 4; ++st) {
          if (i == 0) mfma_vav0(acc[dt][st], Wd[i][dt], H[i % (PD + 1)][st]); else mfma_vav(acc[dt][st], Wd[i][dt], H[i % (PD + 1)][st]);
        }
      SB();
      if (i == 3) {
        WAIT_VM(12); asm volatile("s_barrier" ::: "memory");
        if (threadIdx.x == 0) flag_st(ws + WS_FY + (b >> 5) * 256 + (b & 31) * 8, epoch);
        SB();
      }
    }
    MFMA_DRAIN();
    STAMP(10);
    const int li = lane & 15, gq = lane >> 4;
#pragma unroll
    for (int dt = 0; dt < 4; ++dt)
#pragma unroll
      for (int st = 0; st < 4; ++st) {
        const int s = 16 * st + li, chunk = 4 * dt + gq;
        *(LAS f32x4*)(lds0 + w * 16384 + s * 256 + ((chunk ^ li) << 4)) = acc[dt][st];
      }
  }
  LGKM_BAR();
  {
#pragma unroll
    for (int r = 0; r < 2; ++r) {
      const int s = 2 * sp + r;
      f32x4 b0 = {0, 0, 0, 0}, b1 = {0, 0, 0, 0};
#pragma unroll
      for (int ww = 0; ww < 4; ++ww) {
        b0 += *(const LAS f32x4*)(lds0 + ww * 16384 + s * 256 + (((2 * cc) ^ (s & 15)) << 4));
        b1 += *(const LAS f32x4*)(lds0 + ww * 16384 + s * 256 + (((2 * cc + 1) ^ (s & 15)) << 4));
      }
      const float g = __uint_as_float(gt[r]);
      u32x4 o;
#pragma unroll
      for (int e = 0; e < 4; ++e) {
        float bl = e < 2 ? b0[2 * e] : b1[2 * e - 4], bh = e < 2 ? b0[2 * e + 1] : b1[2 * e - 3];
        float yl = r ? bfhi(yv[2 * e]) : bflo(yv[2 * e]), yh = r ? bfhi(yv[2 * e + 1]) : bflo(yv[2 * e + 1]);
        float al = bflo(qv[r][e]) * yl, ah = bfhi(qv[r][e]) * yh;
        o[e] = cvtpk(g * al + (1.f - g) * bl, g * ah + (1.f - g) * bh);
      }
      *(u32x4*)(p.out + (size_t)(s0 + s) * D_ + d0 + cc * 8) = o;
    }
  }
  STAMP(11);
  STAMP_FLUSH(12);
}

extern "C" void kernel_launch(void* const* d_in, const int* in_sizes, int n_in, void* d_out, int out_size, void* d_ws, size_t ws_size, hipStream_t stream) {
  Params p;
  p.q = (const u16*)d_in[0]; p.k = (const u16*)d_in[1]; p.v = (const u16*)d_in[2];
  p.shk = (const u16*)d_in[3]; p.hk = (const u16*)d_in[4]; p.Dv = (const u16*)d_in[5];
  p.wg = (const u16*)d_in[7]; p.wu = (const u16*)d_in[8]; p.wd = (const u16*)d_in[9]; p.gw = (const u16*)d_in[10];
  p.out = (u16*)d_out; p.ws = (char*)d_ws;
  k_fused<<<256, 256, 0, stream>>>(p);
}
```

```cpp
#include <hip/hip_runtime.h>
#include <hip/hip_bf16.h>
#include <utility>
#pragma clang diagnostic ignored "-Winline-asm"
#pragma clang diagnostic ignored "-Wint-to-pointer-cast"

#define DI __device__ __forceinline__
#define LAS __attribute__((address_space(3)))
#define GAS __attribute__((address_space(1)))
typedef unsigned short u16;
typedef unsigned long long u64;
typedef float f32x4 __attribute__((ext_vector_type(4)));
typedef float f32x2 __attribute__((ext_vector_type(2)));
typedef __bf16 bf16x8 __attribute__((ext_vector_type(8)));
typedef __bf16 bf16x2 __attribute__((ext_vector_type(2)));
typedef short s16x4 __attribute__((ext_vector_type(4)));
typedef short s16x8 __attribute__((ext_vector_type(8)));
typedef unsigned u32x4 __attribute__((ext_vector_type(4)));
typedef unsigned u32x2 __attribute__((ext_vector_type(2)));
typedef __amdgpu_buffer_rsrc_t rsrc_t;
template <int... Is> using iseq = std::integer_sequence<int, Is...>;
template <int N> using make_iseq = std::make_integer_sequence<int, N>;

constexpr int S_ = 2048, D_ = 512, F_ = 1792;
constexpr unsigned WS_E = 0, WS_FS = 256, WS_FH = 4096, WS_FY = 8192, WS_TMO = 12288;
constexpr unsigned WS_G   = 16384;
constexpr unsigned WS_KVT = 1u << 20;
constexpr unsigned WS_Y2T = WS_KVT + (2u << 20);
constexpr unsigned WS_WDT = WS_Y2T + (2u << 20);
constexpr unsigned WS_H   = WS_WDT + (2u << 20);
constexpr unsigned WS_END = WS_H + 128u * 56 * 1024;

constexpr unsigned G1_XS = 16384, G1_WS = 28672, G1_XOFF = 0, G1_WOFF = 3 * G1_XS;
constexpr unsigned ST_K = 135168, ST_V = ST_K + 9216, ST_SH = ST_V + 8192, ST_WD = ST_SH + 1024, ST_T = 2 * G1_XS, ST_DUMMY = ST_WD + 8192;
constexpr unsigned CV_HS = 4160, CV_H = 4 * CV_HS, CV_X = 5120, CV_CH = CV_H + CV_X, CV_BASE = 106496, CV_RED = 0;
constexpr unsigned CV_Z = CV_BASE + 2 * CV_CH;
constexpr unsigned LDS_BYTES = ST_DUMMY + 1024;
static_assert(CV_Z + 2 * 1088 <= LDS_BYTES && LDS_BYTES <= 163840, "lds");

#define SB() __builtin_amdgcn_sched_barrier(0)
#define STAMP_DECL
#define STAMP(i)
#define DSTAMP(i)
#define STAMP_FLUSH(n)

DI unsigned rfl(unsigned x) { return __builtin_amdgcn_readfirstlane(x); }
DI rsrc_t mk_rsrc(const void* p, unsigned bytes) { return __builtin_amdgcn_make_buffer_rsrc((void*)p, 0, bytes, 0x00020000); }
DI unsigned cvtpk(float lo, float hi) { f32x2 v = {lo, hi}; bf16x2 b = __builtin_convertvector(v, bf16x2); return __builtin_bit_cast(unsigned, b); }
DI float bflo(unsigned x) { return __uint_as_float(x << 16); }
DI float bfhi(unsigned x) { return __uint_as_float(x & 0xffff0000u); }
DI void dma16(rsrc_t r, unsigned voff, unsigned soff, unsigned ldsaddr) {
  asm volatile("s_mov_b32 m0, %0\n\ts_nop 0\n\tbuffer_load_dwordx4 %1, %2, %3 offen lds" :: "s"(ldsaddr), "v"(voff), "s"(r), "s"(soff) : "m0", "memory");
}
DI void m0_set(unsigned v) { asm volatile("s_mov_b32 m0, %0" :: "s"(v) : "m0", "memory"); }
template <int OFF> DI void dma_go(rsrc_t r, unsigned voff, unsigned soff) {
  asm volatile("buffer_load_dwordx4 %0, %1, %2 offen offset:%3 lds" :: "v"(voff), "s"(r), "s"(soff), "i"(OFF) : "memory");
}
DI void touch(const char* a, unsigned lds_dummy) {
  asm volatile("s_mov_b32 m0, %0\n\ts_nop 0\n\tglobal_load_lds_dword %1, off" :: "s"(lds_dummy), "v"(a) : "m0", "memory");
}
DI void ld128_a(u32x4& d, rsrc_t r, unsigned voff, unsigned soff) { asm volatile("buffer_load_dwordx4 %0, %1, %2, %3 offen" : "=a"(d) : "v"(voff), "s"(r), "s"(soff) : "memory"); }
#define WAIT_LGKM0() __builtin_amdgcn_s_waitcnt(0xC07F)
#define WAIT_VM(n) asm volatile("s_waitcnt vmcnt(" #n ")" ::: "memory")
#define WAIT_VM_BAR(n) asm volatile("s_waitcnt vmcnt(" #n ") lgkmcnt(0)\n\ts_barrier" ::: "memory")
#define LGKM_BAR() asm volatile("s_waitcnt lgkmcnt(0)\n\ts_barrier" ::: "memory")
DI bf16x8 lds_b128(unsigned a) { return *(const LAS bf16x8*)a; }
DI u32x4 lds_u128(unsigned a) { return *(const LAS u32x4*)a; }
DI bf16x8 lds_tr2(unsigned a0, unsigned a1) {
  s16x4 lo = __builtin_amdgcn_ds_read_tr16_b64_v4i16((LAS s16x4*)a0);
  s16x4 hi = __builtin_amdgcn_ds_read_tr16_b64_v4i16((LAS s16x4*)a1);
  s16x8 v = __builtin_shufflevector(lo, hi, 0, 1, 2, 3, 4, 5, 6, 7);
  return __builtin_bit_cast(bf16x8, v);
}
DI void mfma_vvv(f32x4& c, const bf16x8& a, const bf16x8& b) { asm volatile("v_mfma_f32_16x16x32_bf16 %0, %1, %2, %0" : "+v"(c) : "v"(a), "v"(b)); }
DI void mfma_vvv0(f32x4& c, const bf16x8& a, const bf16x8& b) { asm volatile("v_mfma_f32_16x16x32_bf16 %0, %1, %2, 0" : "=&v"(c) : "v"(a), "v"(b)); }
DI void mfma_vav(f32x4& c, const u32x4& a, const u32x4& b) { asm volatile("v_mfma_f32_16x16x32_bf16 %0, %1, %2, %0" : "+v"(c) : "a"(a), "v"(b)); }
DI void mfma_vav0(f32x4& c, const u32x4& a, const u32x4& b) { asm volatile("v_mfma_f32_16x16x32_bf16 %0, %1, %2, 0" : "=&v"(c) : "a"(a), "v"(b)); }
#define MFMA_DRAIN() do { SB(); asm volatile("s_nop 7\n\ts_nop 3" ::: "memory"); SB(); } while (0)
constexpr int SC1 = 16;
DI u32x4 gld128(rsrc_t r, unsigned voff, unsigned soff) { return __builtin_amdgcn_raw_buffer_load_b128(r, voff, soff, SC1); }
DI unsigned gld32(rsrc_t r, unsigned voff, unsigned soff) { return __builtin_amdgcn_raw_buffer_load_b32(r, voff, soff, SC1); }
DI void gst128(u32x4 v, rsrc_t r, unsigned voff, unsigned soff) { __builtin_amdgcn_raw_buffer_store_b128(v, r, voff, soff, SC1); }
DI void gst64(u32x2 v, rsrc_t r, unsigned voff, unsigned soff) { __builtin_amdgcn_raw_buffer_store_b64(v, r, voff, soff, SC1); }
DI void gst32(unsigned v, rsrc_t r, unsigned voff, unsigned soff) { __builtin_amdgcn_raw_buffer_store_b32(v, r, voff, soff, SC1); }
#define RLX_AGENT __ATOMIC_RELAXED, __HIP_MEMORY_SCOPE_AGENT
DI u64 flag_ld(const char* a) { return __hip_atomic_load((const GAS u64*)a, RLX_AGENT); }
DI void flag_st(char* a, u64 v) { __hip_atomic_store((GAS u64*)a, v, RLX_AGENT); }
template <int N> DI void wait_flags(const char* base, u64 epoch, char* tmo, int lane) {
  for (unsigned spins = 0;; ++spins) {
    bool ok = true;
#pragma unroll
    for (int i = 0; i < (N + 63) / 64; ++i) { const int idx = i * 64 + lane; const u64 v = flag_ld(base + (idx < N ? idx : 0) * 8); ok &= (v == epoch); }
    if (__all(ok)) break;
    if (spins > 30000u) { if (lane == 0) flag_st(tmo, 1); break; }
    __builtin_amdgcn_s_sleep(1);
  }
  __builtin_amdgcn_fence(__ATOMIC_ACQUIRE, "wavefront");
}
DI f32x4 selv(unsigned m, f32x4 a, f32x4 b) {
  f32x4 r;
#pragma unroll
  for (int e = 0; e < 4; ++e) r[e] = __uint_as_float((__float_as_uint(a[e]) & m) | (__float_as_uint(b[e]) & ~m));
  return r;
}
DI unsigned gelu_mul2(f32x2 g, f32x2 u) {
  const f32x2 c1 = {-2.302208198f, -2.302208198f}, c2 = {-0.1029432397f, -0.1029432397f}, one = {1.f, 1.f};
  f32x2 a = g * __builtin_elementwise_fma(g * g, c2, c1);
  f32x2 e = {__builtin_amdgcn_exp2f(a[0]), __builtin_amdgcn_exp2f(a[1])};
  f32x2 d = e + one;
  f32x2 r = {__builtin_amdgcn_rcpf(d[0]), __builtin_amdgcn_rcpf(d[1])};
  f32x2 h = (g * u) * r;
  return cvtpk(h[0], h[1]);
}

DI float wave_sum(float a) {
  a += __builtin_bit_cast(float, __builtin_amdgcn_update_dpp(0, __builtin_bit_cast(int, a), 0xB1, 0xf, 0xf, true));
  a += __builtin_bit_cast(float, __builtin_amdgcn_update_dpp(0, __builtin_bit_cast(int, a), 0x4E, 0xf, 0xf, true));
  a += __builtin_bit_cast(float, __builtin_amdgcn_update_dpp(0, __builtin_bit_cast(int, a), 0x141, 0xf, 0xf, true));
  a += __builtin_bit_cast(float, __builtin_amdgcn_update_dpp(0, __builtin_bit_cast(int, a), 0x140, 0xf, 0xf, true));
  const int x = __builtin_bit_cast(int, a);
  return (__builtin_bit_cast(float, __builtin_amdgcn_readlane(x, 0)) + __builtin_bit_cast(float, __builtin_amdgcn_readlane(x, 16))) +
         (__builtin_bit_cast(float, __builtin_amdgcn_readlane(x, 32)) + __builtin_bit_cast(float, __builtin_amdgcn_readlane(x, 48)));
}
struct Params { const u16 *q, *k, *v, *shk, *hk, *Dv, *wg, *wu, *wd, *gw; u16* out; char* ws; };

struct G1 {
  rsrc_t xs, wsr;
  unsigned xv[4], wv[7];
  unsigned sx, sw;
  unsigned mx, mw;
  unsigned xm0, wm0, slot;
  unsigned dx, dw;
  unsigned xa[2], xb[2], wt, w6;
  f32x4 acc[3][2][4], acc6[2][2];
  bf16x8 X[2][4], W[2][8];
};
template <int N> DI void g1_go(G1& c) {
  if constexpr (N < 4) dma_go<N * 1024>(c.xs, c.xv[N], c.sx);
  else if constexpr (N < 8) dma_go<(N - 4) * 1024>(c.wsr, c.wv[N - 4], c.sw);
  else dma_go<(N - 8) * 1024>(c.wsr, c.wv[N - 4], c.sw);
}
template <int N> DI void g1_dma(G1& c, unsigned st, unsigned slot) {
  const unsigned m = N < 4 ? c.xm0 + slot * G1_XS : c.wm0 + slot * G1_WS + (N >= 8 ? 4096 : 0);
  asm volatile("s_mov_b32 m0, %0\n\ts_nop 0" :: "s"(m) : "m0", "memory");
  if constexpr (N < 4) dma_go<N * 1024>(c.xs, c.xv[N], c.sx + st * 128);
  else dma_go<(N < 8 ? N - 4 : N - 8) * 1024>(c.wsr, c.wv[N - 4], c.sw + st * (64 * 3584));
}
template <int... Ns> DI void g1_dma_all(G1& c, unsigned st, unsigned slot, iseq<Ns...>) { (g1_dma<Ns>(c, st, slot), ...); }
template <int KK, int R, bool ABL = true> DI void g1_read(G1& c) {
  constexpr int xi[12] = {0, -1, 1, 2, 3, -1, -1, -1, -1, -1, -1, -1};
  constexpr int wi[12] = {-1, 0, -1, -1, -1, 1, 2, 3, 4, 5, 6, 7};
  if constexpr (xi[R] >= 0) { constexpr int i = xi[R]; c.X[KK][i] = lds_b128((i < 2 ? c.xa[KK] : c.xb[KK]) + (i & 1) * 2048); }
  else {
    constexpr int i = wi[R], pr = i >> 1, gu = i & 1;
    constexpr unsigned o = KK * 32 * 224 + gu * 14336;
    if constexpr (pr < 3) c.W[KK][i] = lds_tr2(c.wt + o + pr * 32, c.wt + o + pr * 32 + 8 * 224);
    else c.W[KK][i] = lds_tr2(c.w6 + o, c.w6 + o + 8 * 224);
  }
}
template <int KK, int M, bool ZERO = false> DI void g1_mfma(G1& c) {
  if constexpr (M < 24) {
    constexpr int pr = M >> 3, gu = (M >> 2) & 1, st = M & 3;
    if constexpr (ZERO) mfma_vvv0(c.acc[pr][gu][st], c.W[KK][2 * pr + gu], c.X[KK][st]); else mfma_vvv(c.acc[pr][gu][st], c.W[KK][2 * pr + gu], c.X[KK][st]);
  } else {
    constexpr int gu = (M - 24) >> 1, st = M & 1;
    if constexpr (ZERO) mfma_vvv0(c.acc6[gu][st], c.W[KK][6 + gu], c.X[KK][st]); else mfma_vvv(c.acc6[gu][st], c.W[KK][6 + gu], c.X[KK][st]);
  }
}
struct Tail {
  rsrc_t hs, xr, wsr; unsigned hvoff, hm0, zm0, xvoff, wdso, xlds; const char* sflags; u64 epoch; char* tmo;
  u64 pf[4]; u32x4 xv[3];
  rsrc_t qs, gws; unsigned gso; u32x4 gx[2], gw;
  const char* touch67; unsigned dummy;
};
DI void poll_issue(u64& v, const char* a) { asm volatile("global_load_dwordx2 %0, %1, off sc1" : "=v"(v) : "v"(a) : "memory"); }
template <int I> DI void tail_op(Tail& t, u32x4 (&Wd)[14][4], int lane) {
  if constexpr (I < 4) dma_go<I * 1024>(t.hs, t.hvoff, 0);
  else if constexpr (I == 4) dma_go<0>(t.hs, lane * 16, 0);
  else if constexpr (I < 9) dma_go<(I - 5) * 1024>(t.hs, t.hvoff + 2, 0);
  else if constexpr (I < 11) ld128_a(t.gx[I - 9], t.qs, lane * 16, t.gso + (I - 9) * 1024);
  else if constexpr (I == 28) ld128_a(t.gw, t.gws, lane * 16, 0);
  else if constexpr (I < 14) t.xv[I - 11] = gld128(t.xr, t.xvoff + (I - 11) * 1024, 0);
  else { constexpr int n = I - 14; Wd[n >> 2][n & 3] = gld128(t.wsr, lane * 16 + (n & 3) * 1024, t.wdso + (n >> 2) * 4096); }
}
constexpr int TAIL_WD = 14;
constexpr int rd_idx0(int m) { constexpr int g[12] = {1, 2, 3, 4, 6, 7, 8, 9, 11, 12, 13, 16}; for (int i = 0; i < 12; ++i) if (g[i] == m) return i; return -1; }
constexpr int rd_idx1(int m) { constexpr int g[12] = {4, 5, 6, 7, 9, 10, 11, 12, 14, 15, 16, 17}; for (int i = 0; i < 12; ++i) if (g[i] == m) return i; return -1; }
constexpr int first_slot(int m) { constexpr int g[11] = {0, 2, 5, 7, 10, 12, 15, 17, 20, 22, 25}; for (int i = 0; i < 11; ++i) if (g[i] == m) return i; return -1; }
template <int MODE, int M> DI void g1_gap0(G1& c, Tail& t, u32x4 (&Wd)[14][4], int lane) {
  if constexpr (M == 0) {
    WAIT_LGKM0();
    if constexpr (MODE == 5) {
#pragma unroll
      for (int i = 0; i < 4; ++i) poll_issue(t.pf[i], t.sflags + (i * 64 + lane) * 8);
    }
  }
  g1_mfma<0, M, MODE == 9>(c);
  if constexpr (rd_idx0(M) >= 0) g1_read<1, rd_idx0(M)>(c);
  if constexpr (MODE == 9) {
    if constexpr (first_slot(M) >= 0) g1_go<first_slot(M)>(c);
    if constexpr (M == 8) c.sx += 128;
    if constexpr (M == 9) m0_set(c.mw);
    if constexpr (M == 19) m0_set(c.mw + 4096);
    if constexpr (M == 26) m0_set(t.dummy);
    if constexpr (M == 27) asm volatile("global_load_lds_dword %0, off" :: "v"(t.touch67) : "memory");
  } else if constexpr (M % 5 == 0) {
    if constexpr (MODE <= 5) g1_go<5 + M / 5>(c);
    if constexpr (MODE == 6) tail_op<5 + M / 5>(t, Wd, lane);
    if constexpr (MODE == 7) tail_op<16 + M / 5>(t, Wd, lane);
  }
  if constexpr (MODE == 6 && M == 27) tail_op<28>(t, Wd, lane);
  if constexpr (MODE <= 5 && M == 14) m0_set(c.mw + 4096);
  if constexpr ((MODE == 0 || MODE == 9) && M == 21) { c.mw = c.wm0 + c.slot * G1_WS; asm volatile("" : "+s"(c.mw)); }
  if constexpr ((MODE <= 5 || MODE == 9) && M == 25 + (MODE == 9 ? 2 : 1)) c.sw += 64 * 3584;
  if constexpr (MODE != 7) {
    if constexpr (M == 17) { c.xa[0] += c.dx; c.xb[0] += c.dx; }
    if constexpr (M == 18) { c.wt += c.dw; c.w6 += c.dw; }
    if constexpr (M == 19) { c.xa[1] += c.dx; c.xb[1] += c.dx; }
  }
  SB();
}
template <int MODE, int M> DI void g1_gap1(G1& c, Tail& t, u32x4 (&Wd)[14][4], int lane) {
  constexpr bool STEADY = MODE == 0 || MODE == 9;
  if constexpr (M == 0) WAIT_LGKM0();
  g1_mfma<1, M>(c);
  if constexpr (MODE != 7 && M == 2) {
    SB();
    if constexpr (MODE == 5) asm volatile("s_waitcnt vmcnt(15)\n\ts_barrier" ::: "memory");
    else if constexpr (MODE == 6) asm volatile("s_waitcnt vmcnt(12)\n\ts_barrier" : "+v"(t.pf[0]), "+v"(t.pf[1]), "+v"(t.pf[2]), "+v"(t.pf[3]) :: "memory");
    else if constexpr (MODE == 9) asm volatile("s_waitcnt vmcnt(12)\n\ts_barrier" ::: "memory");
    else asm volatile("s_waitcnt vmcnt(11)\n\ts_barrier" ::: "memory");
    if constexpr (MODE == 6) {
      const bool ok = t.pf[0] == t.epoch && t.pf[1] == t.epoch && t.pf[2] == t.epoch && t.pf[3] == t.epoch;
      if (!__all(ok)) wait_flags<256>(t.sflags, t.epoch, t.tmo, lane);
      __builtin_amdgcn_fence(__ATOMIC_ACQUIRE, "wavefront");
    }
    if constexpr (STEADY) m0_set(c.mx);
    if constexpr (MODE == 5) m0_set(t.hm0);
  }
  if constexpr (MODE != 7 && rd_idx1(M) >= 0) g1_read<0, rd_idx1(M)>(c);
  if constexpr (M % 5 == 3) {
    if constexpr (STEADY) g1_go<M / 5>(c);
    if constexpr (MODE == 5) tail_op<M / 5>(t, Wd, lane);
    if constexpr (MODE == 6) tail_op<11 + M / 5>(t, Wd, lane);
    if constexpr (MODE == 7) tail_op<22 + M / 5>(t, Wd, lane);
  }
  if constexpr (STEADY && M == 19) c.sx += 128;
  if constexpr (STEADY && M == 22) m0_set(c.mw);
  if constexpr (MODE == 5 && M == 22) m0_set(t.zm0);
  if constexpr (MODE == 5 && M == 27) m0_set(t.hm0 + CV_HS);
  if constexpr (MODE == 7 && M == 27) tail_op<27>(t, Wd, lane);
  if constexpr (MODE == 7 && M >= 10 && M < 13) *(LAS u32x4*)(t.xlds + (M - 10) * 1024) = t.xv[M - 10];
  if constexpr (STEADY && M == 24) {
    c.slot = c.slot == 2 ? 0 : c.slot + 1;
    asm volatile("" : "+s"(c.slot));
  }
  if constexpr (STEADY && M == 25) {
    c.dx = c.slot == 2 ? 0u - 2 * G1_XS : G1_XS; c.dw = c.slot == 2 ? 0u - 2 * G1_WS : G1_WS;
    asm volatile("" : "+s"(c.dx), "+s"(c.dw));
  }
  if constexpr (STEADY && M == 26) { c.mx = c.xm0 + c.slot * G1_XS; asm volatile("" : "+s"(c.mx)); }
  SB();
}
template <int MODE, int... Ms> DI void g1_stage(G1& c, Tail& t, u32x4 (&Wd)[14][4], int lane, iseq<Ms...>) { (g1_gap0<MODE, Ms>(c, t, Wd, lane), ...); (g1_gap1<MODE, Ms>(c, t, Wd, lane), ...); }
template <int... Rs> DI void g1_read0(G1& c, iseq<Rs...>) { (g1_read<0, Rs, false>(c), ...);
}

struct ConvCtx { unsigned abase, bbase, hm; u32x4 a0; const char* hfl; unsigned wdso; int lane; rsrc_t wsr; };
constexpr int cv_al(int m) { int al = 0, n = 8; while (m >= n) { m -= n; --n; ++al; } return al; }
constexpr int cv_be(int m) { int n = 8; while (m >= n) { m -= n; --n; } return m; }
template <int RR, int M> DI void conv_gap(const ConvCtx& cx, f32x4 (&acc)[8], u32x2 (&alo)[2][8], u32x2 (&ahi)[2][8], bf16x8 (&B)[2][8], u32x4 (&Wd)[14][4], u64 (&hflag)[2]) {
  constexpr int set = RR & 1, al = cv_al(M), be = cv_be(M);
  if constexpr (M == 0) WAIT_LGKM0();
  u32x4 av = {alo[set][al][0], alo[set][al][1], ahi[set][al][0], ahi[set][al][1]};
  if constexpr (RR == 0 && al == 0) {
#pragma unroll
    for (int e = 0; e < 4; ++e) av[e] = (av[e] & cx.hm) | (cx.a0[e] & ~cx.hm);
  }
  bf16x8 A = __builtin_bit_cast(bf16x8, av);
  if constexpr (RR == 0 && al == 0) { asm volatile("s_nop 1" : "+v"(A)); mfma_vvv0(acc[be], A, B[set][be]); }
  else mfma_vvv(acc[al + be], A, B[set][be]);
  if constexpr (RR == 2 && M == 21) hflag[0] = flag_ld(cx.hfl);
  if constexpr (RR == 3 && M == 11) hflag[1] = flag_ld(cx.hfl);
  if constexpr (M % 5 == 3) {
    constexpr int n = TAIL_WD + 14 + RR * 7 + M / 5;
    if constexpr (n < 56) Wd[n >> 2][n & 3] = gld128(cx.wsr, cx.lane * 16 + (n & 3) * 1024, cx.wdso + (n >> 2) * 4096);

  } else if constexpr (RR < 3) {
    constexpr int rd = M - (M + 1) / 5;
    if constexpr (rd < 24) {
      constexpr int i = rd / 3, k = rd % 3;
      if constexpr (k == 0) alo[set ^ 1][i] = *(const volatile LAS u32x2*)(cx.abase + (RR + 1) * 64 + i * 512);
      else if constexpr (k == 1) ahi[set ^ 1][i] = *(const volatile LAS u32x2*)(cx.abase + (RR + 1) * 64 + i * 512 + 8);
      else B[set ^ 1][i] = lds_b128(cx.bbase + 64 * (56 - 8 * i) + 64 * (RR + 1));
    }
  }
  SB();
}
template <int RR, int... Ms> DI void conv_rho(const ConvCtx& cx, f32x4 (&acc)[8], u32x2 (&alo)[2][8], u32x2 (&ahi)[2][8], bf16x8 (&B)[2][8], u32x4 (&Wd)[14][4], u64 (&hflag)[2], iseq<Ms...>) {
  (conv_gap<RR, Ms>(cx, acc, alo, ahi, B, Wd, hflag), ...);
}

__global__ __launch_bounds__(256, 1) void k_fused(Params p) {
  __shared__ __attribute__((aligned(16))) char smem[LDS_BYTES];
  const unsigned lds0 = (unsigned)(size_t)(LAS char*)smem;
  const int lane = threadIdx.x & 63;
  const unsigned w = rfl(threadIdx.x >> 6);
  const unsigned b = blockIdx.x, xcd = b & 7, jb = b >> 3;
  char* const ws = p.ws;
  STAMP_DECL; STAMP(0);
  asm volatile("" :: "s"(p.q), "s"(p.k), "s"(p.v), "s"(p.shk), "s"(p.hk), "s"(p.Dv), "s"(p.wg), "s"(p.wu), "s"(p.wd), "s"(p.gw), "s"(p.out), "s"(p.ws));
  const rsrc_t wsr = mk_rsrc(ws, WS_END);
  u64 eword;
  asm volatile("global_load_dwordx2 %0, %1, off" : "=v"(eword) : "v"(ws + WS_E) : "memory");

  auto touch_addr = [&](unsigned st0, unsigned n) -> const char* {
    const unsigned sl = (lane * 4370u) >> 16, l15 = lane - sl * 15;
    const unsigned st = st0 + (sl < n ? sl : n - 1), idx = jb * 60 + w * 15 + l15;
    const unsigned i2 = idx - 1024, k = (i2 * 4682u) >> 16, rem = i2 - k * 14;
    const bool isx = idx < 1024, isu = rem >= 7;
    const unsigned offx = ((xcd >> 2) * 1024 + idx) * 1024 + st * 128, offw = (k + st * 64) * 3584 + (xcd & 3) * 896 + (isu ? rem - 7 : rem) * 128;
    const u64 bq = (u64)p.q, bg = (u64)p.wg, bu = (u64)p.wu;
    const u64 base = isx ? bq : (isu ? bu : bg);
    const char* a = (const char*)(base + (isx ? offx : offw));
    return a;
  };
  const unsigned sb = b >> 3, db = b & 7, s0p = sb * 64, d0p = db * 64;
  const unsigned kg_w = b >> 2, cq_w = b & 3;
  {
    const unsigned r8 = lane >> 3, c8 = lane & 7;
    const rsrc_t rk = mk_rsrc(p.k, S_ * D_ * 2), rv = mk_rsrc(p.v, S_ * D_ * 2), rw = mk_rsrc(p.wd, F_ * D_ * 2);
    auto dk = [&](int i) { int row = (int)s0p - 3 + 8 * i + (int)r8; unsigned vo = row >= 0 ? (unsigned)row * 1024 + d0p * 2 + c8 * 16 : 0x80000000u; dma16(rk, vo, 0, lds0 + ST_K + i * 1024); };
    auto dv = [&](int i) { dma16(rv, r8 * 1024 + c8 * 16, (s0p + 8 * i) * 1024 + d0p * 2, lds0 + ST_V + i * 1024); };
    auto dw = [&](int i) { dma16(rw, (lane >> 4) * 1024 + (lane & 15) * 16, (kg_w * 32 + 4 * i) * 1024 + cq_w * 256, lds0 + ST_WD + i * 1024); };
    if (w == 0) { dk(0); dk(1); dk(2); dk(3); dk(4); dk(5); dk(6); }
    else if (w == 1) { dk(7); dk(8); dv(0); dv(1); dv(2); dv(3); dv(4); }
    else if (w == 2) { dv(5); dv(6); dv(7); dma16(mk_rsrc(p.shk, D_ * 8), lane * 16, d0p * 8, lds0 + ST_SH); dw(0); dw(1); dw(2); }
    else { dw(3); dw(4); dw(5); dw(6); dw(7); }
  }
  SB();
  const unsigned rb = 8 * (xcd >> 2) + (jb & 7), fc = 4 * (xcd & 3) + (jb >> 3);
  const unsigned mi = w >> 1, ni = w & 1;
  G1 c;
  c.xs = mk_rsrc(p.q, S_ * D_ * 2);
  c.wsr = mk_rsrc((w >> 1) ? p.wu : p.wg, D_ * F_ * 2);
#pragma unroll
  for (int n = 0; n < 4; ++n) c.xv[n] = (lane >> 3) * 1024 + (((lane & 7) ^ ((4 * (n & 1) + (lane >> 4)) & 7)) << 4) + n * (8192 - 1024);
#pragma unroll
  for (int jj = 0; jj < 7; ++jj) {
    unsigned ci = 64 * (7 * (w & 1) + jj) + lane, rho = (ci * 4682u) >> 16, pc = ci - rho * 14;
    unsigned k = (rho & ~12u) | ((rho & 4) << 1) | ((rho & 8) >> 1);
    c.wv[jj] = k * 3584 + pc * 16 - (jj & 3) * 1024;
  }
  c.sx = (rb * 128 + 32 * w) * 1024;
  c.sw = fc * 224;
  c.xm0 = lds0 + G1_XOFF + w * 4096;
  c.wm0 = lds0 + G1_WOFF + (w >> 1) * 14336 + (w & 1) * 7 * 1024;
  g1_dma_all(c, 0, 0, make_iseq<11>{});
  {
    const int li = lane & 15, gq = lane >> 4;
#pragma unroll
    for (int kk = 0; kk < 2; ++kk) {
      unsigned bb = li * 128 + (((4 * kk + gq) ^ ((li >> 1) & 7)) << 4);
      c.xa[kk] = lds0 + G1_XOFF + (4 * mi + 2 * ni) * 2048 + bb;
      c.xb[kk] = lds0 + G1_XOFF + (4 * mi + 2 * (1 - ni)) * 2048 + bb;
    }
    const int q = li >> 2, pp = lane & 3;
    unsigned wb = lds0 + G1_WOFF + (16 * (gq >> 1) + 4 * (gq & 1) + q) * 224 + pp * 8;
    c.wt = wb + ni * 96; c.w6 = wb + 6 * 32;
  }
  SB();
  STAMP(1);
  asm volatile("s_waitcnt vmcnt(11)\n\ts_barrier" : "+v"(eword) :: "memory");
  const u64 epoch = ((u64)rfl((unsigned)(eword >> 32)) << 32 | rfl((unsigned)eword)) * 6364136223846793005ull + 1442695040888963407ull;
  STAMP(2);
  {
    const int t = threadIdx.x, c4 = t & 15, rg = t >> 4;
    u32x2 kr[7], vr[4]; u32x4 w4[2];
#pragma unroll
    for (int i = 0; i < 7; ++i) kr[i] = *(const LAS u32x2*)(lds0 + ST_K + (4 * rg + i) * 128 + c4 * 8);
#pragma unroll
    for (int i = 0; i < 4; ++i) vr[i] = *(const LAS u32x2*)(lds0 + ST_V + (4 * rg + i) * 128 + c4 * 8);
#pragma unroll
    for (int i = 0; i < 2; ++i) w4[i] = lds_u128(lds0 + ST_SH + c4 * 32 + i * 16);
    const int li = lane & 15, gq = lane >> 4;
    bf16x8 wf[2];
#pragma unroll
    for (int i = 0; i < 2; ++i) { const unsigned a = lds0 + ST_WD + (8 * gq + (li >> 2)) * 256 + (2 * w + i) * 32 + (lane & 3) * 8; wf[i] = lds_tr2(a, a + 4 * 256); }
    g1_dma<0>(c, 1, 1); g1_dma<1>(c, 1, 1);
    SB();
#pragma unroll
    for (int j = 0; j < 4; ++j) {
      const unsigned wa = w4[j >> 1][(j & 1) * 2], wb = w4[j >> 1][(j & 1) * 2 + 1];
      const float w0 = bflo(wa), w1 = bfhi(wa), w2 = bflo(wb), w3 = bfhi(wb);
      float kk[7], o[4];
#pragma unroll
      for (int i = 0; i < 7; ++i) kk[i] = (j & 1) ? bfhi(kr[i][j >> 1]) : bflo(kr[i][j >> 1]);
      if (j == 0) g1_dma<2>(c, 1, 1); if (j == 1) g1_dma<4>(c, 1, 1); if (j == 2) g1_dma<6>(c, 1, 1); if (j == 3) g1_dma<8>(c, 1, 1);
      SB();
#pragma unroll
      for (int i = 0; i < 4; ++i) {
        const float vv = (j & 1) ? bfhi(vr[i][j >> 1]) : bflo(vr[i][j >> 1]);
        o[i] = (kk[i] * w3 + kk[i + 1] * w2 + kk[i + 2] * w1 + kk[i + 3] * w0) * vv;
      }
      const int wd = 30 - 2 * rg, dl = 4 * c4 + j;
      *(LAS u32x2*)(lds0 + ST_T + dl * 128 + ((((wd >> 2) ^ c4) & 7) << 4) + (wd & 3) * 4) = u32x2{cvtpk(o[3], o[2]), cvtpk(o[1], o[0])};
      if (j == 0) g1_dma<3>(c, 1, 1); if (j == 1) g1_dma<5>(c, 1, 1); if (j == 2) g1_dma<7>(c, 1, 1); if (j == 3) g1_dma<9>(c, 1, 1);
      SB();
    }
    g1_dma<10>(c, 1, 1);
    touch(touch_addr(2, 4), lds0 + ST_DUMMY);
    SB();
    LGKM_BAR();
    u32x4 val[2];
#pragma unroll
    for (int i = 0; i < 2; ++i) { const int dl = (w * 2 + i) * 8 + (lane >> 3), ch = lane & 7; val[i] = lds_u128(lds0 + ST_T + dl * 128 + ((ch ^ (dl >> 2)) & 7) * 16); }
#pragma unroll
    for (int i = 0; i < 2; ++i) {
      const unsigned D16 = 8 * cq_w + 2 * w + i;
      gst128(__builtin_bit_cast(u32x4, wf[i]), wsr, kg_w < 56 ? WS_WDT + (((D16 >> 2) * 56 + kg_w) * 4 + (D16 & 3)) * 1024 + lane * 16 : 0x80000000u, 0);
    }
#pragma unroll
    for (int i = 0; i < 2; ++i) {
      const int dl = (w * 2 + i) * 8 + (lane >> 3), ch = lane & 7;
      gst128(val[i], wsr, WS_KVT + (d0p + dl) * 4096 + (1984 - s0p) * 2 + ch * 16, 0);
    }
    SB();
  }
  STAMP(3);
  asm volatile("s_waitcnt vmcnt(16) lgkmcnt(0)\n\ts_barrier" ::: "memory");
  g1_read0(c, make_iseq<12>{});
  c.sx += 2 * 128; c.sw += 2 * (64 * 3584);
  c.slot = 0; c.mx = c.xm0; c.mw = c.wm0 + 2 * G1_WS; c.dx = G1_XS; c.dw = G1_WS;
  m0_set(c.xm0 + 2 * G1_XS);
  SB();
  STAMP(4);
  const unsigned cch = w >> 1, hf = w & 1, dch = b * 2 + cch, cvb = lds0 + CV_BASE + cch * CV_CH, cvz = lds0 + CV_Z + cch * 1088;
  const unsigned rb2 = 4 * xcd + (jb & 3), cb = jb >> 2;
  u32x4 Wd[14][4];
  Tail t;
  t.hs = mk_rsrc(p.hk + (size_t)dch * S_, (D_ - dch) * S_ * 2);
  t.hvoff = lane * 16 + (2 * hf + 17) * 2; t.hm0 = cvb + 2 * hf * CV_HS; t.zm0 = cvz + 32;
  t.xr = mk_rsrc(ws + WS_KVT + dch * 4096, 4096); t.xvoff = (hf * 128 + lane) * 16; t.xlds = cvb + CV_H + (hf * 128 + lane) * 16;
  t.wsr = wsr; t.wdso = WS_WDT + (cb * 56 + 14 * w) * 4096;
  t.sflags = ws + WS_FS; t.epoch = epoch; t.tmo = ws + WS_TMO;
  const unsigned grow = 128 * rb + 8 * fc + 2 * w;
  t.qs = c.xs; t.gws = mk_rsrc(p.gw, D_ * 2); t.gso = grow * 1024;
  t.touch67 = touch_addr(6, 2); t.dummy = lds0 + ST_DUMMY;
  {
    DSTAMP(0);
    g1_stage<9>(c, t, Wd, lane, make_iseq<28>{});
    if (threadIdx.x == 0) flag_st(ws + WS_FS + b * 8, epoch);
#pragma unroll 1
    for (unsigned s = 1; s < 5; ++s) {
      DSTAMP(s);
      g1_stage<0>(c, t, Wd, lane, make_iseq<28>{});
    }
    DSTAMP(5);
    g1_stage<5>(c, t, Wd, lane, make_iseq<28>{});
    if (lane < 8) *(LAS unsigned*)(cvz + lane * 4) = 0;
    c.dx = G1_XS; c.dw = G1_WS;
    DSTAMP(6);
    g1_stage<6>(c, t, Wd, lane, make_iseq<28>{});
    if (b == 0 && threadIdx.x == 0) flag_st(ws + WS_E, epoch);
    DSTAMP(7);
    g1_stage<7>(c, t, Wd, lane, make_iseq<28>{});
    DSTAMP(8);
  }
  MFMA_DRAIN();
  STAMP(5);
  LGKM_BAR();
  auto ldWd = [&](int n) { Wd[n >> 2][n & 3] = gld128(wsr, lane * 16 + (n & 3) * 1024, t.wdso + (n >> 2) * 4096); };
  {
    asm volatile("" : "+a"(t.gx[0]), "+a"(t.gx[1]), "+a"(t.gw));
    float gsum[2];
#pragma unroll
    for (int r = 0; r < 2; ++r) {
      float a = 0.f;
#pragma unroll
      for (int j = 0; j < 4; ++j) a += bflo(t.gx[r][j]) * bflo(t.gw[j]) + bfhi(t.gx[r][j]) * bfhi(t.gw[j]);
      a = wave_sum(a);
      gsum[r] = __builtin_amdgcn_rcpf(1.f + __builtin_amdgcn_exp2f(-1.4426950409f * a));
    }
    gst32(__float_as_uint(lane & 1 ? gsum[1] : gsum[0]), wsr, lane < 2 ? WS_G + (grow + lane) * 4 : 0x80000000u, 0);
    SB();
  }
  {
    const int li = lane & 15, gq = lane >> 4;
    const unsigned lo = ((gq >> 1) * 16 + li) * 16 + (gq & 1) * 8;
    const unsigned stA = rb * 8 + 4 * mi + 2 * ni, stB = rb * 8 + 4 * mi + 2 * (1 - ni);
    auto put = [&](int u, const f32x4& g, const f32x4& uu, unsigned stg, unsigned ft) {
      ldWd(TAIL_WD + u);
      u32x2 o = {gelu_mul2(f32x2{g[0], g[1]}, f32x2{uu[0], uu[1]}), gelu_mul2(f32x2{g[2], g[3]}, f32x2{uu[2], uu[3]})};
      gst64(o, wsr, lo, WS_H + (stg * 56 + (ft >> 1)) * 1024 + (ft & 1) * 512);
      SB();
    };
#pragma unroll
    for (int pr = 0; pr < 3; ++pr)
#pragma unroll
      for (int st = 0; st < 4; ++st) put(pr * 4 + st, c.acc[pr][0][st], c.acc[pr][1][st], (st < 2 ? stA : stB) + (st & 1), fc * 7 + 3 * ni + pr);
#pragma unroll
    for (int st = 0; st < 2; ++st) put(12 + st, c.acc6[0][st], c.acc6[1][st], stA + st, fc * 7 + 6);
  }
  STAMP(6);

  constexpr int PD = 4;
  u32x4 H[PD + 1][4];
  const unsigned hbase = WS_H + ((4 * rb2) * 56 + 14 * w) * 1024;
  auto ldH = [&](int i) {
#pragma unroll
    for (int st = 0; st < 4; ++st) H[i % (PD + 1)][st] = gld128(wsr, lane * 16, hbase + (st * 56 + i) * 1024);
  };
  {
    const int li = lane & 15, gq = lane >> 4;
    u32x4 a0;
#pragma unroll
    for (int e = 0; e < 4; ++e) {
      const unsigned o = cvz + (li + 8 * gq + 2 * e + 1) * 2;
      a0[e] = (unsigned)*(const LAS u16*)o | ((unsigned)*(const LAS u16*)(o + 2) << 16);
    }
    const float Dd = __uint_as_float((unsigned)p.Dv[dch] << 16);
    const unsigned abase = cvb + (li & 3) * CV_HS + (li >> 2) * 8 + gq * 16 + hf * 4 * 64 - 64;
    const unsigned bbase = cvb + CV_H + (254 - 2 * li + gq) * 16 - 64 * 56 + hf * 4 * 64;
    f32x4 acc[8];
    u32x2 alo[2][8], ahi[2][8];
    bf16x8 B[2][8];
    auto rdA = [&](int set, int rr, int al) {
      alo[set][al] = *(const volatile LAS u32x2*)(abase + rr * 64 + al * 512);
      ahi[set][al] = *(const volatile LAS u32x2*)(abase + rr * 64 + al * 512 + 8);
    };
    auto rdB = [&](int set, int rr, int be) { B[set][be] = lds_b128(bbase + 64 * (56 - 8 * be) + 64 * rr); };
#pragma unroll
    for (int i = 0; i < 8; ++i) { rdA(0, 0, i); rdB(0, 0, i); }
    SB();
    STAMP(7);
    u64 hflag[2] = {0, 0};
    ConvCtx cx{abase, bbase, 0u - hf, a0, ws + WS_FH + (rb2 >> 1) * 128 + li * 8, t.wdso, lane, wsr};
    DSTAMP(9);
    conv_rho<0>(cx, acc, alo, ahi, B, Wd, hflag, make_iseq<36>{});
    DSTAMP(10);
    WAIT_VM(7); asm volatile("s_barrier" ::: "memory");
    if (threadIdx.x == 0) flag_st(ws + WS_FH + rb * 128 + fc * 8, epoch);
    SB();
    DSTAMP(11);
    conv_rho<1>(cx, acc, alo, ahi, B, Wd, hflag, make_iseq<36>{});
    conv_rho<2>(cx, acc, alo, ahi, B, Wd, hflag, make_iseq<36>{});
    conv_rho<3>(cx, acc, alo, ahi, B, Wd, hflag, make_iseq<36>{});
    DSTAMP(12);
    MFMA_DRAIN();
    STAMP(8);
    if (!__all(hflag[0] == epoch)) { if (!__all(hflag[1] == epoch)) wait_flags<16>(ws + WS_FH + (rb2 >> 1) * 128, epoch, ws + WS_TMO, lane); }
    __builtin_amdgcn_fence(__ATOMIC_ACQUIRE, "wavefront");
    DSTAMP(13);
    touch(ws + WS_H + (4 * rb2) * 57344 + (cb * 224 + w * 56 + (lane < 56 ? lane : 55)) * 128, lds0 + ST_DUMMY);
    const unsigned red = lds0 + CV_RED, hm = 0u - hf;
#pragma unroll
    for (int g = 0; g < 4; ++g) *(LAS f32x4*)(red + w * 4096 + g * 1024 + lane * 16) = selv(hm, acc[g], acc[4 + g]);
    SB();
    ldH(0);
    SB();
    DSTAMP(14);
    LGKM_BAR();
    DSTAMP(15);
    f32x4 oth[4]; u32x2 kvr[4];
#pragma unroll
    for (int g = 0; g < 4; ++g) {
      oth[g] = *(const LAS f32x4*)(red + (w ^ 1) * 4096 + g * 1024 + lane * 16);
      kvr[g] = *(const LAS u32x2*)(cvb + CV_H + (2044 - (256 * (g + 4 * hf) + 16 * li + 4 * gq)) * 2);
    }
    SB();
    ldH(1);
    SB();
    u32x2 yo[4];
#pragma unroll
    for (int g = 0; g < 4; ++g) {
      f32x4 y = selv(hm, acc[4 + g], acc[g]) + oth[g];
      float r0 = y[0] + bfhi(kvr[g][1]) * Dd, r1 = y[1] + bflo(kvr[g][1]) * Dd, r2 = y[2] + bfhi(kvr[g][0]) * Dd, r3 = y[3] + bflo(kvr[g][0]) * Dd;
      yo[g] = u32x2{cvtpk(r0, r1), cvtpk(r2, r3)};
    }
    SB();
    ldH(2);
#pragma unroll
    for (int g = 0; g < 4; ++g) gst64(yo[g], wsr, (256 * (g + 4 * hf) + 16 * li + 4 * gq) * 2, WS_Y2T + dch * 4096);
    SB();
    ldH(3);
    SB();
  }
  STAMP(9);

  const int tt = threadIdx.x, cc = tt & 7, sp = tt >> 3;
  const unsigned s0 = rb2 * 64, d0 = cb * 64;
  unsigned yv[8]; u32x2 gt; u32x4 qv[2];
  {
    f32x4 acc[4][4];
    u64 yflag = 0;
#pragma unroll
    for (int i = 0; i < 14; ++i) {
      DSTAMP(16 + i);
      if (i + PD < 14) ldH(i + PD);
      if (i == 8) yflag = flag_ld(ws + WS_FY + cb * 256 + (lane & 31) * 8);
      if (i == 10) {
        if (!__all(yflag == epoch)) wait_flags<32>(ws + WS_FY + cb * 256, epoch, ws + WS_TMO, lane);
        __builtin_amdgcn_fence(__ATOMIC_ACQUIRE, "wavefront");
#pragma unroll
        for (int e = 0; e < 8; ++e) yv[e] = gld32(wsr, (8 * cc + e) * 4096 + sp * 4, WS_Y2T + d0 * 4096 + s0 * 2);
      }
      if (i == 11) {
        gt = __builtin_amdgcn_raw_buffer_load_b64(wsr, sp * 8, WS_G + s0 * 4, SC1);
#pragma unroll
        for (int r = 0; r < 2; ++r) qv[r] = *(const u32x4*)(p.q + (size_t)(s0 + 2 * sp + r) * D_ + d0 + cc * 8);
      }
      SB();
#pragma unroll
      for (int dt = 0; dt < 4; ++dt)
#pragma unroll
        for (int st = 0; st < 4; ++st) {
          if (i == 0) mfma_vav0(acc[dt][st], Wd[i][dt], H[i % (PD + 1)][st]); else mfma_vav(acc[dt][st], Wd[i][dt], H[i % (PD + 1)][st]);
        }
      SB();
      if (i == 3) {
        WAIT_VM(16); asm volatile("s_barrier" ::: "memory");
        if (threadIdx.x == 0) flag_st(ws + WS_FY + (b >> 5) * 256 + (b & 31) * 8, epoch);
        SB();
      }
    }
    DSTAMP(30);
    MFMA_DRAIN();
    STAMP(10);
    const int li = lane & 15, gq = lane >> 4;
#pragma unroll
    for (int dt = 0; dt < 4; ++dt)
#pragma unroll
      for (int st = 0; st < 4; ++st) {
        const int s = 16 * st + li, chunk = 4 * dt + gq;
        *(LAS f32x4*)(lds0 + w * 16384 + s * 256 + ((chunk ^ li) << 4)) = acc[dt][st];
      }
  }
  LGKM_BAR();
  {
#pragma unroll
    for (int r = 0; r < 2; ++r) {
      const int s = 2 * sp + r;
      f32x4 b0 = {0, 0, 0, 0}, b1 = {0, 0, 0, 0};
#pragma unroll
      for (int ww = 0; ww < 4; ++ww) {
        b0 += *(const LAS f32x4*)(lds0 + ww * 16384 + s * 256 + (((2 * cc) ^ (s & 15)) << 4));
        b1 += *(const LAS f32x4*)(lds0 + ww * 16384 + s * 256 + (((2 * cc + 1) ^ (s & 15)) << 4));
      }
      const float g = __uint_as_float(gt[r]);
      u32x4 o;
#pragma unroll
      for (int e = 0; e < 4; ++e) {
        float bl = e < 2 ? b0[2 * e] : b1[2 * e - 4], bh = e < 2 ? b0[2 * e + 1] : b1[2 * e - 3];
        float yl = r ? bfhi(yv[2 * e]) : bflo(yv[2 * e]), yh = r ? bfhi(yv[2 * e + 1]) : bflo(yv[2 * e + 1]);
        float al = bflo(qv[r][e]) * yl, ah = bfhi(qv[r][e]) * yh;
        o[e] = cvtpk(g * al + (1.f - g) * bl, g * ah + (1.f - g) * bh);
      }
      *(u32x4*)(p.out + (size_t)(s0 + s) * D_ + d0 + cc * 8) = o;
    }
  }
  STAMP(11);
  STAMP_FLUSH(12);
}

extern "C" void kernel_launch(void* const* d_in, const int* in_sizes, int n_in, void* d_out, int out_size, void* d_ws, size_t ws_size, hipStream_t stream) {
  Params p;
  p.q = (const u16*)d_in[0]; p.k = (const u16*)d_in[1]; p.v = (const u16*)d_in[2];
  p.shk = (const u16*)d_in[3]; p.hk = (const u16*)d_in[4]; p.Dv = (const u16*)d_in[5];
  p.wg = (const u16*)d_in[7]; p.wu = (const u16*)d_in[8]; p.wd = (const u16*)d_in[9]; p.gw = (const u16*)d_in[10];
  p.out = (u16*)d_out; p.ws = (char*)d_ws;
  k_fused<<<256, 256, 0, stream>>>(p);
}
```

```cpp
#include <hip/hip_runtime.h>
#include <hip/hip_bf16.h>
#include <utility>
#define NO_TOUCH
#define NO_HTOUCH
#define PDV 3
#pragma clang diagnostic ignored "-Winline-asm"
#pragma clang diagnostic ignored "-Wint-to-pointer-cast"

#define DI __device__ __forceinline__
#define LAS __attribute__((address_space(3)))
#define GAS __attribute__((address_space(1)))
typedef unsigned short u16;
typedef unsigned long long u64;
typedef float f32x4 __attribute__((ext_vector_type(4)));
typedef float f32x2 __attribute__((ext_vector_type(2)));
typedef __bf16 bf16x8 __attribute__((ext_vector_type(8)));
typedef __bf16 bf16x2 __attribute__((ext_vector_type(2)));
typedef short s16x4 __attribute__((ext_vector_type(4)));
typedef short s16x8 __attribute__((ext_vector_type(8)));
typedef unsigned u32x4 __attribute__((ext_vector_type(4)));
typedef unsigned u32x2 __attribute__((ext_vector_type(2)));
typedef __amdgpu_buffer_rsrc_t rsrc_t;
template <int... Is> using iseq = std::integer_sequence<int, Is...>;
template <int N> using make_iseq = std::make_integer_sequence<int, N>;

constexpr int S_ = 2048, D_ = 512, F_ = 1792;
constexpr unsigned WS_E = 0, WS_FS = 256, WS_FH = 4096, WS_FY = 8192, WS_TMO = 12288;
constexpr unsigned WS_G   = 16384;
constexpr unsigned WS_KVT = 1u << 20;
constexpr unsigned WS_Y2T = WS_KVT + (2u << 20);
constexpr unsigned WS_WDT = WS_Y2T + (2u << 20);
constexpr unsigned WS_H   = WS_WDT + (2u << 20);
constexpr unsigned WS_END = WS_H + 128u * 56 * 1024;

constexpr unsigned G1_XS = 16384, G1_WS = 28672, G1_XOFF = 0, G1_WOFF = 3 * G1_XS;
constexpr unsigned ST_K = 135168, ST_V = ST_K + 9216, ST_SH = ST_V + 8192, ST_WD = ST_SH + 1024, ST_T = 2 * G1_XS, ST_DUMMY = ST_WD + 8192;
constexpr unsigned CV_HS = 4160, CV_H = 4 * CV_HS, CV_X = 5120, CV_CH = CV_H + CV_X, CV_BASE = 106496, CV_RED = 0;
constexpr unsigned CV_Z = CV_BASE + 2 * CV_CH;
constexpr unsigned LDS_BYTES = ST_DUMMY + 1024;
static_assert(CV_Z + 2 * 1088 <= LDS_BYTES && LDS_BYTES <= 163840, "lds");

#define SB() __builtin_amdgcn_sched_barrier(0)
#define STAMP_DECL
#define STAMP(i)
#define DSTAMP(i)
#define STAMP_FLUSH(n)

DI unsigned rfl(unsigned x) { return __builtin_amdgcn_readfirstlane(x); }
DI rsrc_t mk_rsrc(const void* p, unsigned bytes) { return __builtin_amdgcn_make_buffer_rsrc((void*)p, 0, bytes, 0x00020000); }
DI unsigned cvtpk(float lo, float hi) { f32x2 v = {lo, hi}; bf16x2 b = __builtin_convertvector(v, bf16x2); return __builtin_bit_cast(unsigned, b); }
DI float bflo(unsigned x) { return __uint_as_float(x << 16); }
DI float bfhi(unsigned x) { return __uint_as_float(x & 0xffff0000u); }
DI void dma16(rsrc_t r, unsigned voff, unsigned soff, unsigned ldsaddr) {
  asm volatile("s_mov_b32 m0, %0\n\ts_nop 0\n\tbuffer_load_dwordx4 %1, %2, %3 offen lds" :: "s"(ldsaddr), "v"(voff), "s"(r), "s"(soff) : "m0", "memory");
}
DI void m0_set(unsigned v) { asm volatile("s_mov_b32 m0, %0" :: "s"(v) : "m0", "memory"); }
template <int OFF> DI void dma_go(rsrc_t r, unsigned voff, unsigned soff) {
  asm volatile("buffer_load_dwordx4 %0, %1, %2 offen offset:%3 lds" :: "v"(voff), "s"(r), "s"(soff), "i"(OFF) : "memory");
}
DI void touch(const char* a, unsigned lds_dummy) {
  asm volatile("s_mov_b32 m0, %0\n\ts_nop 0\n\tglobal_load_lds_dword %1, off" :: "s"(lds_dummy), "v"(a) : "m0", "memory");
}
DI void ld128_a(u32x4& d, rsrc_t r, unsigned voff, unsigned soff) { asm volatile("buffer_load_dwordx4 %0, %1, %2, %3 offen" : "=a"(d) : "v"(voff), "s"(r), "s"(soff) : "memory"); }
#define WAIT_LGKM0() __builtin_amdgcn_s_waitcnt(0xC07F)
#define WAIT_VM(n) asm volatile("s_waitcnt vmcnt(" #n ")" ::: "memory")
#define WAIT_VM_BAR(n) asm volatile("s_waitcnt vmcnt(" #n ") lgkmcnt(0)\n\ts_barrier" ::: "memory")
#define LGKM_BAR() asm volatile("s_waitcnt lgkmcnt(0)\n\ts_barrier" ::: "memory")
DI bf16x8 lds_b128(unsigned a) { return *(const LAS bf16x8*)a; }
DI u32x4 lds_u128(unsigned a) { return *(const LAS u32x4*)a; }
DI bf16x8 lds_tr2(unsigned a0, unsigned a1) {
  s16x4 lo = __builtin_amdgcn_ds_read_tr16_b64_v4i16((LAS s16x4*)a0);
  s16x4 hi = __builtin_amdgcn_ds_read_tr16_b64_v4i16((LAS s16x4*)a1);
  s16x8 v = __builtin_shufflevector(lo, hi, 0, 1, 2, 3, 4, 5, 6, 7);
  return __builtin_bit_cast(bf16x8, v);
}
DI void mfma_vvv(f32x4& c, const bf16x8& a, const bf16x8& b) { asm volatile("v_mfma_f32_16x16x32_bf16 %0, %1, %2, %0" : "+v"(c) : "v"(a), "v"(b)); }
DI void mfma_vvv0(f32x4& c, const bf16x8& a, const bf16x8& b) { asm volatile("v_mfma_f32_16x16x32_bf16 %0, %1, %2, 0" : "=&v"(c) : "v"(a), "v"(b)); }
DI void mfma_vav(f32x4& c, const u32x4& a, const u32x4& b) { asm volatile("v_mfma_f32_16x16x32_bf16 %0, %1, %2, %0" : "+v"(c) : "a"(a), "v"(b)); }
DI void mfma_vav0(f32x4& c, const u32x4& a, const u32x4& b) { asm volatile("v_mfma_f32_16x16x32_bf16 %0, %1, %2, 0" : "=&v"(c) : "a"(a), "v"(b)); }
#define MFMA_DRAIN() do { SB(); asm volatile("s_nop 7\n\ts_nop 3" ::: "memory"); SB(); } while (0)
constexpr int SC1 = 16;
DI u32x4 gld128(rsrc_t r, unsigned voff, unsigned soff) { return __builtin_amdgcn_raw_buffer_load_b128(r, voff, soff, SC1); }
DI unsigned gld32(rsrc_t r, unsigned voff, unsigned soff) { return __builtin_amdgcn_raw_buffer_load_b32(r, voff, soff, SC1); }
DI void gst128(u32x4 v, rsrc_t r, unsigned voff, unsigned soff) { __builtin_amdgcn_raw_buffer_store_b128(v, r, voff, soff, SC1); }
DI void gst64(u32x2 v, rsrc_t r, unsigned voff, unsigned soff) { __builtin_amdgcn_raw_buffer_store_b64(v, r, voff, soff, SC1); }
DI void gst32(unsigned v, rsrc_t r, unsigned voff, unsigned soff) { __builtin_amdgcn_raw_buffer_store_b32(v, r, voff, soff, SC1); }
#define RLX_AGENT __ATOMIC_RELAXED, __HIP_MEMORY_SCOPE_AGENT
DI u64 flag_ld(const char* a) { return __hip_atomic_load((const GAS u64*)a, RLX_AGENT); }
DI void flag_st(char* a, u64 v) { __hip_atomic_store((GAS u64*)a, v, RLX_AGENT); }
template <int N> DI void wait_flags(const char* base, u64 epoch, char* tmo, int lane) {
  for (unsigned spins = 0;; ++spins) {
    bool ok = true;
#pragma unroll
    for (int i = 0; i < (N + 63) / 64; ++i) { const int idx = i * 64 + lane; const u64 v = flag_ld(base + (idx < N ? idx : 0) * 8); ok &= (v == epoch); }
    if (__all(ok)) break;
    if (spins > 30000u) { if (lane == 0) flag_st(tmo, 1); break; }
    __builtin_amdgcn_s_sleep(1);
  }
  __builtin_amdgcn_fence(__ATOMIC_ACQUIRE, "wavefront");
}
DI f32x4 selv(unsigned m, f32x4 a, f32x4 b) {
  f32x4 r;
#pragma unroll
  for (int e = 0; e < 4; ++e) r[e] = __uint_as_float((__float_as_uint(a[e]) & m) | (__float_as_uint(b[e]) & ~m));
  return r;
}
DI unsigned gelu_mul2(f32x2 g, f32x2 u) {
  const f32x2 c1 = {-2.302208198f, -2.302208198f}, c2 = {-0.1029432397f, -0.1029432397f}, one = {1.f, 1.f};
  f32x2 a = g * __builtin_elementwise_fma(g * g, c2, c1);
  f32x2 e = {__builtin_amdgcn_exp2f(a[0]), __builtin_amdgcn_exp2f(a[1])};
  f32x2 d = e + one;
  f32x2 r = {__builtin_amdgcn_rcpf(d[0]), __builtin_amdgcn_rcpf(d[1])};
  f32x2 h = (g * u) * r;
  return cvtpk(h[0], h[1]);
}

DI float wave_sum(float a) {
  a += __builtin_bit_cast(float, __builtin_amdgcn_update_dpp(0, __builtin_bit_cast(int, a), 0xB1, 0xf, 0xf, true));
  a += __builtin_bit_cast(float, __builtin_amdgcn_update_dpp(0, __builtin_bit_cast(int, a), 0x4E, 0xf, 0xf, true));
  a += __builtin_bit_cast(float, __builtin_amdgcn_update_dpp(0, __builtin_bit_cast(int, a), 0x141, 0xf, 0xf, true));
  a += __builtin_bit_cast(float, __builtin_amdgcn_update_dpp(0, __builtin_bit_cast(int, a), 0x140, 0xf, 0xf, true));
  const int x = __builtin_bit_cast(int, a);
  return (__builtin_bit_cast(float, __builtin_amdgcn_readlane(x, 0)) + __builtin_bit_cast(float, __builtin_amdgcn_readlane(x, 16))) +
         (__builtin_bit_cast(float, __builtin_amdgcn_readlane(x, 32)) + __builtin_bit_cast(float, __builtin_amdgcn_readlane(x, 48)));
}
struct Params { const u16 *q, *k, *v, *shk, *hk, *Dv, *wg, *wu, *wd, *gw; u16* out; char* ws; };

struct G1 {
  rsrc_t xs, wsr;
  unsigned xv[4], wv[7];
  unsigned sx, sw;
  unsigned mx, mw;
  unsigned xm0, wm0, slot;
  unsigned dx, dw;
  unsigned xa[2], xb[2], wt, w6;
  f32x4 acc[3][2][4], acc6[2][2];
  bf16x8 X[2][4], W[2][8];
};
template <int N> DI void g1_go(G1& c) {
  if constexpr (N < 4) dma_go<N * 1024>(c.xs, c.xv[N], c.sx);
  else if constexpr (N < 8) dma_go<(N - 4) * 1024>(c.wsr, c.wv[N - 4], c.sw);
  else dma_go<(N - 8) * 1024>(c.wsr, c.wv[N - 4], c.sw);
}
template <int N> DI void g1_dma(G1& c, unsigned st, unsigned slot) {
  const unsigned m = N < 4 ? c.xm0 + slot * G1_XS : c.wm0 + slot * G1_WS + (N >= 8 ? 4096 : 0);
  asm volatile("s_mov_b32 m0, %0\n\ts_nop 0" :: "s"(m) : "m0", "memory");
  if constexpr (N < 4) dma_go<N * 1024>(c.xs, c.xv[N], c.sx + st * 128);
  else dma_go<(N < 8 ? N - 4 : N - 8) * 1024>(c.wsr, c.wv[N - 4], c.sw + st * (64 * 3584));
}
template <int... Ns> DI void g1_dma_all(G1& c, unsigned st, unsigned slot, iseq<Ns...>) { (g1_dma<Ns>(c, st, slot), ...); }
template <int KK, int R, bool ABL = true> DI void g1_read(G1& c) {
  constexpr int xi[12] = {0, -1, 1, 2, 3, -1, -1, -1, -1, -1, -1, -1};
  constexpr int wi[12] = {-1, 0, -1, -1, -1, 1, 2, 3, 4, 5, 6, 7};
  if constexpr (xi[R] >= 0) { constexpr int i = xi[R]; c.X[KK][i] = lds_b128((i < 2 ? c.xa[KK] : c.xb[KK]) + (i & 1) * 2048); }
  else {
    constexpr int i = wi[R], pr = i >> 1, gu = i & 1;
    constexpr unsigned o = KK * 32 * 224 + gu * 14336;
    if constexpr (pr < 3) c.W[KK][i] = lds_tr2(c.wt + o + pr * 32, c.wt + o + pr * 32 + 8 * 224);
    else c.W[KK][i] = lds_tr2(c.w6 + o, c.w6 + o + 8 * 224);
  }
}
template <int KK, int M, bool ZERO = false> DI void g1_mfma(G1& c) {
  if constexpr (M < 24) {
    constexpr int pr = M >> 3, gu = (M >> 2) & 1, st = M & 3;
    if constexpr (ZERO) mfma_vvv0(c.acc[pr][gu][st], c.W[KK][2 * pr + gu], c.X[KK][st]); else mfma_vvv(c.acc[pr][gu][st], c.W[KK][2 * pr + gu], c.X[KK][st]);
  } else {
    constexpr int gu = (M - 24) >> 1, st = M & 1;
    if constexpr (ZERO) mfma_vvv0(c.acc6[gu][st], c.W[KK][6 + gu], c.X[KK][st]); else mfma_vvv(c.acc6[gu][st], c.W[KK][6 + gu], c.X[KK][st]);
  }
}
struct Tail {
  rsrc_t hs, xr, wsr; unsigned hvoff, hm0, zm0, xvoff, wdso, xlds; const char* sflags; u64 epoch; char* tmo;
  u64 pf[4]; u32x4 xv[3];
  rsrc_t qs, gws; unsigned gso; u32x4 gx[2], gw;
  const char* touch67; unsigned dummy;
};
DI void poll_issue(u64& v, const char* a) { asm volatile("global_load_dwordx2 %0, %1, off sc1" : "=v"(v) : "v"(a) : "memory"); }
template <int I> DI void tail_op(Tail& t, u32x4 (&Wd)[14][4], int lane) {
  if constexpr (I < 4) dma_go<I * 1024>(t.hs, t.hvoff, 0);
  else if constexpr (I == 4) dma_go<0>(t.hs, lane * 16, 0);
  else if constexpr (I < 9) dma_go<(I - 5) * 1024>(t.hs, t.hvoff + 2, 0);
  else if constexpr (I < 11) ld128_a(t.gx[I - 9], t.qs, lane * 16, t.gso + (I - 9) * 1024);
  else if constexpr (I == 28) ld128_a(t.gw, t.gws, lane * 16, 0);
  else if constexpr (I < 14) t.xv[I - 11] = gld128(t.xr, t.xvoff + (I - 11) * 1024, 0);
  else { constexpr int n = I - 14; Wd[n >> 2][n & 3] = gld128(t.wsr, lane * 16 + (n & 3) * 1024, t.wdso + (n >> 2) * 4096); }
}
constexpr int TAIL_WD = 14;
constexpr int rd_idx0(int m) { constexpr int g[12] = {1, 2, 3, 4, 6, 7, 8, 9, 11, 12, 13, 16}; for (int i = 0; i < 12; ++i) if (g[i] == m) return i; return -1; }
constexpr int rd_idx1(int m) { constexpr int g[12] = {4, 5, 6, 7, 9, 10, 11, 12, 14, 15, 16, 17}; for (int i = 0; i < 12; ++i) if (g[i] == m) return i; return -1; }
constexpr int first_slot(int m) { constexpr int g[11] = {0, 2, 5, 7, 10, 12, 15, 17, 20, 22, 25}; for (int i = 0; i < 11; ++i) if (g[i] == m) return i; return -1; }
template <int MODE, int M> DI void g1_gap0(G1& c, Tail& t, u32x4 (&Wd)[14][4], int lane) {
  if constexpr (M == 0) {
    WAIT_LGKM0();
    if constexpr (MODE == 5) {
#pragma unroll
      for (int i = 0; i < 4; ++i) poll_issue(t.pf[i], t.sflags + (i * 64 + lane) * 8);
    }
  }
  g1_mfma<0, M, MODE == 9>(c);
  if constexpr (rd_idx0(M) >= 0) g1_read<1, rd_idx0(M)>(c);
  if constexpr (MODE == 9) {
    if constexpr (first_slot(M) >= 0) g1_go<first_slot(M)>(c);
    if constexpr (M == 8) c.sx += 128;
    if constexpr (M == 9) m0_set(c.mw);
    if constexpr (M == 19) m0_set(c.mw + 4096);
  } else if constexpr (M % 5 == 0) {
    if constexpr (MODE <= 5) g1_go<5 + M / 5>(c);
    if constexpr (MODE == 6) tail_op<5 + M / 5>(t, Wd, lane);
    if constexpr (MODE == 7) tail_op<16 + M / 5>(t, Wd, lane);
  }
  if constexpr (MODE == 6 && M == 27) tail_op<28>(t, Wd, lane);
  if constexpr (MODE <= 5 && M == 14) m0_set(c.mw + 4096);
  if constexpr ((MODE == 0 || MODE == 9) && M == 21) { c.mw = c.wm0 + c.slot * G1_WS; asm volatile("" : "+s"(c.mw)); }
  if constexpr ((MODE <= 5 || MODE == 9) && M == 25 + (MODE == 9 ? 2 : 1)) c.sw += 64 * 3584;
  if constexpr (MODE != 7) {
    if constexpr (M == 17) { c.xa[0] += c.dx; c.xb[0] += c.dx; }
    if constexpr (M == 18) { c.wt += c.dw; c.w6 += c.dw; }
    if constexpr (M == 19) { c.xa[1] += c.dx; c.xb[1] += c.dx; }
  }
  SB();
}
template <int MODE, int M> DI void g1_gap1(G1& c, Tail& t, u32x4 (&Wd)[14][4], int lane) {
  constexpr bool STEADY = MODE == 0 || MODE == 9;
  if constexpr (M == 0) WAIT_LGKM0();
  g1_mfma<1, M>(c);
  if constexpr (MODE != 7 && M == 2) {
    SB();
    if constexpr (MODE == 5) asm volatile("s_waitcnt vmcnt(15)\n\ts_barrier" ::: "memory");
    else if constexpr (MODE == 6) asm volatile("s_waitcnt vmcnt(12)\n\ts_barrier" : "+v"(t.pf[0]), "+v"(t.pf[1]), "+v"(t.pf[2]), "+v"(t.pf[3]) :: "memory");
    else asm volatile("s_waitcnt vmcnt(11)\n\ts_barrier" ::: "memory");
    if constexpr (MODE == 6) {
      const bool ok = t.pf[0] == t.epoch && t.pf[1] == t.epoch && t.pf[2] == t.epoch && t.pf[3] == t.epoch;
      if (!__all(ok)) wait_flags<256>(t.sflags, t.epoch, t.tmo, lane);
      __builtin_amdgcn_fence(__ATOMIC_ACQUIRE, "wavefront");
    }
    if constexpr (STEADY) m0_set(c.mx);
    if constexpr (MODE == 5) m0_set(t.hm0);
  }
  if constexpr (MODE != 7 && rd_idx1(M) >= 0) g1_read<0, rd_idx1(M)>(c);
  if constexpr (M % 5 == 3) {
    if constexpr (STEADY) g1_go<M / 5>(c);
    if constexpr (MODE == 5) tail_op<M / 5>(t, Wd, lane);
    if constexpr (MODE == 6) tail_op<11 + M / 5>(t, Wd, lane);
    if constexpr (MODE == 7) tail_op<22 + M / 5>(t, Wd, lane);
  }
  if constexpr (STEADY && M == 19) c.sx += 128;
  if constexpr (STEADY && M == 22) m0_set(c.mw);
  if constexpr (MODE == 5 && M == 22) m0_set(t.zm0);
  if constexpr (MODE == 5 && M == 27) m0_set(t.hm0 + CV_HS);
  if constexpr (MODE == 7 && M == 27) tail_op<27>(t, Wd, lane);
  if constexpr (MODE == 7 && M >= 10 && M < 13) *(LAS u32x4*)(t.xlds + (M - 10) * 1024) = t.xv[M - 10];
  if constexpr (STEADY && M == 24) {
    c.slot = c.slot == 2 ? 0 : c.slot + 1;
    asm volatile("" : "+s"(c.slot));
  }
  if constexpr (STEADY && M == 25) {
    c.dx = c.slot == 2 ? 0u - 2 * G1_XS : G1_XS; c.dw = c.slot == 2 ? 0u - 2 * G1_WS : G1_WS;
    asm volatile("" : "+s"(c.dx), "+s"(c.dw));
  }
  if constexpr (STEADY && M == 26) { c.mx = c.xm0 + c.slot * G1_XS; asm volatile("" : "+s"(c.mx)); }
  SB();
}
template <int MODE, int... Ms> DI void g1_stage(G1& c, Tail& t, u32x4 (&Wd)[14][4], int lane, iseq<Ms...>) { (g1_gap0<MODE, Ms>(c, t, Wd, lane), ...); (g1_gap1<MODE, Ms>(c, t, Wd, lane), ...); }
template <int... Rs> DI void g1_read0(G1& c, iseq<Rs...>) { (g1_read<0, Rs, false>(c), ...);
}

struct ConvCtx { unsigned abase, bbase, hm; u32x4 a0; const char* hfl; unsigned wdso; int lane; rsrc_t wsr; };
constexpr int cv_al(int m) { int al = 0, n = 8; while (m >= n) { m -= n; --n; ++al; } return al; }
constexpr int cv_be(int m) { int n = 8; while (m >= n) { m -= n; --n; } return m; }
template <int RR, int M> DI void conv_gap(const ConvCtx& cx, f32x4 (&acc)[8], u32x2 (&alo)[2][8], u32x2 (&ahi)[2][8], bf16x8 (&B)[2][8], u32x4 (&Wd)[14][4], u64 (&hflag)[2]) {
  constexpr int set = RR & 1, al = cv_al(M), be = cv_be(M);
  if constexpr (M == 0) WAIT_LGKM0();
  u32x4 av = {alo[set][al][0], alo[set][al][1], ahi[set][al][0], ahi[set][al][1]};
  if constexpr (RR == 0 && al == 0) {
#pragma unroll
    for (int e = 0; e < 4; ++e) av[e] = (av[e] & cx.hm) | (cx.a0[e] & ~cx.hm);
  }
  bf16x8 A = __builtin_bit_cast(bf16x8, av);
  if constexpr (RR == 0 && al == 0) { asm volatile("s_nop 1" : "+v"(A)); mfma_vvv0(acc[be], A, B[set][be]); }
  else mfma_vvv(acc[al + be], A, B[set][be]);
  if constexpr (RR == 2 && M == 21) hflag[0] = flag_ld(cx.hfl);
  if constexpr (RR == 3 && M == 11) hflag[1] = flag_ld(cx.hfl);
  if constexpr (M % 5 == 3) {
    constexpr int n = TAIL_WD + 14 + RR * 7 + M / 5;
    if constexpr (n < 56) Wd[n >> 2][n & 3] = gld128(cx.wsr, cx.lane * 16 + (n & 3) * 1024, cx.wdso + (n >> 2) * 4096);

  } else if constexpr (RR < 3) {
    constexpr int rd = M - (M + 1) / 5;
    if constexpr (rd < 24) {
      constexpr int i = rd / 3, k = rd % 3;
      if constexpr (k == 0) alo[set ^ 1][i] = *(const volatile LAS u32x2*)(cx.abase + (RR + 1) * 64 + i * 512);
      else if constexpr (k == 1) ahi[set ^ 1][i] = *(const volatile LAS u32x2*)(cx.abase + (RR + 1) * 64 + i * 512 + 8);
      else B[set ^ 1][i] = lds_b128(cx.bbase + 64 * (56 - 8 * i) + 64 * (RR + 1));
    }
  }
  SB();
}
template <int RR, int... Ms> DI void conv_rho(const ConvCtx& cx, f32x4 (&acc)[8], u32x2 (&alo)[2][8], u32x2 (&ahi)[2][8], bf16x8 (&B)[2][8], u32x4 (&Wd)[14][4], u64 (&hflag)[2], iseq<Ms...>) {
  (conv_gap<RR, Ms>(cx, acc, alo, ahi, B, Wd, hflag), ...);
}

__global__ __launch_bounds__(256, 1) void k_fused(Params p) {
  __shared__ __attribute__((aligned(16))) char smem[LDS_BYTES];
  const unsigned lds0 = (unsigned)(size_t)(LAS char*)smem;
  const int lane = threadIdx.x & 63;
  const unsigned w = rfl(threadIdx.x >> 6);
  const unsigned b = blockIdx.x, xcd = b & 7, jb = b >> 3;
  char* const ws = p.ws;
  STAMP_DECL; STAMP(0);
  asm volatile("" :: "s"(p.q), "s"(p.k), "s"(p.v), "s"(p.shk), "s"(p.hk), "s"(p.Dv), "s"(p.wg), "s"(p.wu), "s"(p.wd), "s"(p.gw), "s"(p.out), "s"(p.ws));
  const rsrc_t wsr = mk_rsrc(ws, WS_END);
  u64 eword;
  asm volatile("global_load_dwordx2 %0, %1, off" : "=v"(eword) : "v"(ws + WS_E) : "memory");

  auto touch_addr = [&](unsigned st0, unsigned n) -> const char* {
    const unsigned sl = (lane * 4370u) >> 16, l15 = lane - sl * 15;
    const unsigned st = st0 + (sl < n ? sl : n - 1), idx = jb * 60 + w * 15 + l15;
    const unsigned i2 = idx - 1024, k = (i2 * 4682u) >> 16, rem = i2 - k * 14;
    const bool isx = idx < 1024, isu = rem >= 7;
    const unsigned offx = ((xcd >> 2) * 1024 + idx) * 1024 + st * 128, offw = (k + st * 64) * 3584 + (xcd & 3) * 896 + (isu ? rem - 7 : rem) * 128;
    const u64 bq = (u64)p.q, bg = (u64)p.wg, bu = (u64)p.wu;
    const u64 base = isx ? bq : (isu ? bu : bg);
    const char* a = (const char*)(base + (isx ? offx : offw));
    return a;
  };
  const unsigned sb = b >> 3, db = b & 7, s0p = sb * 64, d0p = db * 64;
  const unsigned kg_w = b >> 2, cq_w = b & 3;
  {
    const unsigned r8 = lane >> 3, c8 = lane & 7;
    const rsrc_t rk = mk_rsrc(p.k, S_ * D_ * 2), rv = mk_rsrc(p.v, S_ * D_ * 2), rw = mk_rsrc(p.wd, F_ * D_ * 2);
    auto dk = [&](int i) { int row = (int)s0p - 3 + 8 * i + (int)r8; unsigned vo = row >= 0 ? (unsigned)row * 1024 + d0p * 2 + c8 * 16 : 0x80000000u; dma16(rk, vo, 0, lds0 + ST_K + i * 1024); };
    auto dv = [&](int i) { dma16(rv, r8 * 1024 + c8 * 16, (s0p + 8 * i) * 1024 + d0p * 2, lds0 + ST_V + i * 1024); };
    auto dw = [&](int i) { dma16(rw, (lane >> 4) * 1024 + (lane & 15) * 16, (kg_w * 32 + 4 * i) * 1024 + cq_w * 256, lds0 + ST_WD + i * 1024); };
    if (w == 0) { dk(0); dk(1); dk(2); dk(3); dk(4); dk(5); dk(6); }
    else if (w == 1) { dk(7); dk(8); dv(0); dv(1); dv(2); dv(3); dv(4); }
    else if (w == 2) { dv(5); dv(6); dv(7); dma16(mk_rsrc(p.shk, D_ * 8), lane * 16, d0p * 8, lds0 + ST_SH); dw(0); dw(1); dw(2); }
    else { dw(3); dw(4); dw(5); dw(6); dw(7); }
  }
  SB();
  const unsigned rb = 8 * (xcd >> 2) + (jb & 7), fc = 4 * (xcd & 3) + (jb >> 3);
  const unsigned mi = w >> 1, ni = w & 1;
  G1 c;
  c.xs = mk_rsrc(p.q, S_ * D_ * 2);
  c.wsr = mk_rsrc((w >> 1) ? p.wu : p.wg, D_ * F_ * 2);
#pragma unroll
  for (int n = 0; n < 4; ++n) c.xv[n] = (lane >> 3) * 1024 + (((lane & 7) ^ ((4 * (n & 1) + (lane >> 4)) & 7)) << 4) + n * (8192 - 1024);
#pragma unroll
  for (int jj = 0; jj < 7; ++jj) {
    unsigned ci = 64 * (7 * (w & 1) + jj) + lane, rho = (ci * 4682u) >> 16, pc = ci - rho * 14;
    unsigned k = (rho & ~12u) | ((rho & 4) << 1) | ((rho & 8) >> 1);
    c.wv[jj] = k * 3584 + pc * 16 - (jj & 3) * 1024;
  }
  c.sx = (rb * 128 + 32 * w) * 1024;
  c.sw = fc * 224;
  c.xm0 = lds0 + G1_XOFF + w * 4096;
  c.wm0 = lds0 + G1_WOFF + (w >> 1) * 14336 + (w & 1) * 7 * 1024;
  g1_dma_all(c, 0, 0, make_iseq<11>{});
  {
    const int li = lane & 15, gq = lane >> 4;
#pragma unroll
    for (int kk = 0; kk < 2; ++kk) {
      unsigned bb = li * 128 + (((4 * kk + gq) ^ ((li >> 1) & 7)) << 4);
      c.xa[kk] = lds0 + G1_XOFF + (4 * mi + 2 * ni) * 2048 + bb;
      c.xb[kk] = lds0 + G1_XOFF + (4 * mi + 2 * (1 - ni)) * 2048 + bb;
    }
    const int q = li >> 2, pp = lane & 3;
    unsigned wb = lds0 + G1_WOFF + (16 * (gq >> 1) + 4 * (gq & 1) + q) * 224 + pp * 8;
    c.wt = wb + ni * 96; c.w6 = wb + 6 * 32;
  }
  SB();
  STAMP(1);
  asm volatile("s_waitcnt vmcnt(11)\n\ts_barrier" : "+v"(eword) :: "memory");
  const u64 epoch = ((u64)rfl((unsigned)(eword >> 32)) << 32 | rfl((unsigned)eword)) * 6364136223846793005ull + 1442695040888963407ull;
  STAMP(2);
  {
    const int t = threadIdx.x, c4 = t & 15, rg = t >> 4;
    u32x2 kr[7], vr[4]; u32x4 w4[2];
#pragma unroll
    for (int i = 0; i < 7; ++i) kr[i] = *(const LAS u32x2*)(lds0 + ST_K + (4 * rg + i) * 128 + c4 * 8);
#pragma unroll
    for (int i = 0; i < 4; ++i) vr[i] = *(const LAS u32x2*)(lds0 + ST_V + (4 * rg + i) * 128 + c4 * 8);
#pragma unroll
    for (int i = 0; i < 2; ++i) w4[i] = lds_u128(lds0 + ST_SH + c4 * 32 + i * 16);
    const int li = lane & 15, gq = lane >> 4;
    bf16x8 wf[2];
#pragma unroll
    for (int i = 0; i < 2; ++i) { const unsigned a = lds0 + ST_WD + (8 * gq + (li >> 2)) * 256 + (2 * w + i) * 32 + (lane & 3) * 8; wf[i] = lds_tr2(a, a + 4 * 256); }
    g1_dma<0>(c, 1, 1); g1_dma<1>(c, 1, 1);
    SB();
#pragma unroll
    for (int j = 0; j < 4; ++j) {
      const unsigned wa = w4[j >> 1][(j & 1) * 2], wb = w4[j >> 1][(j & 1) * 2 + 1];
      const float w0 = bflo(wa), w1 = bfhi(wa), w2 = bflo(wb), w3 = bfhi(wb);
      float kk[7], o[4];
#pragma unroll
      for (int i = 0; i < 7; ++i) kk[i] = (j & 1) ? bfhi(kr[i][j >> 1]) : bflo(kr[i][j >> 1]);
      if (j == 0) g1_dma<2>(c, 1, 1); if (j == 1) g1_dma<4>(c, 1, 1); if (j == 2) g1_dma<6>(c, 1, 1); if (j == 3) g1_dma<8>(c, 1, 1);
      SB();
#pragma unroll
      for (int i = 0; i < 4; ++i) {
        const float vv = (j & 1) ? bfhi(vr[i][j >> 1]) : bflo(vr[i][j >> 1]);
        o[i] = (kk[i] * w3 + kk[i + 1] * w2 + kk[i + 2] * w1 + kk[i + 3] * w0) * vv;
      }
      const int wd = 30 - 2 * rg, dl = 4 * c4 + j;
      *(LAS u32x2*)(lds0 + ST_T + dl * 128 + ((((wd >> 2) ^ c4) & 7) << 4) + (wd & 3) * 4) = u32x2{cvtpk(o[3], o[2]), cvtpk(o[1], o[0])};
      if (j == 0) g1_dma<3>(c, 1, 1); if (j == 1) g1_dma<5>(c, 1, 1); if (j == 2) g1_dma<7>(c, 1, 1); if (j == 3) g1_dma<9>(c, 1, 1);
      SB();
    }
    g1_dma<10>(c, 1, 1);
    SB();
    LGKM_BAR();
    u32x4 val[2];
#pragma unroll
    for (int i = 0; i < 2; ++i) { const int dl = (w * 2 + i) * 8 + (lane >> 3), ch = lane & 7; val[i] = lds_u128(lds0 + ST_T + dl * 128 + ((ch ^ (dl >> 2)) & 7) * 16); }
#pragma unroll
    for (int i = 0; i < 2; ++i) {
      const unsigned D16 = 8 * cq_w + 2 * w + i;
      gst128(__builtin_bit_cast(u32x4, wf[i]), wsr, kg_w < 56 ? WS_WDT + (((D16 >> 2) * 56 + kg_w) * 4 + (D16 & 3)) * 1024 + lane * 16 : 0x80000000u, 0);
    }
#pragma unroll
    for (int i = 0; i < 2; ++i) {
      const int dl = (w * 2 + i) * 8 + (lane >> 3), ch = lane & 7;
      gst128(val[i], wsr, WS_KVT + (d0p + dl) * 4096 + (1984 - s0p) * 2 + ch * 16, 0);
    }
    SB();
  }
  STAMP(3);
  asm volatile("s_waitcnt vmcnt(15) lgkmcnt(0)\n\ts_barrier" ::: "memory");
  g1_read0(c, make_iseq<12>{});
  c.sx += 2 * 128; c.sw += 2 * (64 * 3584);
  c.slot = 0; c.mx = c.xm0; c.mw = c.wm0 + 2 * G1_WS; c.dx = G1_XS; c.dw = G1_WS;
  m0_set(c.xm0 + 2 * G1_XS);
  SB();
  STAMP(4);
  const unsigned cch = w >> 1, hf = w & 1, dch = b * 2 + cch, cvb = lds0 + CV_BASE + cch * CV_CH, cvz = lds0 + CV_Z + cch * 1088;
  const unsigned rb2 = 4 * xcd + (jb & 3), cb = jb >> 2;
  u32x4 Wd[14][4];
  Tail t;
  t.hs = mk_rsrc(p.hk + (size_t)dch * S_, (D_ - dch) * S_ * 2);
  t.hvoff = lane * 16 + (2 * hf + 17) * 2; t.hm0 = cvb + 2 * hf * CV_HS; t.zm0 = cvz + 32;
  t.xr = mk_rsrc(ws + WS_KVT + dch * 4096, 4096); t.xvoff = (hf * 128 + lane) * 16; t.xlds = cvb + CV_H + (hf * 128 + lane) * 16;
  t.wsr = wsr; t.wdso = WS_WDT + (cb * 56 + 14 * w) * 4096;
  t.sflags = ws + WS_FS; t.epoch = epoch; t.tmo = ws + WS_TMO;
  const unsigned grow = 128 * rb + 8 * fc + 2 * w;
  t.qs = c.xs; t.gws = mk_rsrc(p.gw, D_ * 2); t.gso = grow * 1024;
  t.touch67 = touch_addr(6, 2); t.dummy = lds0 + ST_DUMMY;
  {
    DSTAMP(0);
    g1_stage<9>(c, t, Wd, lane, make_iseq<28>{});
    if (threadIdx.x == 0) flag_st(ws + WS_FS + b * 8, epoch);
#pragma unroll 1
    for (unsigned s = 1; s < 5; ++s) {
      DSTAMP(s);
      g1_stage<0>(c, t, Wd, lane, make_iseq<28>{});
    }
    DSTAMP(5);
    g1_stage<5>(c, t, Wd, lane, make_iseq<28>{});
    if (lane < 8) *(LAS unsigned*)(cvz + lane * 4) = 0;
    c.dx = G1_XS; c.dw = G1_WS;
    DSTAMP(6);
    g1_stage<6>(c, t, Wd, lane, make_iseq<28>{});
    if (b == 0 && threadIdx.x == 0) flag_st(ws + WS_E, epoch);
    DSTAMP(7);
    g1_stage<7>(c, t, Wd, lane, make_iseq<28>{});
    DSTAMP(8);
  }
  MFMA_DRAIN();
  STAMP(5);
  LGKM_BAR();
  auto ldWd = [&](int n) { Wd[n >> 2][n & 3] = gld128(wsr, lane * 16 + (n & 3) * 1024, t.wdso + (n >> 2) * 4096); };
  {
    asm volatile("" : "+a"(t.gx[0]), "+a"(t.gx[1]), "+a"(t.gw));
    float gsum[2];
#pragma unroll
    for (int r = 0; r < 2; ++r) {
      float a = 0.f;
#pragma unroll
      for (int j = 0; j < 4; ++j) a += bflo(t.gx[r][j]) * bflo(t.gw[j]) + bfhi(t.gx[r][j]) * bfhi(t.gw[j]);
      a = wave_sum(a);
      gsum[r] = __builtin_amdgcn_rcpf(1.f + __builtin_amdgcn_exp2f(-1.4426950409f * a));
    }
    gst32(__float_as_uint(lane & 1 ? gsum[1] : gsum[0]), wsr, lane < 2 ? WS_G + (grow + lane) * 4 : 0x80000000u, 0);
    SB();
  }
  {
    const int li = lane & 15, gq = lane >> 4;
    const unsigned lo = ((gq >> 1) * 16 + li) * 16 + (gq & 1) * 8;
    const unsigned stA = rb * 8 + 4 * mi + 2 * ni, stB = rb * 8 + 4 * mi + 2 * (1 - ni);
    auto put = [&](int u, const f32x4& g, const f32x4& uu, unsigned stg, unsigned ft) {
      ldWd(TAIL_WD + u);
      u32x2 o = {gelu_mul2(f32x2{g[0], g[1]}, f32x2{uu[0], uu[1]}), gelu_mul2(f32x2{g[2], g[3]}, f32x2{uu[2], uu[3]})};
      gst64(o, wsr, lo, WS_H + (stg * 56 + (ft >> 1)) * 1024 + (ft & 1) * 512);
      SB();
    };
#pragma unroll
    for (int pr = 0; pr < 3; ++pr)
#pragma unroll
      for (int st = 0; st < 4; ++st) put(pr * 4 + st, c.acc[pr][0][st], c.acc[pr][1][st], (st < 2 ? stA : stB) + (st & 1), fc * 7 + 3 * ni + pr);
#pragma unroll
    for (int st = 0; st < 2; ++st) put(12 + st, c.acc6[0][st], c.acc6[1][st], stA + st, fc * 7 + 6);
  }
  STAMP(6);

  constexpr int PD = PDV;
  u32x4 H[PD + 1][4];
  const unsigned hbase = WS_H + ((4 * rb2) * 56 + 14 * w) * 1024;
  auto ldH = [&](int i) {
#pragma unroll
    for (int st = 0; st < 4; ++st) H[i % (PD + 1)][st] = gld128(wsr, lane * 16, hbase + (st * 56 + i) * 1024);
  };
  {
    const int li = lane & 15, gq = lane >> 4;
    u32x4 a0;
#pragma unroll
    for (int e = 0; e < 4; ++e) {
      const unsigned o = cvz + (li + 8 * gq + 2 * e + 1) * 2;
      a0[e] = (unsigned)*(const LAS u16*)o | ((unsigned)*(const LAS u16*)(o + 2) << 16);
    }
    const float Dd = __uint_as_float((unsigned)p.Dv[dch] << 16);
    const unsigned abase = cvb + (li & 3) * CV_HS + (li >> 2) * 8 + gq * 16 + hf * 4 * 64 - 64;
    const unsigned bbase = cvb + CV_H + (254 - 2 * li + gq) * 16 - 64 * 56 + hf * 4 * 64;
    f32x4 acc[8];
    u32x2 alo[2][8], ahi[2][8];
    bf16x8 B[2][8];
    auto rdA = [&](int set, int rr, int al) {
      alo[set][al] = *(const volatile LAS u32x2*)(abase + rr * 64 + al * 512);
      ahi[set][al] = *(const volatile LAS u32x2*)(abase + rr * 64 + al * 512 + 8);
    };
    auto rdB = [&](int set, int rr, int be) { B[set][be] = lds_b128(bbase + 64 * (56 - 8 * be) + 64 * rr); };
#pragma unroll
    for (int i = 0; i < 8; ++i) { rdA(0, 0, i); rdB(0, 0, i); }
    SB();
    STAMP(7);
    u64 hflag[2] = {0, 0};
    ConvCtx cx{abase, bbase, 0u - hf, a0, ws + WS_FH + (rb2 >> 1) * 128 + li * 8, t.wdso, lane, wsr};
    DSTAMP(9);
    conv_rho<0>(cx, acc, alo, ahi, B, Wd, hflag, make_iseq<36>{});
    DSTAMP(10);
    WAIT_VM(7); asm volatile("s_barrier" ::: "memory");
    if (threadIdx.x == 0) flag_st(ws + WS_FH + rb * 128 + fc * 8, epoch);
    SB();
    DSTAMP(11);
    conv_rho<1>(cx, acc, alo, ahi, B, Wd, hflag, make_iseq<36>{});
    conv_rho<2>(cx, acc, alo, ahi, B, Wd, hflag, make_iseq<36>{});
    conv_rho<3>(cx, acc, alo, ahi, B, Wd, hflag, make_iseq<36>{});
    DSTAMP(12);
    MFMA_DRAIN();
    STAMP(8);
    if (!__all(hflag[0] == epoch)) { if (!__all(hflag[1] == epoch)) wait_flags<16>(ws + WS_FH + (rb2 >> 1) * 128, epoch, ws + WS_TMO, lane); }
    __builtin_amdgcn_fence(__ATOMIC_ACQUIRE, "wavefront");
    DSTAMP(13);
    const unsigned red = lds0 + CV_RED, hm = 0u - hf;
#pragma unroll
    for (int g = 0; g < 4; ++g) *(LAS f32x4*)(red + w * 4096 + g * 1024 + lane * 16) = selv(hm, acc[g], acc[4 + g]);
    SB();
    ldH(0);
    SB();
    DSTAMP(14);
    LGKM_BAR();
    DSTAMP(15);
    f32x4 oth[4]; u32x2 kvr[4];
#pragma unroll
    for (int g = 0; g < 4; ++g) {
      oth[g] = *(const LAS f32x4*)(red + (w ^ 1) * 4096 + g * 1024 + lane * 16);
      kvr[g] = *(const LAS u32x2*)(cvb + CV_H + (2044 - (256 * (g + 4 * hf) + 16 * li + 4 * gq)) * 2);
    }
    SB();
    ldH(1);
    SB();
    u32x2 yo[4];
#pragma unroll
    for (int g = 0; g < 4; ++g) {
      f32x4 y = selv(hm, acc[4 + g], acc[g]) + oth[g];
      float r0 = y[0] + bfhi(kvr[g][1]) * Dd, r1 = y[1] + bflo(kvr[g][1]) * Dd, r2 = y[2] + bfhi(kvr[g][0]) * Dd, r3 = y[3] + bflo(kvr[g][0]) * Dd;
      yo[g] = u32x2{cvtpk(r0, r1), cvtpk(r2, r3)};
    }
    SB();
    ldH(2);
#pragma unroll
    for (int g = 0; g < 4; ++g) gst64(yo[g], wsr, (256 * (g + 4 * hf) + 16 * li + 4 * gq) * 2, WS_Y2T + dch * 4096);
    SB();
    if (PD > 3) ldH(3);
    SB();
  }
  STAMP(9);

  const int tt = threadIdx.x, cc = tt & 7, sp = tt >> 3;
  const unsigned s0 = rb2 * 64, d0 = cb * 64;
  unsigned yv[8]; u32x2 gt; u32x4 qv[2];
  {
    f32x4 acc[4][4];
    u64 yflag = 0;
#pragma unroll
    for (int i = 0; i < 14; ++i) {
      DSTAMP(16 + i);
      if (i + PD < 14) ldH(i + PD);
      if (i == 8) yflag = flag_ld(ws + WS_FY + cb * 256 + (lane & 31) * 8);
      if (i == 10) {
        if (!__all(yflag == epoch)) wait_flags<32>(ws + WS_FY + cb * 256, epoch, ws + WS_TMO, lane);
        __builtin_amdgcn_fence(__ATOMIC_ACQUIRE, "wavefront");
#pragma unroll
        for (int e = 0; e < 8; ++e) yv[e] = gld32(wsr, (8 * cc + e) * 4096 + sp * 4, WS_Y2T + d0 * 4096 + s0 * 2);
      }
      if (i == 11) {
        gt = __builtin_amdgcn_raw_buffer_load_b64(wsr, sp * 8, WS_G + s0 * 4, SC1);
#pragma unroll
        for (int r = 0; r < 2; ++r) qv[r] = *(const u32x4*)(p.q + (size_t)(s0 + 2 * sp + r) * D_ + d0 + cc * 8);
      }
      SB();
#pragma unroll
      for (int dt = 0; dt < 4; ++dt)
#pragma unroll
        for (int st = 0; st < 4; ++st) {
          if (i == 0) mfma_vav0(acc[dt][st], Wd[i][dt], H[i % (PD + 1)][st]); else mfma_vav(acc[dt][st], Wd[i][dt], H[i % (PD + 1)][st]);
        }
      SB();
      if (i == 3) {
        if (PD == 4) WAIT_VM(16); else WAIT_VM(12);
        asm volatile("s_barrier" ::: "memory");
        if (threadIdx.x == 0) flag_st(ws + WS_FY + (b >> 5) * 256 + (b & 31) * 8, epoch);
        SB();
      }
    }
    DSTAMP(30);
    MFMA_DRAIN();
    STAMP(10);
    const int li = lane & 15, gq = lane >> 4;
#pragma unroll
    for (int dt = 0; dt < 4; ++dt)
#pragma unroll
      for (int st = 0; st < 4; ++st) {
        const int s = 16 * st + li, chunk = 4 * dt + gq;
        *(LAS f32x4*)(lds0 + w * 16384 + s * 256 + ((chunk ^ li) << 4)) = acc[dt][st];
      }
  }
  LGKM_BAR();
  {
#pragma unroll
    for (int r = 0; r < 2; ++r) {
      const int s = 2 * sp + r;
      f32x4 b0 = {0, 0, 0, 0}, b1 = {0, 0, 0, 0};
#pragma unroll
      for (int ww = 0; ww < 4; ++ww) {
        b0 += *(const LAS f32x4*)(lds0 + ww * 16384 + s * 256 + (((2 * cc) ^ (s & 15)) << 4));
        b1 += *(const LAS f32x4*)(lds0 + ww * 16384 + s * 256 + (((2 * cc + 1) ^ (s & 15)) << 4));
      }
      const float g = __uint_as_float(gt[r]);
      u32x4 o;
#pragma unroll
      for (int e = 0; e < 4; ++e) {
        float bl = e < 2 ? b0[2 * e] : b1[2 * e - 4], bh = e < 2 ? b0[2 * e + 1] : b1[2 * e - 3];
        float yl = r ? bfhi(yv[2 * e]) : bflo(yv[2 * e]), yh = r ? bfhi(yv[2 * e + 1]) : bflo(yv[2 * e + 1]);
        float al = bflo(qv[r][e]) * yl, ah = bfhi(qv[r][e]) * yh;
        o[e] = cvtpk(g * al + (1.f - g) * bl, g * ah + (1.f - g) * bh);
      }
      *(u32x4*)(p.out + (size_t)(s0 + s) * D_ + d0 + cc * 8) = o;
    }
  }
  STAMP(11);
  STAMP_FLUSH(12);
}

extern "C" void kernel_launch(void* const* d_in, const int* in_sizes, int n_in, void* d_out, int out_size, void* d_ws, size_t ws_size, hipStream_t stream) {
  Params p;
  p.q = (const u16*)d_in[0]; p.k = (const u16*)d_in[1]; p.v = (const u16*)d_in[2];
  p.shk = (const u16*)d_in[3]; p.hk = (const u16*)d_in[4]; p.Dv = (const u16*)d_in[5];
  p.wg = (const u16*)d_in[7]; p.wu = (const u16*)d_in[8]; p.wd = (const u16*)d_in[9]; p.gw = (const u16*)d_in[10];
  p.out = (u16*)d_out; p.ws = (char*)d_ws;
  k_fused<<<256, 256, 0, stream>>>(p);
}
```

```cpp
#include <hip/hip_runtime.h>
#include <hip/hip_bf16.h>
#include <utility>
#pragma clang diagnostic ignored "-Winline-asm"
#pragma clang diagnostic ignored "-Wint-to-pointer-cast"

#define DI __device__ __forceinline__
#define LAS __attribute__((address_space(3)))
#define GAS __attribute__((address_space(1)))
typedef unsigned short u16;
typedef unsigned long long u64;
typedef float f32x16 __attribute__((ext_vector_type(16)));
typedef float f32x4 __attribute__((ext_vector_type(4)));
typedef float f32x2 __attribute__((ext_vector_type(2)));
typedef __bf16 bf16x8 __attribute__((ext_vector_type(8)));
typedef __bf16 bf16x2 __attribute__((ext_vector_type(2)));
typedef short s16x4 __attribute__((ext_vector_type(4)));
typedef short s16x8 __attribute__((ext_vector_type(8)));
typedef unsigned u32x4 __attribute__((ext_vector_type(4)));
typedef unsigned u32x2 __attribute__((ext_vector_type(2)));
typedef __amdgpu_buffer_rsrc_t rsrc_t;
template <int... Is> using iseq = std::integer_sequence<int, Is...>;
template <int N> using make_iseq = std::make_integer_sequence<int, N>;

constexpr int S_ = 2048, D_ = 512, F_ = 1792;
constexpr unsigned WS_E = 0, WS_FS = 256, WS_FH = 4096, WS_FY = 8192, WS_TMO = 12288;
constexpr unsigned WS_G   = 16384;
constexpr unsigned WS_KVT = 1u << 20;
constexpr unsigned WS_Y2T = WS_KVT + (2u << 20);
constexpr unsigned WS_WDT = WS_Y2T + (2u << 20);
constexpr unsigned WS_H   = WS_WDT + (2u << 20);
constexpr unsigned WS_END = WS_H + 128u * 56 * 1024;

constexpr unsigned G1_XS = 16384, G1_WIMG = 14336 + 128, G1_WS = 28800, G1_XOFF = 0, G1_WOFF = 3 * G1_XS;
constexpr unsigned ST_K = G1_WOFF + 3 * G1_WS, ST_V = ST_K + 9216, ST_SH = ST_V + 8192, ST_WD = ST_SH + 1024, ST_T = 2 * G1_XS;
constexpr unsigned CV_HS = 4160, CV_H = 4 * CV_HS, CV_X = 5120, CV_CH = CV_H + CV_X, CV_BASE = G1_WOFF + 2 * G1_WS, CV_RED = 0;
constexpr unsigned CV_Z = CV_BASE + 2 * CV_CH;
constexpr unsigned LDS_BYTES = ST_WD + 8192;
static_assert(CV_Z + 2 * 1088 <= LDS_BYTES && LDS_BYTES <= 163840, "lds");

#define SB() __builtin_amdgcn_sched_barrier(0)
#define STAMP_DECL
#define STAMP(i)
#define DSTAMP(i)
#define STAMP_FLUSH(n)

DI unsigned rfl(unsigned x) { return __builtin_amdgcn_readfirstlane(x); }
DI rsrc_t mk_rsrc(const void* p, unsigned bytes) { return __builtin_amdgcn_make_buffer_rsrc((void*)p, 0, bytes, 0x00020000); }
DI unsigned cvtpk(float lo, float hi) { f32x2 v = {lo, hi}; bf16x2 b = __builtin_convertvector(v, bf16x2); return __builtin_bit_cast(unsigned, b); }
DI float bflo(unsigned x) { return __uint_as_float(x << 16); }
DI float bfhi(unsigned x) { return __uint_as_float(x & 0xffff0000u); }
DI void dma16(rsrc_t r, unsigned voff, unsigned soff, unsigned ldsaddr) {
  asm volatile("s_mov_b32 m0, %0\n\ts_nop 0\n\tbuffer_load_dwordx4 %1, %2, %3 offen lds" :: "s"(ldsaddr), "v"(voff), "s"(r), "s"(soff) : "m0", "memory");
}
DI void m0_set(unsigned v) { asm volatile("s_mov_b32 m0, %0" :: "s"(v) : "m0", "memory"); }
template <int OFF> DI void dma_go(rsrc_t r, unsigned voff, unsigned soff) {
  asm volatile("buffer_load_dwordx4 %0, %1, %2 offen offset:%3 lds" :: "v"(voff), "s"(r), "s"(soff), "i"(OFF) : "memory");
}
#define WAIT_LGKM0() __builtin_amdgcn_s_waitcnt(0xC07F)
#define WAIT_VM(n) asm volatile("s_waitcnt vmcnt(" #n ")" ::: "memory")
#define WAIT_VM_BAR(n) asm volatile("s_waitcnt vmcnt(" #n ") lgkmcnt(0)\n\ts_barrier" ::: "memory")
#define LGKM_BAR() asm volatile("s_waitcnt lgkmcnt(0)\n\ts_barrier" ::: "memory")
DI bf16x8 lds_b128(unsigned a) { return *(const LAS bf16x8*)a; }
DI u32x4 lds_u128(unsigned a) { return *(const LAS u32x4*)a; }
DI bf16x8 lds_tr2(unsigned a0, unsigned a1) {
  s16x4 lo = __builtin_amdgcn_ds_read_tr16_b64_v4i16((LAS s16x4*)a0);
  s16x4 hi = __builtin_amdgcn_ds_read_tr16_b64_v4i16((LAS s16x4*)a1);
  s16x8 v = __builtin_shufflevector(lo, hi, 0, 1, 2, 3, 4, 5, 6, 7);
  return __builtin_bit_cast(bf16x8, v);
}
DI void mfma_vvv(f32x4& c, const bf16x8& a, const bf16x8& b) { asm volatile("v_mfma_f32_16x16x32_bf16 %0, %1, %2, %0" : "+v"(c) : "v"(a), "v"(b)); }
DI void mfma_vvv0(f32x4& c, const bf16x8& a, const bf16x8& b) { asm volatile("v_mfma_f32_16x16x32_bf16 %0, %1, %2, 0" : "=&v"(c) : "v"(a), "v"(b)); }
DI void mfma32(f32x16& c, const bf16x8& a, const bf16x8& b) { asm volatile("v_mfma_f32_32x32x16_bf16 %0, %1, %2, %0" : "+v"(c) : "v"(a), "v"(b)); }
DI void mfma_vav(f32x4& c, const u32x4& a, const u32x4& b) { asm volatile("v_mfma_f32_16x16x32_bf16 %0, %1, %2, %0" : "+v"(c) : "a"(a), "v"(b)); }
DI void mfma_vav0(f32x4& c, const u32x4& a, const u32x4& b) { asm volatile("v_mfma_f32_16x16x32_bf16 %0, %1, %2, 0" : "=&v"(c) : "a"(a), "v"(b)); }
#define MFMA_DRAIN() do { SB(); asm volatile("s_nop 7\n\ts_nop 4" ::: "memory"); SB(); } while (0)
constexpr int SC1 = 16;
DI u32x4 gld128(rsrc_t r, unsigned voff, unsigned soff) { return __builtin_amdgcn_raw_buffer_load_b128(r, voff, soff, SC1); }
DI unsigned gld32(rsrc_t r, unsigned voff, unsigned soff) { return __builtin_amdgcn_raw_buffer_load_b32(r, voff, soff, SC1); }
DI void gst128(u32x4 v, rsrc_t r, unsigned voff, unsigned soff) { __builtin_amdgcn_raw_buffer_store_b128(v, r, voff, soff, SC1); }
DI void gst64(u32x2 v, rsrc_t r, unsigned voff, unsigned soff) { __builtin_amdgcn_raw_buffer_store_b64(v, r, voff, soff, SC1); }
DI void gst32(unsigned v, rsrc_t r, unsigned voff, unsigned soff) { __builtin_amdgcn_raw_buffer_store_b32(v, r, voff, soff, SC1); }
#define RLX_AGENT __ATOMIC_RELAXED, __HIP_MEMORY_SCOPE_AGENT
DI u64 flag_ld(const char* a) { return __hip_atomic_load((const GAS u64*)a, RLX_AGENT); }
DI void flag_st(char* a, u64 v) { __hip_atomic_store((GAS u64*)a, v, RLX_AGENT); }
template <int N> DI void wait_flags(const char* base, u64 epoch, char* tmo, int lane) {
  for (unsigned spins = 0;; ++spins) {
    bool ok = true;
#pragma unroll
    for (int i = 0; i < (N + 63) / 64; ++i) { const int idx = i * 64 + lane; const u64 v = flag_ld(base + (idx < N ? idx : 0) * 8); ok &= (v == epoch); }
    if (__all(ok)) break;
    if (spins > 30000u) { if (lane == 0) flag_st(tmo, 1); break; }
    __builtin_amdgcn_s_sleep(1);
  }
  __builtin_amdgcn_fence(__ATOMIC_ACQUIRE, "wavefront");
}
DI f32x4 selv(unsigned m, f32x4 a, f32x4 b) {
  f32x4 r;
#pragma unroll
  for (int e = 0; e < 4; ++e) r[e] = __uint_as_float((__float_as_uint(a[e]) & m) | (__float_as_uint(b[e]) & ~m));
  return r;
}
DI unsigned gelu_mul2(f32x2 g, f32x2 u) {
  const f32x2 c1 = {-2.302208198f, -2.302208198f}, c2 = {-0.1029432397f, -0.1029432397f}, one = {1.f, 1.f};
  f32x2 a = g * __builtin_elementwise_fma(g * g, c2, c1);
  f32x2 e = {__builtin_amdgcn_exp2f(a[0]), __builtin_amdgcn_exp2f(a[1])};
  f32x2 d = e + one;
  f32x2 r = {__builtin_amdgcn_rcpf(d[0]), __builtin_amdgcn_rcpf(d[1])};
  f32x2 h = (g * u) * r;
  return cvtpk(h[0], h[1]);
}

DI float wave_sum(float a) {
  a += __builtin_bit_cast(float, __builtin_amdgcn_update_dpp(0, __builtin_bit_cast(int, a), 0xB1, 0xf, 0xf, true));
  a += __builtin_bit_cast(float, __builtin_amdgcn_update_dpp(0, __builtin_bit_cast(int, a), 0x4E, 0xf, 0xf, true));
  a += __builtin_bit_cast(float, __builtin_amdgcn_update_dpp(0, __builtin_bit_cast(int, a), 0x141, 0xf, 0xf, true));
  a += __builtin_bit_cast(float, __builtin_amdgcn_update_dpp(0, __builtin_bit_cast(int, a), 0x140, 0xf, 0xf, true));
  const int x = __builtin_bit_cast(int, a);
  return (__builtin_bit_cast(float, __builtin_amdgcn_readlane(x, 0)) + __builtin_bit_cast(float, __builtin_amdgcn_readlane(x, 16))) +
         (__builtin_bit_cast(float, __builtin_amdgcn_readlane(x, 32)) + __builtin_bit_cast(float, __builtin_amdgcn_readlane(x, 48)));
}
DI float dot2(unsigned a, unsigned b, float c) { return __builtin_amdgcn_fdot2_f32_bf16(__builtin_bit_cast(bf16x2, a), __builtin_bit_cast(bf16x2, b), c, false); }
struct Params { const u16 *q, *k, *v, *shk, *hk, *Dv, *wg, *wu, *wd, *gw; u16* out; char* ws; };

struct G1 {
  rsrc_t xs, wsr;
  unsigned xv[4], wv[7];
  unsigned sx, sw;
  unsigned mx, mw;
  unsigned xm0, wm0, slot;
  unsigned dx, dw;
  unsigned xa[4], xb[4], wt, w6;
  f32x16 acc[3][2], acc6;
  bf16x8 XA[2], XB[2], W[2][4];
};
template <int N> DI void g1_go(G1& c) {
  if constexpr (N < 4) dma_go<N * 1024>(c.xs, c.xv[N], c.sx);
  else dma_go<(N < 8 ? N - 4 : N - 8) * 1024>(c.wsr, c.wv[N - 4], c.sw);
}
template <int N> DI void g1_dma(G1& c, unsigned st, unsigned slot) {
  const unsigned m = N < 4 ? c.xm0 + slot * G1_XS : c.wm0 + slot * G1_WS + (N >= 8 ? 4096 : 0);
  asm volatile("s_mov_b32 m0, %0\n\ts_nop 0" :: "s"(m) : "m0", "memory");
  if constexpr (N < 4) dma_go<N * 1024>(c.xs, c.xv[N], c.sx + st * 128);
  else dma_go<(N < 8 ? N - 4 : N - 8) * 1024>(c.wsr, c.wv[N - 4], c.sw + st * (64 * 3584));
}
template <int... Ns> DI void g1_dma_all(G1& c, unsigned st, unsigned slot, iseq<Ns...>) { (g1_dma<Ns>(c, st, slot), ...); }
template <int KK, int R> DI void g1_read(G1& c) {
  constexpr int set = KK & 1;
  if constexpr (R == 0) c.XA[set] = lds_b128(c.xa[KK]);
  else if constexpr (R == 2) c.XB[set] = lds_b128(c.xb[KK]);
  else {
    constexpr int p = R == 1 ? 0 : R - 2;
    constexpr unsigned o = KK * 16 * 224;
    if constexpr (p < 3) c.W[set][p] = lds_tr2(c.wt + o + p * 32, c.wt + o + p * 32 + 4 * 224);
    else c.W[set][p] = lds_tr2(c.w6 + o, c.w6 + o + 4 * 224);
  }
}
template <int KK, int M> DI void g1_mfma(G1& c) {
  constexpr int set = KK & 1;
  if constexpr (M < 6) mfma32(c.acc[M >> 1][M & 1], c.W[set][M >> 1], (M & 1) ? c.XB[set] : c.XA[set]);
  else mfma32(c.acc6, c.W[set][3], c.XA[set]);
}
struct Tail {
  rsrc_t hs, xr, wsr; unsigned hvoff, hm0, zm0, xvoff, wdso, xlds; const char* sflags; u64 epoch; char* tmo;
  u64 pf[4]; u32x4 xv[3];
};
DI void poll_issue(u64& v, const char* a) { asm volatile("global_load_dwordx2 %0, %1, off sc1" : "=v"(v) : "v"(a) : "memory"); }
template <int I> DI void tail_op(Tail& t, u32x4 (&Wd)[14][4], int lane) {
  if constexpr (I < 4) dma_go<I * 1024>(t.hs, t.hvoff, 0);
  else if constexpr (I == 4) dma_go<0>(t.hs, lane * 16, 0);
  else if constexpr (I < 9) dma_go<(I - 5) * 1024>(t.hs, t.hvoff + 2, 0);
  else if constexpr (I < 11) { }
  else if constexpr (I < 14) t.xv[I - 11] = gld128(t.xr, t.xvoff + (I - 11) * 1024, 0);
  else { constexpr int n = I - 14; Wd[n >> 2][n & 3] = gld128(t.wsr, lane * 16 + (n & 3) * 1024, t.wdso + (n >> 2) * 4096); }
}
constexpr int TAIL_WD = 12;
constexpr int vm_slot(int g) { constexpr int s[11] = {24, 26, 1, 3, 5, 8, 10, 12, 15, 17, 19}; for (int i = 0; i < 11; ++i) if (s[i] == g) return i; return -1; }
template <int MODE, int G> DI void g1_gap(G1& c, Tail& t, u32x4 (&Wd)[14][4], int lane) {
  constexpr int kk = G / 7, m = G % 7, v = vm_slot(G);
  if constexpr (MODE == 5 && G == 0) {
#pragma unroll
    for (int i = 0; i < 4; ++i) poll_issue(t.pf[i], t.sflags + (i * 64 + lane) * 8);
  }
  g1_mfma<kk, m>(c);
  if constexpr (kk < 3) { if constexpr (m < 6) g1_read<kk + 1, m>(c); }
  else if constexpr (MODE != 7) {
    if constexpr (m == 1) {
      SB();
      if constexpr (MODE == 5) asm volatile("s_waitcnt vmcnt(15) lgkmcnt(0)\n\ts_barrier" ::: "memory");
      else if constexpr (MODE == 6) asm volatile("s_waitcnt vmcnt(9) lgkmcnt(0)\n\ts_barrier" : "+v"(t.pf[0]), "+v"(t.pf[1]), "+v"(t.pf[2]), "+v"(t.pf[3]) :: "memory");
      else asm volatile("s_waitcnt vmcnt(11) lgkmcnt(0)\n\ts_barrier" ::: "memory");
      if constexpr (MODE == 6) {
        const bool ok = t.pf[0] == t.epoch && t.pf[1] == t.epoch && t.pf[2] == t.epoch && t.pf[3] == t.epoch;
        if (!__all(ok)) wait_flags<256>(t.sflags, t.epoch, t.tmo, lane);
        __builtin_amdgcn_fence(__ATOMIC_ACQUIRE, "wavefront");
      }
    }
    if constexpr (m == 2) { g1_read<0, 0>(c); g1_read<0, 1>(c); }
    if constexpr (m == 3) { g1_read<0, 2>(c); g1_read<0, 3>(c); }
    if constexpr (m == 4) g1_read<0, 4>(c);
    if constexpr (m == 5) g1_read<0, 5>(c);
  }
  if constexpr (v >= 0) {
    if constexpr (MODE == 0) g1_go<v>(c);
    if constexpr (MODE == 5) { if constexpr (v >= 2) g1_go<v>(c); else tail_op<v>(t, Wd, lane); }
    if constexpr (MODE == 6) { if constexpr (v >= 2) tail_op<v>(t, Wd, lane); else tail_op<11 + v>(t, Wd, lane); }
    if constexpr (MODE == 7) { if constexpr (v >= 2) tail_op<11 + v>(t, Wd, lane); else tail_op<23 + v>(t, Wd, lane); }
  }
  if constexpr (MODE == 7 && G == 22) tail_op<22>(t, Wd, lane);
  if constexpr (MODE == 7 && G == 27) tail_op<25>(t, Wd, lane);
  if constexpr (MODE == 7 && (G == 20 || G == 21 || G == 23)) { constexpr int i = G == 20 ? 0 : G == 21 ? 1 : 2; *(LAS u32x4*)(t.xlds + i * 1024) = t.xv[i]; }
  if constexpr (MODE == 0 || MODE == 5) {
    if constexpr (G == 4) m0_set(c.mw);
    if constexpr (G == 6) c.sx += 128;
    if constexpr (G == 14) m0_set(c.mw + 4096);
    if constexpr (G == 20) c.sw += 64 * 3584;
  }
  if constexpr (MODE == 0 && G == 23) m0_set(c.mx);
  if constexpr (MODE == 5 && G == 23) m0_set(t.hm0);
  if constexpr (MODE == 6 && G == 4) m0_set(t.zm0);
  if constexpr (MODE == 6 && G == 7) m0_set(t.hm0 + CV_HS);
  if constexpr (MODE != 7) {
    if constexpr (G == 9) { c.xa[0] += c.dx; c.xb[0] += c.dx; }
    if constexpr (G == 11) { c.xa[1] += c.dx; c.xb[1] += c.dx; }
    if constexpr (G == 16) { c.xa[2] += c.dx; c.xb[2] += c.dx; }
    if constexpr (G == 21) { c.xa[3] += c.dx; c.xb[3] += c.dx; c.wt += c.dw; c.w6 += c.dw; }
  }
  if constexpr (MODE == 0) {
    if constexpr (G == 25) { c.mw = c.wm0 + c.slot * G1_WS; c.slot = c.slot == 2 ? 0 : c.slot + 1; asm volatile("" : "+s"(c.mw), "+s"(c.slot)); }
    if constexpr (G == 27) {
      c.dx = c.slot == 2 ? 0u - 2 * G1_XS : G1_XS; c.dw = c.slot == 2 ? 0u - 2 * G1_WS : G1_WS; c.mx = c.xm0 + c.slot * G1_XS;
      asm volatile("" : "+s"(c.dx), "+s"(c.dw), "+s"(c.mx));
    }
  }
  SB();
}
template <int MODE, int... Gs> DI void g1_stage(G1& c, Tail& t, u32x4 (&Wd)[14][4], int lane, iseq<Gs...>) { (g1_gap<MODE, Gs>(c, t, Wd, lane), ...); }
template <int... Rs> DI void g1_read0(G1& c, iseq<Rs...>) { (g1_read<0, Rs>(c), ...); }

struct ConvCtx { unsigned abase, bbase, hm; u32x4 a0; const char* hfl; unsigned wdso; int lane; rsrc_t wsr; };
constexpr int cv_al(int m) { int al = 0, n = 8; while (m >= n) { m -= n; --n; ++al; } return al; }
constexpr int cv_be(int m) { int n = 8; while (m >= n) { m -= n; --n; } return m; }
template <int RR, int M> DI void conv_gap(const ConvCtx& cx, f32x4 (&acc)[8], u32x2 (&alo)[2][8], u32x2 (&ahi)[2][8], bf16x8 (&B)[2][8], u32x4 (&Wd)[14][4], u64& hflag) {
  constexpr int set = RR & 1, al = cv_al(M), be = cv_be(M);
  if constexpr (M == 0) WAIT_LGKM0();
  u32x4 av = {alo[set][al][0], alo[set][al][1], ahi[set][al][0], ahi[set][al][1]};
  if constexpr (RR == 0 && al == 0) {
#pragma unroll
    for (int e = 0; e < 4; ++e) av[e] = (av[e] & cx.hm) | (cx.a0[e] & ~cx.hm);
  }
  bf16x8 A = __builtin_bit_cast(bf16x8, av);
  if constexpr (RR == 0 && al == 0) { asm volatile("s_nop 1" : "+v"(A)); mfma_vvv0(acc[be], A, B[set][be]); }
  else mfma_vvv(acc[al + be], A, B[set][be]);
  if constexpr (RR == 3 && M == 1) hflag = flag_ld(cx.hfl);
  if constexpr (M % 5 == 3) {
    constexpr int n = 28 + RR * 7 + M / 5;
    if constexpr (n < 56) Wd[n >> 2][n & 3] = gld128(cx.wsr, cx.lane * 16 + (n & 3) * 1024, cx.wdso + (n >> 2) * 4096);

  } else if constexpr (RR < 3) {
    constexpr int rd = M - (M + 1) / 5;
    if constexpr (rd < 24) {
      constexpr int i = rd / 3, k = rd % 3;
      if constexpr (k == 0) alo[set ^ 1][i] = *(const volatile LAS u32x2*)(cx.abase + (RR + 1) * 64 + i * 512);
      else if constexpr (k == 1) ahi[set ^ 1][i] = *(const volatile LAS u32x2*)(cx.abase + (RR + 1) * 64 + i * 512 + 8);
      else B[set ^ 1][i] = lds_b128(cx.bbase + 64 * (56 - 8 * i) + 64 * (RR + 1));
    }
  }
  SB();
}
template <int RR, int... Ms> DI void conv_rho(const ConvCtx& cx, f32x4 (&acc)[8], u32x2 (&alo)[2][8], u32x2 (&ahi)[2][8], bf16x8 (&B)[2][8], u32x4 (&Wd)[14][4], u64& hflag, iseq<Ms...>) {
  (conv_gap<RR, Ms>(cx, acc, alo, ahi, B, Wd, hflag), ...);
}

struct G2Ctx {
  rsrc_t wsr; const char* yfl; u64 epoch; char* tmo; int lane;
  unsigned yvo, yso, gvo, gso; const u16* qp; unsigned hso, rbase; int gq, li; char* myy;
};
constexpr int G2PD = 3;
template <int I, int M> DI void g2_gap(const G2Ctx& g, f32x4 (&acc)[4][4], u32x4 (&Wd)[14][4], u32x4 (&H)[G2PD + 1][4], const unsigned (&hvo)[4], u64& yflag,
                                       unsigned (&yv)[8], u32x2& gt, u32x4 (&qv)[2]) {
  constexpr int dt = M >> 2, st = M & 3;
  if constexpr (I == 0) mfma_vav0(acc[dt][st], Wd[I][dt], H[I % (G2PD + 1)][st]); else mfma_vav(acc[dt][st], Wd[I][dt], H[I % (G2PD + 1)][st]);
  if constexpr (I + G2PD < 14 && (M & 3) == 1) H[(I + G2PD) % (G2PD + 1)][M >> 2] = gld128(g.wsr, hvo[M >> 2], g.hso + (I + G2PD) * 1024);
  if constexpr (I == 8 && M == 15) yflag = flag_ld(g.yfl + (g.lane & 31) * 8);
  if constexpr (I == 11) {
    if constexpr (M == 0) {
      if (!__all(yflag == g.epoch)) wait_flags<32>(g.yfl, g.epoch, g.tmo, g.lane);
      __builtin_amdgcn_fence(__ATOMIC_ACQUIRE, "wavefront");
    }
    if constexpr ((M & 1) == 1) yv[M >> 1] = gld32(g.wsr, g.yvo + (M >> 1) * 4096, g.yso);
  }
  if constexpr (I == 12) {
    if constexpr (M == 1) gt = __builtin_amdgcn_raw_buffer_load_b64(g.wsr, g.gvo, g.gso, SC1);
    if constexpr (M == 5) qv[0] = *(const u32x4*)g.qp;
    if constexpr (M == 9) qv[1] = *(const u32x4*)(g.qp + D_);
  }
  if constexpr (I == 13 && M >= 9) { constexpr int q = M - 9; *(LAS f32x4*)(g.rbase + (q & 3) * 4096 + (((4 * (q >> 2) + g.gq) ^ g.li) << 4)) = acc[q >> 2][q & 3]; }
  SB();
}
template <int I, int... Ms> DI void g2_step(const G2Ctx& g, f32x4 (&acc)[4][4], u32x4 (&Wd)[14][4], u32x4 (&H)[G2PD + 1][4], const unsigned (&hvo)[4], u64& yflag,
                                            unsigned (&yv)[8], u32x2& gt, u32x4 (&qv)[2], iseq<Ms...>) {
  (g2_gap<I, Ms>(g, acc, Wd, H, hvo, yflag, yv, gt, qv), ...);
  if constexpr (I == 3) {
    WAIT_VM(12); asm volatile("s_barrier" ::: "memory");
    if (threadIdx.x == 0) flag_st(g.myy, g.epoch);
    SB();
  }
  if constexpr (I == 13) {
    MFMA_DRAIN();
#pragma unroll
    for (int q = 7; q < 16; ++q) *(LAS f32x4*)(g.rbase + (q & 3) * 4096 + (((4 * (q >> 2) + g.gq) ^ g.li) << 4)) = acc[q >> 2][q & 3];
  }
}
template <int... Is> DI void g2_loop(const G2Ctx& g, f32x4 (&acc)[4][4], u32x4 (&Wd)[14][4], u32x4 (&H)[G2PD + 1][4], const unsigned (&hvo)[4], u64& yflag,
                                     unsigned (&yv)[8], u32x2& gt, u32x4 (&qv)[2], iseq<Is...>) {
  (g2_step<Is>(g, acc, Wd, H, hvo, yflag, yv, gt, qv, make_iseq<16>{}), ...);
}

__global__ __launch_bounds__(256, 1) void k_fused(Params p) {
  __shared__ __attribute__((aligned(16))) char smem[LDS_BYTES];
  const unsigned lds0 = (unsigned)(size_t)(LAS char*)smem;
  const int lane = threadIdx.x & 63;
  const unsigned w = rfl(threadIdx.x >> 6);
  const unsigned b = blockIdx.x, xcd = b & 7, jb = b >> 3;
  char* const ws = p.ws;
  STAMP_DECL; STAMP(0);
  asm volatile("" :: "s"(p.q), "s"(p.k), "s"(p.v), "s"(p.shk), "s"(p.hk), "s"(p.Dv), "s"(p.wg), "s"(p.wu), "s"(p.wd), "s"(p.gw), "s"(p.out), "s"(p.ws));
  const rsrc_t wsr = mk_rsrc(ws, WS_END);
  u64 eword;
  asm volatile("global_load_dwordx2 %0, %1, off" : "=v"(eword) : "v"(ws + WS_E) : "memory");

  const unsigned sb = b >> 3, db = b & 7, s0p = sb * 64, d0p = db * 64;
  const unsigned kg_w = b >> 2, cq_w = b & 3;
  {
    const unsigned r8 = lane >> 3, c8 = lane & 7;
    const rsrc_t rk = mk_rsrc(p.k, S_ * D_ * 2), rv = mk_rsrc(p.v, S_ * D_ * 2), rw = mk_rsrc(p.wd, F_ * D_ * 2);
    auto dk = [&](int i) { int row = (int)s0p - 3 + 8 * i + (int)r8; unsigned vo = row >= 0 ? (unsigned)row * 1024 + d0p * 2 + c8 * 16 : 0x80000000u; dma16(rk, vo, 0, lds0 + ST_K + i * 1024); };
    auto dv = [&](int i) { dma16(rv, r8 * 1024 + c8 * 16, (s0p + 8 * i) * 1024 + d0p * 2, lds0 + ST_V + i * 1024); };
    auto dw = [&](int i) { dma16(rw, (lane >> 4) * 1024 + (lane & 15) * 16, (kg_w * 32 + 4 * i) * 1024 + cq_w * 256, lds0 + ST_WD + i * 1024); };
    if (w == 0) { dk(0); dk(1); dk(2); dk(3); dk(4); dk(5); dk(6); }
    else if (w == 1) { dk(7); dk(8); dv(0); dv(1); dv(2); dv(3); dv(4); }
    else if (w == 2) { dv(5); dv(6); dv(7); dma16(mk_rsrc(p.shk, D_ * 8), lane * 16, d0p * 8, lds0 + ST_SH); dw(0); dw(1); dw(2); }
    else { dw(3); dw(4); dw(5); dw(6); dw(7); }
  }
  SB();
  const unsigned rb = 8 * (xcd >> 2) + (jb & 7), fc = 4 * (xcd & 3) + (jb >> 3);
  const unsigned mi = w >> 1, ni = w & 1;
  G1 c;
  c.xs = mk_rsrc(p.q, S_ * D_ * 2);
  c.wsr = mk_rsrc((w >> 1) ? p.wu : p.wg, D_ * F_ * 2);
#pragma unroll
  for (int n = 0; n < 4; ++n) c.xv[n] = (lane >> 3) * 1024 + (((lane & 7) ^ ((4 * (n & 1) + (lane >> 4)) & 7)) << 4) + n * (8192 - 1024);
#pragma unroll
  for (int jj = 0; jj < 7; ++jj) {
    const unsigned ci = 64 * (7 * (w & 1) + jj) + lane, k = (ci * 4682u) >> 16, pc = ci - k * 14;
    c.wv[jj] = k * 3584 + pc * 16 - (jj & 3) * 1024;
  }
  c.sx = (rb * 128 + 32 * w) * 1024;
  c.sw = fc * 224;
  c.xm0 = lds0 + G1_XOFF + w * 4096;
  c.wm0 = lds0 + G1_WOFF + (w >> 1) * G1_WIMG + (w & 1) * 7 * 1024;
  g1_dma_all(c, 0, 0, make_iseq<11>{});
  {
    const int r = lane & 31, hh = lane >> 5;
    const unsigned base = r * 128 + ((hh ^ ((r >> 1) & 7)) << 4);
#pragma unroll
    for (int kk = 0; kk < 4; ++kk) {
      c.xa[kk] = lds0 + G1_XOFF + (2 * mi + ni) * 4096 + (base ^ (kk << 5));
      c.xb[kk] = lds0 + G1_XOFF + (2 * mi + (1 - ni)) * 4096 + (base ^ (kk << 5));
    }
    const unsigned wb = lds0 + G1_WOFF + ((lane >> 4) & 1) * G1_WIMG + (8 * hh + ((lane & 15) >> 2)) * 224 + (lane & 3) * 8;
    c.wt = wb + ni * 96; c.w6 = wb + 6 * 32;
  }
  SB();
  STAMP(1);
  asm volatile("s_waitcnt vmcnt(11)\n\ts_barrier" : "+v"(eword) :: "memory");
  const u64 epoch = ((u64)rfl((unsigned)(eword >> 32)) << 32 | rfl((unsigned)eword)) * 6364136223846793005ull + 1442695040888963407ull;
  STAMP(2);
  {
    const int t = threadIdx.x, c4 = t & 15, rg = t >> 4;
    u32x2 kr[7], vr[4]; u32x4 w4[2];
#pragma unroll
    for (int i = 0; i < 7; ++i) kr[i] = *(const LAS u32x2*)(lds0 + ST_K + (4 * rg + i) * 128 + c4 * 8);
#pragma unroll
    for (int i = 0; i < 4; ++i) vr[i] = *(const LAS u32x2*)(lds0 + ST_V + (4 * rg + i) * 128 + c4 * 8);
#pragma unroll
    for (int i = 0; i < 2; ++i) w4[i] = lds_u128(lds0 + ST_SH + c4 * 32 + i * 16);
    const int li = lane & 15, gq = lane >> 4;
    bf16x8 wf[2];
#pragma unroll
    for (int i = 0; i < 2; ++i) { const unsigned a = lds0 + ST_WD + (8 * gq + (li >> 2)) * 256 + (2 * w + i) * 32 + (lane & 3) * 8; wf[i] = lds_tr2(a, a + 4 * 256); }
    g1_dma<0>(c, 1, 1); g1_dma<1>(c, 1, 1);
    SB();
#pragma unroll
    for (int j = 0; j < 4; ++j) {
      const unsigned wa = w4[j >> 1][(j & 1) * 2], wb = w4[j >> 1][(j & 1) * 2 + 1];
      const float w0 = bflo(wa), w1 = bfhi(wa), w2 = bflo(wb), w3 = bfhi(wb);
      float kk[7], o[4];
#pragma unroll
      for (int i = 0; i < 7; ++i) kk[i] = (j & 1) ? bfhi(kr[i][j >> 1]) : bflo(kr[i][j >> 1]);
      if (j == 0) g1_dma<2>(c, 1, 1); if (j == 1) g1_dma<4>(c, 1, 1); if (j == 2) g1_dma<6>(c, 1, 1); if (j == 3) g1_dma<8>(c, 1, 1);
      SB();
#pragma unroll
      for (int i = 0; i < 4; ++i) {
        const float vv = (j & 1) ? bfhi(vr[i][j >> 1]) : bflo(vr[i][j >> 1]);
        o[i] = (kk[i] * w3 + kk[i + 1] * w2 + kk[i + 2] * w1 + kk[i + 3] * w0) * vv;
      }
      const int wd = 30 - 2 * rg, dl = 4 * c4 + j;
      *(LAS u32x2*)(lds0 + ST_T + dl * 128 + ((((wd >> 2) ^ c4) & 7) << 4) + (wd & 3) * 4) = u32x2{cvtpk(o[3], o[2]), cvtpk(o[1], o[0])};
      if (j == 0) g1_dma<3>(c, 1, 1); if (j == 1) g1_dma<5>(c, 1, 1); if (j == 2) g1_dma<7>(c, 1, 1); if (j == 3) g1_dma<9>(c, 1, 1);
      SB();
    }
    g1_dma<10>(c, 1, 1);
    SB();
    LGKM_BAR();
    u32x4 val[2];
#pragma unroll
    for (int i = 0; i < 2; ++i) { const int dl = (w * 2 + i) * 8 + (lane >> 3), ch = lane & 7; val[i] = lds_u128(lds0 + ST_T + dl * 128 + ((ch ^ (dl >> 2)) & 7) * 16); }
#pragma unroll
    for (int i = 0; i < 2; ++i) {
      const unsigned D16 = 8 * cq_w + 2 * w + i;
      gst128(__builtin_bit_cast(u32x4, wf[i]), wsr, kg_w < 56 ? WS_WDT + (((D16 >> 2) * 56 + kg_w) * 4 + (D16 & 3)) * 1024 + lane * 16 : 0x80000000u, 0);
    }
#pragma unroll
    for (int i = 0; i < 2; ++i) {
      const int dl = (w * 2 + i) * 8 + (lane >> 3), ch = lane & 7;
      gst128(val[i], wsr, WS_KVT + (d0p + dl) * 4096 + (1984 - s0p) * 2 + ch * 16, 0);
    }
    SB();
  }
  STAMP(3);
  asm volatile("s_waitcnt vmcnt(15) lgkmcnt(0)\n\ts_barrier" ::: "memory");
  g1_read0(c, make_iseq<6>{});
  g1_dma<0>(c, 2, 2); g1_dma<1>(c, 2, 2);
  c.sx += 2 * 128; c.sw += 2 * (64 * 3584);
  c.slot = 0; c.mx = c.xm0; c.mw = c.wm0 + 2 * G1_WS; c.dx = G1_XS; c.dw = G1_WS;
#pragma unroll
  for (int i = 0; i < 3; ++i) { c.acc[i][0] = f32x16{}; c.acc[i][1] = f32x16{}; }
  c.acc6 = f32x16{};
  SB();
  asm volatile("s_nop 1" ::: "memory");
  SB();
  STAMP(4);
  const unsigned cch = w >> 1, hf = w & 1, dch = b * 2 + cch, cvb = lds0 + CV_BASE + cch * CV_CH, cvz = lds0 + CV_Z + cch * 1088;
  const unsigned rb2 = 4 * xcd + (jb & 3), cb = jb >> 2;
  u32x4 Wd[14][4];
  Tail t;
  t.hs = mk_rsrc(p.hk + (size_t)dch * S_, (D_ - dch) * S_ * 2);
  t.hvoff = lane * 16 + (2 * hf + 17) * 2; t.hm0 = cvb + 2 * hf * CV_HS; t.zm0 = cvz + 32;
  t.xr = mk_rsrc(ws + WS_KVT + dch * 4096, 4096); t.xvoff = (hf * 128 + lane) * 16; t.xlds = cvb + CV_H + (hf * 128 + lane) * 16;
  t.wsr = wsr; t.wdso = WS_WDT + (cb * 56 + 14 * w) * 4096;
  t.sflags = ws + WS_FS; t.epoch = epoch; t.tmo = ws + WS_TMO;
  {
#pragma unroll 1
    for (unsigned s = 0; s < 5; ++s) {
      DSTAMP(s);
      g1_stage<0>(c, t, Wd, lane, make_iseq<28>{});
      if (s == 0 && threadIdx.x == 0) flag_st(ws + WS_FS + b * 8, epoch);
    }
    DSTAMP(5);
    g1_stage<5>(c, t, Wd, lane, make_iseq<28>{});
    if (lane < 8) *(LAS unsigned*)(cvz + lane * 4) = 0;
    c.dx = G1_XS; c.dw = G1_WS;
    DSTAMP(6);
    g1_stage<6>(c, t, Wd, lane, make_iseq<28>{});
    if (b == 0 && threadIdx.x == 0) flag_st(ws + WS_E, epoch);
    DSTAMP(7);
    g1_stage<7>(c, t, Wd, lane, make_iseq<28>{});
    DSTAMP(8);
  }
  MFMA_DRAIN();
  STAMP(5);
  auto ldWd = [&](int n) { Wd[n >> 2][n & 3] = gld128(wsr, lane * 16 + (n & 3) * 1024, t.wdso + (n >> 2) * 4096); };
  {
    const unsigned grow = 128 * rb + 8 * fc + 2 * w;
    const u32x4 gx0 = *(const u32x4*)(p.q + (size_t)grow * D_ + lane * 8), gx1 = *(const u32x4*)(p.q + (size_t)(grow + 1) * D_ + lane * 8), gw4 = *(const u32x4*)(p.gw + lane * 8);
    SB();
    const int s32 = lane & 31, hh = lane >> 5;
    const unsigned lo = (s32 >> 4) * 57344 + (s32 & 15) * 16 + 8 * hh;
    const unsigned hb = WS_H + rb * 8 * 57344 + fc * 7 * 512;
    const unsigned bA = hb + (2 * mi + ni) * 2 * 57344, bB = hb + (2 * mi + (1 - ni)) * 2 * 57344, t3 = 3 * ni * 512;
    const unsigned sA = bA + t3, sB = bB + t3, s6 = bA + 6 * 512;
    auto put = [&](int n0, int nl, const f32x16& a, unsigned so, int imm) {
      if (nl > 0) ldWd(n0);
      u32x2 o0 = {gelu_mul2(f32x2{a[0], a[1]}, f32x2{a[8], a[9]}), gelu_mul2(f32x2{a[2], a[3]}, f32x2{a[10], a[11]})};
      gst64(o0, wsr, lo + imm, so);
      SB();
      if (nl > 1) ldWd(n0 + 1);
      u32x2 o1 = {gelu_mul2(f32x2{a[4], a[5]}, f32x2{a[12], a[13]}), gelu_mul2(f32x2{a[6], a[7]}, f32x2{a[14], a[15]})};
      if (nl > 2) ldWd(n0 + 2);
      gst64(o1, wsr, lo + imm + 256, so);
      SB();
    };
    put(12, 3, c.acc[0][0], sA, 0); put(15, 3, c.acc[0][1], sB, 0);
    put(18, 2, c.acc[1][0], sA, 512); put(20, 2, c.acc[1][1], sB, 512);
    put(22, 2, c.acc[2][0], sA, 1024); put(24, 2, c.acc[2][1], sB, 1024);
    put(26, 2, c.acc6, s6, 0);
    float gsum[2] = {0.f, 0.f};
#pragma unroll
    for (int j = 0; j < 4; ++j) { gsum[0] = dot2(gx0[j], gw4[j], gsum[0]); gsum[1] = dot2(gx1[j], gw4[j], gsum[1]); }
#pragma unroll
    for (int r = 0; r < 2; ++r) gsum[r] = __builtin_amdgcn_rcpf(1.f + __builtin_amdgcn_exp2f(-1.4426950409f * wave_sum(gsum[r])));
    gst32(__float_as_uint(lane & 1 ? gsum[1] : gsum[0]), wsr, lane < 2 ? WS_G + (grow + lane) * 4 : 0x80000000u, 0);
    SB();
  }
  LGKM_BAR();
  STAMP(6);

  constexpr int PD = 3;
  u32x4 H[PD + 1][4];
  const unsigned hso = WS_H + ((4 * rb2) * 56 + 14 * w) * 1024;
  unsigned hvo[4];
#pragma unroll
  for (int st = 0; st < 4; ++st) hvo[st] = lane * 16 + st * 57344;
  auto ldH1 = [&](int i, int st) { H[i % (PD + 1)][st] = gld128(wsr, hvo[st], hso + i * 1024); };
  auto ldH = [&](int i) { ldH1(i, 0); ldH1(i, 1); ldH1(i, 2); ldH1(i, 3); };
  {
    const int li = lane & 15, gq = lane >> 4;
    u32x4 a0;
#pragma unroll
    for (int e = 0; e < 4; ++e) {
      const unsigned o = cvz + (li + 8 * gq + 2 * e + 1) * 2;
      a0[e] = (unsigned)*(const LAS u16*)o | ((unsigned)*(const LAS u16*)(o + 2) << 16);
    }
    const float Dd = __uint_as_float((unsigned)p.Dv[dch] << 16);
    const unsigned abase = cvb + (li & 3) * CV_HS + (li >> 2) * 8 + gq * 16 + hf * 4 * 64 - 64;
    const unsigned bbase = cvb + CV_H + (254 - 2 * li + gq) * 16 - 64 * 56 + hf * 4 * 64;
    f32x4 acc[8];
    u32x2 alo[2][8], ahi[2][8];
    bf16x8 B[2][8];
#pragma unroll
    for (int i = 0; i < 8; ++i) {
      alo[0][i] = *(const volatile LAS u32x2*)(abase + i * 512);
      ahi[0][i] = *(const volatile LAS u32x2*)(abase + i * 512 + 8);
      B[0][i] = lds_b128(bbase + 64 * (56 - 8 * i));
    }
    SB();
    STAMP(7);
    u64 hflag = 0;
    ConvCtx cx{abase, bbase, 0u - hf, a0, ws + WS_FH + (rb2 >> 1) * 128 + li * 8, t.wdso, lane, wsr};
    DSTAMP(9);
    conv_rho<0>(cx, acc, alo, ahi, B, Wd, hflag, make_iseq<36>{});
    DSTAMP(10);
    WAIT_VM(7); asm volatile("s_barrier" ::: "memory");
    if (threadIdx.x == 0) flag_st(ws + WS_FH + rb * 128 + fc * 8, epoch);
    SB();
    DSTAMP(11);
    conv_rho<1>(cx, acc, alo, ahi, B, Wd, hflag, make_iseq<36>{});
    conv_rho<2>(cx, acc, alo, ahi, B, Wd, hflag, make_iseq<36>{});
    conv_rho<3>(cx, acc, alo, ahi, B, Wd, hflag, make_iseq<36>{});
    DSTAMP(12);
    STAMP(8);
    if (!__all(hflag == epoch)) wait_flags<16>(ws + WS_FH + (rb2 >> 1) * 128, epoch, ws + WS_TMO, lane);
    __builtin_amdgcn_fence(__ATOMIC_ACQUIRE, "wavefront");
    DSTAMP(13);
    ldH(0);
    MFMA_DRAIN();
    const unsigned red = lds0 + CV_RED, hm = 0u - hf;
#pragma unroll
    for (int g = 0; g < 4; ++g) *(LAS f32x4*)(red + w * 4096 + g * 1024 + lane * 16) = selv(hm, acc[g], acc[4 + g]);
    SB();
    ldH(1);
    SB();
    DSTAMP(14);
    LGKM_BAR();
    DSTAMP(15);
    f32x4 oth[4]; u32x2 kvr[4];
#pragma unroll
    for (int g = 0; g < 4; ++g) {
      oth[g] = *(const LAS f32x4*)(red + (w ^ 1) * 4096 + g * 1024 + lane * 16);
      kvr[g] = *(const LAS u32x2*)(cvb + CV_H + (2044 - (256 * (g + 4 * hf) + 16 * li + 4 * gq)) * 2);
    }
    SB();
    ldH(2);
    SB();
#pragma unroll
    for (int g = 0; g < 4; ++g) {
      f32x4 y = selv(hm, acc[4 + g], acc[g]) + oth[g];
      float r0 = y[0] + bfhi(kvr[g][1]) * Dd, r1 = y[1] + bflo(kvr[g][1]) * Dd, r2 = y[2] + bfhi(kvr[g][0]) * Dd, r3 = y[3] + bflo(kvr[g][0]) * Dd;
      gst64(u32x2{cvtpk(r0, r1), cvtpk(r2, r3)}, wsr, (16 * li + 4 * gq) * 2 + g * 512, WS_Y2T + dch * 4096 + hf * 2048);
    }
    SB();
  }
  STAMP(9);

  const int tt = threadIdx.x, cc = tt & 7, sp = tt >> 3;
  const unsigned s0 = rb2 * 64, d0 = cb * 64;
  unsigned yv[8]; u32x2 gt; u32x4 qv[2];
  {
    f32x4 acc[4][4];
    u64 yflag = 0;
    const int li = lane & 15, gq = lane >> 4;
    const unsigned rbase = lds0 + w * 16384 + li * 256;
    G2Ctx g2{wsr, ws + WS_FY + cb * 256, epoch, ws + WS_TMO, lane, (unsigned)(8 * cc * 4096 + sp * 4), WS_Y2T + d0 * 4096 + s0 * 2, (unsigned)(sp * 8), WS_G + s0 * 4,
             p.q + (size_t)(s0 + 2 * sp) * D_ + d0 + cc * 8, hso, rbase, gq, li, ws + WS_FY + (b >> 5) * 256 + (b & 31) * 8};
    g2_loop(g2, acc, Wd, H, hvo, yflag, yv, gt, qv, make_iseq<14>{});
    STAMP(10);
  }
  LGKM_BAR();
  {
#pragma unroll
    for (int r = 0; r < 2; ++r) {
      const int s = 2 * sp + r;
      f32x4 b0 = {0, 0, 0, 0}, b1 = {0, 0, 0, 0};
#pragma unroll
      for (int ww = 0; ww < 4; ++ww) {
        b0 += *(const LAS f32x4*)(lds0 + ww * 16384 + s * 256 + (((2 * cc) ^ (s & 15)) << 4));
        b1 += *(const LAS f32x4*)(lds0 + ww * 16384 + s * 256 + (((2 * cc + 1) ^ (s & 15)) << 4));
      }
      const float g = __uint_as_float(gt[r]);
      u32x4 o;
#pragma unroll
      for (int e = 0; e < 4; ++e) {
        float bl = e < 2 ? b0[2 * e] : b1[2 * e - 4], bh = e < 2 ? b0[2 * e + 1] : b1[2 * e - 3];
        float yl = r ? bfhi(yv[2 * e]) : bflo(yv[2 * e]), yh = r ? bfhi(yv[2 * e + 1]) : bflo(yv[2 * e + 1]);
        float al = bflo(qv[r][e]) * yl, ah = bfhi(qv[r][e]) * yh;
        o[e] = cvtpk(g * al + (1.f - g) * bl, g * ah + (1.f - g) * bh);
      }
      *(u32x4*)(p.out + (size_t)(s0 + s) * D_ + d0 + cc * 8) = o;
    }
  }
  STAMP(11);
  STAMP_FLUSH(12);
}

extern "C" void kernel_launch(void* const* d_in, const int* in_sizes, int n_in, void* d_out, int out_size, void* d_ws, size_t ws_size, hipStream_t stream) {
  Params p;
  p.q = (const u16*)d_in[0]; p.k = (const u16*)d_in[1]; p.v = (const u16*)d_in[2];
  p.shk = (const u16*)d_in[3]; p.hk = (const u16*)d_in[4]; p.Dv = (const u16*)d_in[5];
  p.wg = (const u16*)d_in[7]; p.wu = (const u16*)d_in[8]; p.wd = (const u16*)d_in[9]; p.gw = (const u16*)d_in[10];
  p.out = (u16*)d_out; p.ws = (char*)d_ws;
  k_fused<<<256, 256, 0, stream>>>(p);
}
```

```cpp
#include <hip/hip_runtime.h>
#include <hip/hip_bf16.h>
#include <utility>
#define TLB_TOUCH
#pragma clang diagnostic ignored "-Winline-asm"
#pragma clang diagnostic ignored "-Wint-to-pointer-cast"

#define DI __device__ __forceinline__
#define LAS __attribute__((address_space(3)))
#define GAS __attribute__((address_space(1)))
typedef unsigned short u16;
typedef unsigned long long u64;
typedef float f32x16 __attribute__((ext_vector_type(16)));
typedef float f32x4 __attribute__((ext_vector_type(4)));
typedef float f32x2 __attribute__((ext_vector_type(2)));
typedef __bf16 bf16x8 __attribute__((ext_vector_type(8)));
typedef __bf16 bf16x2 __attribute__((ext_vector_type(2)));
typedef short s16x4 __attribute__((ext_vector_type(4)));
typedef short s16x8 __attribute__((ext_vector_type(8)));
typedef unsigned u32x4 __attribute__((ext_vector_type(4)));
typedef unsigned u32x2 __attribute__((ext_vector_type(2)));
typedef __amdgpu_buffer_rsrc_t rsrc_t;
template <int... Is> using iseq = std::integer_sequence<int, Is...>;
template <int N> using make_iseq = std::make_integer_sequence<int, N>;

constexpr int S_ = 2048, D_ = 512, F_ = 1792;
constexpr unsigned WS_E = 0, WS_FS = 256, WS_FH = 4096, WS_FY = 8192, WS_TMO = 12288;
constexpr unsigned WS_G   = 16384;
constexpr unsigned WS_KVT = 1u << 20;
constexpr unsigned WS_Y2T = WS_KVT + (2u << 20);
constexpr unsigned WS_WDT = WS_Y2T + (2u << 20);
constexpr unsigned WS_H   = WS_WDT + (2u << 20);
constexpr unsigned WS_END = WS_H + 128u * 56 * 1024;

constexpr unsigned G1_XS = 16384, G1_WIMG = 14336 + 128, G1_WS = 28800, G1_XOFF = 0, G1_WOFF = 3 * G1_XS;
constexpr unsigned ST_K = G1_WOFF + 3 * G1_WS, ST_V = ST_K + 9216, ST_SH = ST_V + 8192, ST_WD = ST_SH + 1024, ST_T = 2 * G1_XS;
constexpr unsigned CV_HS = 4160, CV_H = 4 * CV_HS, CV_X = 5120, CV_CH = CV_H + CV_X, CV_BASE = G1_WOFF + 2 * G1_WS, CV_RED = 0;
constexpr unsigned CV_Z = CV_BASE + 2 * CV_CH;
constexpr unsigned ST_DUMMY = ST_WD + 8192, LDS_BYTES = ST_DUMMY + 256;
static_assert(CV_Z + 2 * 1088 <= LDS_BYTES && LDS_BYTES <= 163840, "lds");

#define SB() __builtin_amdgcn_sched_barrier(0)
#define STAMP_DECL
#define STAMP(i)
#define DSTAMP(i)
#define STAMP_FLUSH(n)

DI unsigned rfl(unsigned x) { return __builtin_amdgcn_readfirstlane(x); }
DI rsrc_t mk_rsrc(const void* p, unsigned bytes) { return __builtin_amdgcn_make_buffer_rsrc((void*)p, 0, bytes, 0x00020000); }
DI unsigned cvtpk(float lo, float hi) { f32x2 v = {lo, hi}; bf16x2 b = __builtin_convertvector(v, bf16x2); return __builtin_bit_cast(unsigned, b); }
DI float bflo(unsigned x) { return __uint_as_float(x << 16); }
DI float bfhi(unsigned x) { return __uint_as_float(x & 0xffff0000u); }
DI void dma16(rsrc_t r, unsigned voff, unsigned soff, unsigned ldsaddr) {
  asm volatile("s_mov_b32 m0, %0\n\ts_nop 0\n\tbuffer_load_dwordx4 %1, %2, %3 offen lds" :: "s"(ldsaddr), "v"(voff), "s"(r), "s"(soff) : "m0", "memory");
}
DI void m0_set(unsigned v) { asm volatile("s_mov_b32 m0, %0" :: "s"(v) : "m0", "memory"); }
template <int OFF> DI void dma_go(rsrc_t r, unsigned voff, unsigned soff) {
  asm volatile("buffer_load_dwordx4 %0, %1, %2 offen offset:%3 lds" :: "v"(voff), "s"(r), "s"(soff), "i"(OFF) : "memory");
}
#define WAIT_LGKM0() __builtin_amdgcn_s_waitcnt(0xC07F)
#define WAIT_VM(n) asm volatile("s_waitcnt vmcnt(" #n ")" ::: "memory")
#define WAIT_VM_BAR(n) asm volatile("s_waitcnt vmcnt(" #n ") lgkmcnt(0)\n\ts_barrier" ::: "memory")
#define LGKM_BAR() asm volatile("s_waitcnt lgkmcnt(0)\n\ts_barrier" ::: "memory")
DI bf16x8 lds_b128(unsigned a) { return *(const LAS bf16x8*)a; }
DI u32x4 lds_u128(unsigned a) { return *(const LAS u32x4*)a; }
DI bf16x8 lds_tr2(unsigned a0, unsigned a1) {
  s16x4 lo = __builtin_amdgcn_ds_read_tr16_b64_v4i16((LAS s16x4*)a0);
  s16x4 hi = __builtin_amdgcn_ds_read_tr16_b64_v4i16((LAS s16x4*)a1);
  s16x8 v = __builtin_shufflevector(lo, hi, 0, 1, 2, 3, 4, 5, 6, 7);
  return __builtin_bit_cast(bf16x8, v);
}
DI void mfma_vvv(f32x4& c, const bf16x8& a, const bf16x8& b) { asm volatile("v_mfma_f32_16x16x32_bf16 %0, %1, %2, %0" : "+v"(c) : "v"(a), "v"(b)); }
DI void mfma_vvv0(f32x4& c, const bf16x8& a, const bf16x8& b) { asm volatile("v_mfma_f32_16x16x32_bf16 %0, %1, %2, 0" : "=&v"(c) : "v"(a), "v"(b)); }
DI void mfma32(f32x16& c, const bf16x8& a, const bf16x8& b) { asm volatile("v_mfma_f32_32x32x16_bf16 %0, %1, %2, %0" : "+v"(c) : "v"(a), "v"(b)); }
DI void mfma_vav(f32x4& c, const u32x4& a, const u32x4& b) { asm volatile("v_mfma_f32_16x16x32_bf16 %0, %1, %2, %0" : "+v"(c) : "a"(a), "v"(b)); }
DI void mfma_vav0(f32x4& c, const u32x4& a, const u32x4& b) { asm volatile("v_mfma_f32_16x16x32_bf16 %0, %1, %2, 0" : "=&v"(c) : "a"(a), "v"(b)); }
#define MFMA_DRAIN() do { SB(); asm volatile("s_nop 7\n\ts_nop 4" ::: "memory"); SB(); } while (0)
constexpr int SC1 = 16;
DI u32x4 gld128(rsrc_t r, unsigned voff, unsigned soff) { return __builtin_amdgcn_raw_buffer_load_b128(r, voff, soff, SC1); }
DI unsigned gld32(rsrc_t r, unsigned voff, unsigned soff) { return __builtin_amdgcn_raw_buffer_load_b32(r, voff, soff, SC1); }
DI void gst128(u32x4 v, rsrc_t r, unsigned voff, unsigned soff) { __builtin_amdgcn_raw_buffer_store_b128(v, r, voff, soff, SC1); }
DI void gst64(u32x2 v, rsrc_t r, unsigned voff, unsigned soff) { __builtin_amdgcn_raw_buffer_store_b64(v, r, voff, soff, SC1); }
DI void gst32(unsigned v, rsrc_t r, unsigned voff, unsigned soff) { __builtin_amdgcn_raw_buffer_store_b32(v, r, voff, soff, SC1); }
#define RLX_AGENT __ATOMIC_RELAXED, __HIP_MEMORY_SCOPE_AGENT
DI u64 flag_ld(const char* a) { return __hip_atomic_load((const GAS u64*)a, RLX_AGENT); }
DI void flag_st(char* a, u64 v) { __hip_atomic_store((GAS u64*)a, v, RLX_AGENT); }
template <int N> DI void wait_flags(const char* base, u64 epoch, char* tmo, int lane) {
  for (unsigned spins = 0;; ++spins) {
    bool ok = true;
#pragma unroll
    for (int i = 0; i < (N + 63) / 64; ++i) { const int idx = i * 64 + lane; const u64 v = flag_ld(base + (idx < N ? idx : 0) * 8); ok &= (v == epoch); }
    if (__all(ok)) break;
    if (spins > 30000u) { if (lane == 0) flag_st(tmo, 1); break; }
    __builtin_amdgcn_s_sleep(1);
  }
  __builtin_amdgcn_fence(__ATOMIC_ACQUIRE, "wavefront");
}
DI f32x4 selv(unsigned m, f32x4 a, f32x4 b) {
  f32x4 r;
#pragma unroll
  for (int e = 0; e < 4; ++e) r[e] = __uint_as_float((__float_as_uint(a[e]) & m) | (__float_as_uint(b[e]) & ~m));
  return r;
}
DI unsigned gelu_mul2(f32x2 g, f32x2 u) {
  const f32x2 c1 = {-2.302208198f, -2.302208198f}, c2 = {-0.1029432397f, -0.1029432397f}, one = {1.f, 1.f};
  f32x2 a = g * __builtin_elementwise_fma(g * g, c2, c1);
  f32x2 e = {__builtin_amdgcn_exp2f(a[0]), __builtin_amdgcn_exp2f(a[1])};
  f32x2 d = e + one;
  f32x2 r = {__builtin_amdgcn_rcpf(d[0]), __builtin_amdgcn_rcpf(d[1])};
  f32x2 h = (g * u) * r;
  return cvtpk(h[0], h[1]);
}

DI float wave_sum(float a) {
  a += __builtin_bit_cast(float, __builtin_amdgcn_update_dpp(0, __builtin_bit_cast(int, a), 0xB1, 0xf, 0xf, true));
  a += __builtin_bit_cast(float, __builtin_amdgcn_update_dpp(0, __builtin_bit_cast(int, a), 0x4E, 0xf, 0xf, true));
  a += __builtin_bit_cast(float, __builtin_amdgcn_update_dpp(0, __builtin_bit_cast(int, a), 0x141, 0xf, 0xf, true));
  a += __builtin_bit_cast(float, __builtin_amdgcn_update_dpp(0, __builtin_bit_cast(int, a), 0x140, 0xf, 0xf, true));
  const int x = __builtin_bit_cast(int, a);
  return (__builtin_bit_cast(float, __builtin_amdgcn_readlane(x, 0)) + __builtin_bit_cast(float, __builtin_amdgcn_readlane(x, 16))) +
         (__builtin_bit_cast(float, __builtin_amdgcn_readlane(x, 32)) + __builtin_bit_cast(float, __builtin_amdgcn_readlane(x, 48)));
}
DI float dot2(unsigned a, unsigned b, float c) { return __builtin_amdgcn_fdot2_f32_bf16(__builtin_bit_cast(bf16x2, a), __builtin_bit_cast(bf16x2, b), c, false); }
struct Params { const u16 *q, *k, *v, *shk, *hk, *Dv, *wg, *wu, *wd, *gw; u16* out; char* ws; };

struct G1 {
  rsrc_t xs, wsr;
  unsigned xv[4], wv[7];
  unsigned sx, sw;
  unsigned mx, mw;
  unsigned xm0, wm0, slot;
  unsigned dx, dw;
  unsigned xa[4], xb[4], wt, w6;
  f32x16 acc[3][2], acc6;
  bf16x8 XA[2], XB[2], W[2][4];
};
template <int N> DI void g1_go(G1& c) {
  if constexpr (N < 4) dma_go<N * 1024>(c.xs, c.xv[N], c.sx);
  else dma_go<(N < 8 ? N - 4 : N - 8) * 1024>(c.wsr, c.wv[N - 4], c.sw);
}
template <int N> DI void g1_dma(G1& c, unsigned st, unsigned slot) {
  const unsigned m = N < 4 ? c.xm0 + slot * G1_XS : c.wm0 + slot * G1_WS + (N >= 8 ? 4096 : 0);
  asm volatile("s_mov_b32 m0, %0\n\ts_nop 0" :: "s"(m) : "m0", "memory");
  if constexpr (N < 4) dma_go<N * 1024>(c.xs, c.xv[N], c.sx + st * 128);
  else dma_go<(N < 8 ? N - 4 : N - 8) * 1024>(c.wsr, c.wv[N - 4], c.sw + st * (64 * 3584));
}
template <int... Ns> DI void g1_dma_all(G1& c, unsigned st, unsigned slot, iseq<Ns...>) { (g1_dma<Ns>(c, st, slot), ...); }
template <int KK, int R> DI void g1_read(G1& c) {
  constexpr int set = KK & 1;
  if constexpr (R == 0) c.XA[set] = lds_b128(c.xa[KK]);
  else if constexpr (R == 2) c.XB[set] = lds_b128(c.xb[KK]);
  else {
    constexpr int p = R == 1 ? 0 : R - 2;
    constexpr unsigned o = KK * 16 * 224;
    if constexpr (p < 3) c.W[set][p] = lds_tr2(c.wt + o + p * 32, c.wt + o + p * 32 + 4 * 224);
    else c.W[set][p] = lds_tr2(c.w6 + o, c.w6 + o + 4 * 224);
  }
}
template <int KK, int M> DI void g1_mfma(G1& c) {
  constexpr int set = KK & 1;
  if constexpr (M < 6) mfma32(c.acc[M >> 1][M & 1], c.W[set][M >> 1], (M & 1) ? c.XB[set] : c.XA[set]);
  else mfma32(c.acc6, c.W[set][3], c.XA[set]);
}
struct Tail {
  rsrc_t hs, xr, wsr; unsigned hvoff, hm0, zm0, xvoff, wdso, xlds; const char* sflags; u64 epoch; char* tmo;
  u64 pf[4]; u32x4 xv[3];
};
DI void poll_issue(u64& v, const char* a) { asm volatile("global_load_dwordx2 %0, %1, off sc1" : "=v"(v) : "v"(a) : "memory"); }
template <int I> DI void tail_op(Tail& t, u32x4 (&Wd)[14][4], int lane) {
  if constexpr (I < 4) dma_go<I * 1024>(t.hs, t.hvoff, 0);
  else if constexpr (I == 4) dma_go<0>(t.hs, lane * 16, 0);
  else if constexpr (I < 9) dma_go<(I - 5) * 1024>(t.hs, t.hvoff + 2, 0);
  else if constexpr (I < 11) { }
  else if constexpr (I < 14) t.xv[I - 11] = gld128(t.xr, t.xvoff + (I - 11) * 1024, 0);
  else { constexpr int n = I - 14; Wd[n >> 2][n & 3] = gld128(t.wsr, lane * 16 + (n & 3) * 1024, t.wdso + (n >> 2) * 4096); }
}
constexpr int TAIL_WD = 12;
constexpr int vm_slot(int g) { constexpr int s[11] = {24, 26, 1, 3, 5, 8, 10, 12, 15, 17, 19}; for (int i = 0; i < 11; ++i) if (s[i] == g) return i; return -1; }
template <int MODE, int G> DI void g1_gap(G1& c, Tail& t, u32x4 (&Wd)[14][4], int lane) {
  constexpr int kk = G / 7, m = G % 7, v = vm_slot(G);
  if constexpr (MODE == 5 && G == 0) {
#pragma unroll
    for (int i = 0; i < 4; ++i) poll_issue(t.pf[i], t.sflags + (i * 64 + lane) * 8);
  }
  g1_mfma<kk, m>(c);
  if constexpr (kk < 3) { if constexpr (m < 6) g1_read<kk + 1, m>(c); }
  else if constexpr (MODE != 7) {
    if constexpr (m == 1) {
      SB();
      if constexpr (MODE == 5) asm volatile("s_waitcnt vmcnt(15) lgkmcnt(0)\n\ts_barrier" ::: "memory");
      else if constexpr (MODE == 6) asm volatile("s_waitcnt vmcnt(9) lgkmcnt(0)\n\ts_barrier" : "+v"(t.pf[0]), "+v"(t.pf[1]), "+v"(t.pf[2]), "+v"(t.pf[3]) :: "memory");
      else asm volatile("s_waitcnt vmcnt(11) lgkmcnt(0)\n\ts_barrier" ::: "memory");
      if constexpr (MODE == 6) {
        const bool ok = t.pf[0] == t.epoch && t.pf[1] == t.epoch && t.pf[2] == t.epoch && t.pf[3] == t.epoch;
        if (!__all(ok)) wait_flags<256>(t.sflags, t.epoch, t.tmo, lane);
        __builtin_amdgcn_fence(__ATOMIC_ACQUIRE, "wavefront");
      }
    }
    if constexpr (m == 2) { g1_read<0, 0>(c); g1_read<0, 1>(c); }
    if constexpr (m == 3) { g1_read<0, 2>(c); g1_read<0, 3>(c); }
    if constexpr (m == 4) g1_read<0, 4>(c);
    if constexpr (m == 5) g1_read<0, 5>(c);
  }
  if constexpr (v >= 0) {
    if constexpr (MODE == 0) g1_go<v>(c);
    if constexpr (MODE == 5) { if constexpr (v >= 2) g1_go<v>(c); else tail_op<v>(t, Wd, lane); }
    if constexpr (MODE == 6) { if constexpr (v >= 2) tail_op<v>(t, Wd, lane); else tail_op<11 + v>(t, Wd, lane); }
    if constexpr (MODE == 7) { if constexpr (v >= 2) tail_op<11 + v>(t, Wd, lane); else tail_op<23 + v>(t, Wd, lane); }
  }
  if constexpr (MODE == 7 && G == 22) tail_op<22>(t, Wd, lane);
  if constexpr (MODE == 7 && G == 27) tail_op<25>(t, Wd, lane);
  if constexpr (MODE == 7 && (G == 20 || G == 21 || G == 23)) { constexpr int i = G == 20 ? 0 : G == 21 ? 1 : 2; *(LAS u32x4*)(t.xlds + i * 1024) = t.xv[i]; }
  if constexpr (MODE == 0 || MODE == 5) {
    if constexpr (G == 4) m0_set(c.mw);
    if constexpr (G == 6) c.sx += 128;
    if constexpr (G == 14) m0_set(c.mw + 4096);
    if constexpr (G == 20) c.sw += 64 * 3584;
  }
  if constexpr (MODE == 0 && G == 23) m0_set(c.mx);
  if constexpr (MODE == 5 && G == 23) m0_set(t.hm0);
  if constexpr (MODE == 6 && G == 4) m0_set(t.zm0);
  if constexpr (MODE == 6 && G == 7) m0_set(t.hm0 + CV_HS);
  if constexpr (MODE != 7) {
    if constexpr (G == 9) { c.xa[0] += c.dx; c.xb[0] += c.dx; }
    if constexpr (G == 11) { c.xa[1] += c.dx; c.xb[1] += c.dx; }
    if constexpr (G == 16) { c.xa[2] += c.dx; c.xb[2] += c.dx; }
    if constexpr (G == 21) { c.xa[3] += c.dx; c.xb[3] += c.dx; c.wt += c.dw; c.w6 += c.dw; }
  }
  if constexpr (MODE == 0) {
    if constexpr (G == 25) { c.mw = c.wm0 + c.slot * G1_WS; c.slot = c.slot == 2 ? 0 : c.slot + 1; asm volatile("" : "+s"(c.mw), "+s"(c.slot)); }
    if constexpr (G == 27) {
      c.dx = c.slot == 2 ? 0u - 2 * G1_XS : G1_XS; c.dw = c.slot == 2 ? 0u - 2 * G1_WS : G1_WS; c.mx = c.xm0 + c.slot * G1_XS;
      asm volatile("" : "+s"(c.dx), "+s"(c.dw), "+s"(c.mx));
    }
  }
  SB();
}
template <int MODE, int... Gs> DI void g1_stage(G1& c, Tail& t, u32x4 (&Wd)[14][4], int lane, iseq<Gs...>) { (g1_gap<MODE, Gs>(c, t, Wd, lane), ...); }
template <int... Rs> DI void g1_read0(G1& c, iseq<Rs...>) { (g1_read<0, Rs>(c), ...); }

struct ConvCtx { unsigned abase, bbase, hm; u32x4 a0; const char* hfl; unsigned wdso; int lane; rsrc_t wsr; };
constexpr int cv_al(int m) { int al = 0, n = 8; while (m >= n) { m -= n; --n; ++al; } return al; }
constexpr int cv_be(int m) { int n = 8; while (m >= n) { m -= n; --n; } return m; }
template <int RR, int M> DI void conv_gap(const ConvCtx& cx, f32x4 (&acc)[8], u32x2 (&alo)[2][8], u32x2 (&ahi)[2][8], bf16x8 (&B)[2][8], u32x4 (&Wd)[14][4], u64& hflag) {
  constexpr int set = RR & 1, al = cv_al(M), be = cv_be(M);
  if constexpr (M == 0) WAIT_LGKM0();
  u32x4 av = {alo[set][al][0], alo[set][al][1], ahi[set][al][0], ahi[set][al][1]};
  if constexpr (RR == 0 && al == 0) {
#pragma unroll
    for (int e = 0; e < 4; ++e) av[e] = (av[e] & cx.hm) | (cx.a0[e] & ~cx.hm);
  }
  bf16x8 A = __builtin_bit_cast(bf16x8, av);
  if constexpr (RR == 0 && al == 0) { asm volatile("s_nop 1" : "+v"(A)); mfma_vvv0(acc[be], A, B[set][be]); }
  else mfma_vvv(acc[al + be], A, B[set][be]);
  if constexpr (RR == 3 && M == 1) hflag = flag_ld(cx.hfl);
  if constexpr (M % 5 == 3) {
    constexpr int n = 28 + RR * 7 + M / 5;
    if constexpr (n < 56) Wd[n >> 2][n & 3] = gld128(cx.wsr, cx.lane * 16 + (n & 3) * 1024, cx.wdso + (n >> 2) * 4096);

  } else if constexpr (RR < 3) {
    constexpr int rd = M - (M + 1) / 5;
    if constexpr (rd < 24) {
      constexpr int i = rd / 3, k = rd % 3;
      if constexpr (k == 0) alo[set ^ 1][i] = *(const volatile LAS u32x2*)(cx.abase + (RR + 1) * 64 + i * 512);
      else if constexpr (k == 1) ahi[set ^ 1][i] = *(const volatile LAS u32x2*)(cx.abase + (RR + 1) * 64 + i * 512 + 8);
      else B[set ^ 1][i] = lds_b128(cx.bbase + 64 * (56 - 8 * i) + 64 * (RR + 1));
    }
  }
  SB();
}
template <int RR, int... Ms> DI void conv_rho(const ConvCtx& cx, f32x4 (&acc)[8], u32x2 (&alo)[2][8], u32x2 (&ahi)[2][8], bf16x8 (&B)[2][8], u32x4 (&Wd)[14][4], u64& hflag, iseq<Ms...>) {
  (conv_gap<RR, Ms>(cx, acc, alo, ahi, B, Wd, hflag), ...);
}

struct G2Ctx {
  rsrc_t wsr; const char* yfl; u64 epoch; char* tmo; int lane;
  unsigned yvo, yso, gvo, gso; const u16* qp; unsigned hso, rbase; int gq, li; char* myy;
};
constexpr int G2PD = 3;
template <int I, int M> DI void g2_gap(const G2Ctx& g, f32x4 (&acc)[4][4], u32x4 (&Wd)[14][4], u32x4 (&H)[G2PD + 1][4], const unsigned (&hvo)[4], u64& yflag,
                                       unsigned (&yv)[8], u32x2& gt, u32x4 (&qv)[2]) {
  constexpr int dt = M >> 2, st = M & 3;
  if constexpr (I == 0) mfma_vav0(acc[dt][st], Wd[I][dt], H[I % (G2PD + 1)][st]); else mfma_vav(acc[dt][st], Wd[I][dt], H[I % (G2PD + 1)][st]);
  if constexpr (I + G2PD < 14 && (M & 3) == 1) H[(I + G2PD) % (G2PD + 1)][M >> 2] = gld128(g.wsr, hvo[M >> 2], g.hso + (I + G2PD) * 1024);
  if constexpr (I == 8 && M == 15) yflag = flag_ld(g.yfl + (g.lane & 31) * 8);
  if constexpr (I == 11) {
    if constexpr (M == 0) {
      if (!__all(yflag == g.epoch)) wait_flags<32>(g.yfl, g.epoch, g.tmo, g.lane);
      __builtin_amdgcn_fence(__ATOMIC_ACQUIRE, "wavefront");
    }
    if constexpr ((M & 1) == 1) yv[M >> 1] = gld32(g.wsr, g.yvo + (M >> 1) * 4096, g.yso);
  }
  if constexpr (I == 12) {
    if constexpr (M == 1) gt = __builtin_amdgcn_raw_buffer_load_b64(g.wsr, g.gvo, g.gso, SC1);
    if constexpr (M == 5) qv[0] = *(const u32x4*)g.qp;
    if constexpr (M == 9) qv[1] = *(const u32x4*)(g.qp + D_);
  }
  if constexpr (I == 13 && M >= 9) { constexpr int q = M - 9; *(LAS f32x4*)(g.rbase + (q & 3) * 4096 + (((4 * (q >> 2) + g.gq) ^ g.li) << 4)) = acc[q >> 2][q & 3]; }
  SB();
}
template <int I, int... Ms> DI void g2_step(const G2Ctx& g, f32x4 (&acc)[4][4], u32x4 (&Wd)[14][4], u32x4 (&H)[G2PD + 1][4], const unsigned (&hvo)[4], u64& yflag,
                                            unsigned (&yv)[8], u32x2& gt, u32x4 (&qv)[2], iseq<Ms...>) {
  (g2_gap<I, Ms>(g, acc, Wd, H, hvo, yflag, yv, gt, qv), ...);
  if constexpr (I == 3) {
    WAIT_VM(12); asm volatile("s_barrier" ::: "memory");
    if (threadIdx.x == 0) flag_st(g.myy, g.epoch);
    SB();
  }
  if constexpr (I == 13) {
    MFMA_DRAIN();
#pragma unroll
    for (int q = 7; q < 16; ++q) *(LAS f32x4*)(g.rbase + (q & 3) * 4096 + (((4 * (q >> 2) + g.gq) ^ g.li) << 4)) = acc[q >> 2][q & 3];
  }
}
template <int... Is> DI void g2_loop(const G2Ctx& g, f32x4 (&acc)[4][4], u32x4 (&Wd)[14][4], u32x4 (&H)[G2PD + 1][4], const unsigned (&hvo)[4], u64& yflag,
                                     unsigned (&yv)[8], u32x2& gt, u32x4 (&qv)[2], iseq<Is...>) {
  (g2_step<Is>(g, acc, Wd, H, hvo, yflag, yv, gt, qv, make_iseq<16>{}), ...);
}

__global__ __launch_bounds__(256, 1) void k_fused(Params p) {
  __shared__ __attribute__((aligned(16))) char smem[LDS_BYTES];
  const unsigned lds0 = (unsigned)(size_t)(LAS char*)smem;
  const int lane = threadIdx.x & 63;
  const unsigned w = rfl(threadIdx.x >> 6);
  const unsigned b = blockIdx.x, xcd = b & 7, jb = b >> 3;
  char* const ws = p.ws;
  STAMP_DECL; STAMP(0);
  asm volatile("" :: "s"(p.q), "s"(p.k), "s"(p.v), "s"(p.shk), "s"(p.hk), "s"(p.Dv), "s"(p.wg), "s"(p.wu), "s"(p.wd), "s"(p.gw), "s"(p.out), "s"(p.ws));
  const rsrc_t wsr = mk_rsrc(ws, WS_END);
  u64 eword;
  asm volatile("global_load_dwordx2 %0, %1, off" : "=v"(eword) : "v"(ws + WS_E) : "memory");

  {
    const u64 bases[11] = {(u64)p.q, (u64)p.k, (u64)p.v, (u64)p.hk, (u64)p.out, (u64)p.wg, (u64)p.wu, (u64)p.wd, (u64)p.shk, (u64)p.Dv, (u64)p.gw};
    const unsigned sizes[11] = {S_ * D_ * 2, S_ * D_ * 2, S_ * D_ * 2, S_ * D_ * 2, S_ * D_ * 2, D_ * F_ * 2, D_ * F_ * 2, D_ * F_ * 2, D_ * 8, D_ * 2, D_ * 2};
    u64 a = (u64)ws + 524288;
#pragma unroll
    for (int i = 0; i < 11; ++i) { if ((lane >> 1) == i) a = bases[i] + ((lane & 1) ? sizes[i] - 4 : 0); }
    asm volatile("s_mov_b32 m0, %0\n\ts_nop 0\n\tglobal_load_lds_dword %1, off" :: "s"(lds0 + ST_DUMMY), "v"(a) : "m0", "memory");
  }
  const unsigned sb = b >> 3, db = b & 7, s0p = sb * 64, d0p = db * 64;
  const unsigned kg_w = b >> 2, cq_w = b & 3;
  {
    const unsigned r8 = lane >> 3, c8 = lane & 7;
    const rsrc_t rk = mk_rsrc(p.k, S_ * D_ * 2), rv = mk_rsrc(p.v, S_ * D_ * 2), rw = mk_rsrc(p.wd, F_ * D_ * 2);
    auto dk = [&](int i) { int row = (int)s0p - 3 + 8 * i + (int)r8; unsigned vo = row >= 0 ? (unsigned)row * 1024 + d0p * 2 + c8 * 16 : 0x80000000u; dma16(rk, vo, 0, lds0 + ST_K + i * 1024); };
    auto dv = [&](int i) { dma16(rv, r8 * 1024 + c8 * 16, (s0p + 8 * i) * 1024 + d0p * 2, lds0 + ST_V + i * 1024); };
    auto dw = [&](int i) { dma16(rw, (lane >> 4) * 1024 + (lane & 15) * 16, (kg_w * 32 + 4 * i) * 1024 + cq_w * 256, lds0 + ST_WD + i * 1024); };
    if (w == 0) { dk(0); dk(1); dk(2); dk(3); dk(4); dk(5); dk(6); }
    else if (w == 1) { dk(7); dk(8); dv(0); dv(1); dv(2); dv(3); dv(4); }
    else if (w == 2) { dv(5); dv(6); dv(7); dma16(mk_rsrc(p.shk, D_ * 8), lane * 16, d0p * 8, lds0 + ST_SH); dw(0); dw(1); dw(2); }
    else { dw(3); dw(4); dw(5); dw(6); dw(7); }
  }
  SB();
  const unsigned rb = 8 * (xcd >> 2) + (jb & 7), fc = 4 * (xcd & 3) + (jb >> 3);
  const unsigned mi = w >> 1, ni = w & 1;
  G1 c;
  c.xs = mk_rsrc(p.q, S_ * D_ * 2);
  c.wsr = mk_rsrc((w >> 1) ? p.wu : p.wg, D_ * F_ * 2);
#pragma unroll
  for (int n = 0; n < 4; ++n) c.xv[n] = (lane >> 3) * 1024 + (((lane & 7) ^ ((4 * (n & 1) + (lane >> 4)) & 7)) << 4) + n * (8192 - 1024);
#pragma unroll
  for (int jj = 0; jj < 7; ++jj) {
    const unsigned ci = 64 * (7 * (w & 1) + jj) + lane, k = (ci * 4682u) >> 16, pc = ci - k * 14;
    c.wv[jj] = k * 3584 + pc * 16 - (jj & 3) * 1024;
  }
  c.sx = (rb * 128 + 32 * w) * 1024;
  c.sw = fc * 224;
  c.xm0 = lds0 + G1_XOFF + w * 4096;
  c.wm0 = lds0 + G1_WOFF + (w >> 1) * G1_WIMG + (w & 1) * 7 * 1024;
  g1_dma_all(c, 0, 0, make_iseq<11>{});
  {
    const int r = lane & 31, hh = lane >> 5;
    const unsigned base = r * 128 + ((hh ^ ((r >> 1) & 7)) << 4);
#pragma unroll
    for (int kk = 0; kk < 4; ++kk) {
      c.xa[kk] = lds0 + G1_XOFF + (2 * mi + ni) * 4096 + (base ^ (kk << 5));
      c.xb[kk] = lds0 + G1_XOFF + (2 * mi + (1 - ni)) * 4096 + (base ^ (kk << 5));
    }
    const unsigned wb = lds0 + G1_WOFF + ((lane >> 4) & 1) * G1_WIMG + (8 * hh + ((lane & 15) >> 2)) * 224 + (lane & 3) * 8;
    c.wt = wb + ni * 96; c.w6 = wb + 6 * 32;
  }
  SB();
  STAMP(1);
  asm volatile("s_waitcnt vmcnt(11)\n\ts_barrier" : "+v"(eword) :: "memory");
  const u64 epoch = ((u64)rfl((unsigned)(eword >> 32)) << 32 | rfl((unsigned)eword)) * 6364136223846793005ull + 1442695040888963407ull;
  STAMP(2);
  {
    const int t = threadIdx.x, c4 = t & 15, rg = t >> 4;
    u32x2 kr[7], vr[4]; u32x4 w4[2];
#pragma unroll
    for (int i = 0; i < 7; ++i) kr[i] = *(const LAS u32x2*)(lds0 + ST_K + (4 * rg + i) * 128 + c4 * 8);
#pragma unroll
    for (int i = 0; i < 4; ++i) vr[i] = *(const LAS u32x2*)(lds0 + ST_V + (4 * rg + i) * 128 + c4 * 8);
#pragma unroll
    for (int i = 0; i < 2; ++i) w4[i] = lds_u128(lds0 + ST_SH + c4 * 32 + i * 16);
    const int li = lane & 15, gq = lane >> 4;
    bf16x8 wf[2];
#pragma unroll
    for (int i = 0; i < 2; ++i) { const unsigned a = lds0 + ST_WD + (8 * gq + (li >> 2)) * 256 + (2 * w + i) * 32 + (lane & 3) * 8; wf[i] = lds_tr2(a, a + 4 * 256); }
    g1_dma<0>(c, 1, 1); g1_dma<1>(c, 1, 1);
    SB();
#pragma unroll
    for (int j = 0; j < 4; ++j) {
      const unsigned wa = w4[j >> 1][(j & 1) * 2], wb = w4[j >> 1][(j & 1) * 2 + 1];
      const float w0 = bflo(wa), w1 = bfhi(wa), w2 = bflo(wb), w3 = bfhi(wb);
      float kk[7], o[4];
#pragma unroll
      for (int i = 0; i < 7; ++i) kk[i] = (j & 1) ? bfhi(kr[i][j >> 1]) : bflo(kr[i][j >> 1]);
      if (j == 0) g1_dma<2>(c, 1, 1); if (j == 1) g1_dma<4>(c, 1, 1); if (j == 2) g1_dma<6>(c, 1, 1); if (j == 3) g1_dma<8>(c, 1, 1);
      SB();
#pragma unroll
      for (int i = 0; i < 4; ++i) {
        const float vv = (j & 1) ? bfhi(vr[i][j >> 1]) : bflo(vr[i][j >> 1]);
        o[i] = (kk[i] * w3 + kk[i + 1] * w2 + kk[i + 2] * w1 + kk[i + 3] * w0) * vv;
      }
      const int wd = 30 - 2 * rg, dl = 4 * c4 + j;
      *(LAS u32x2*)(lds0 + ST_T + dl * 128 + ((((wd >> 2) ^ c4) & 7) << 4) + (wd & 3) * 4) = u32x2{cvtpk(o[3], o[2]), cvtpk(o[1], o[0])};
      if (j == 0) g1_dma<3>(c, 1, 1); if (j == 1) g1_dma<5>(c, 1, 1); if (j == 2) g1_dma<7>(c, 1, 1); if (j == 3) g1_dma<9>(c, 1, 1);
      SB();
    }
    g1_dma<10>(c, 1, 1);
    SB();
    LGKM_BAR();
    u32x4 val[2];
#pragma unroll
    for (int i = 0; i < 2; ++i) { const int dl = (w * 2 + i) * 8 + (lane >> 3), ch = lane & 7; val[i] = lds_u128(lds0 + ST_T + dl * 128 + ((ch ^ (dl >> 2)) & 7) * 16); }
#pragma unroll
    for (int i = 0; i < 2; ++i) {
      const unsigned D16 = 8 * cq_w + 2 * w + i;
      gst128(__builtin_bit_cast(u32x4, wf[i]), wsr, kg_w < 56 ? WS_WDT + (((D16 >> 2) * 56 + kg_w) * 4 + (D16 & 3)) * 1024 + lane * 16 : 0x80000000u, 0);
    }
#pragma unroll
    for (int i = 0; i < 2; ++i) {
      const int dl = (w * 2 + i) * 8 + (lane >> 3), ch = lane & 7;
      gst128(val[i], wsr, WS_KVT + (d0p + dl) * 4096 + (1984 - s0p) * 2 + ch * 16, 0);
    }
    SB();
  }
  STAMP(3);
  asm volatile("s_waitcnt vmcnt(15) lgkmcnt(0)\n\ts_barrier" ::: "memory");
  g1_read0(c, make_iseq<6>{});
  g1_dma<0>(c, 2, 2); g1_dma<1>(c, 2, 2);
  c.sx += 2 * 128; c.sw += 2 * (64 * 3584);
  c.slot = 0; c.mx = c.xm0; c.mw = c.wm0 + 2 * G1_WS; c.dx = G1_XS; c.dw = G1_WS;
#pragma unroll
  for (int i = 0; i < 3; ++i) { c.acc[i][0] = f32x16{}; c.acc[i][1] = f32x16{}; }
  c.acc6 = f32x16{};
  SB();
  asm volatile("s_nop 1" ::: "memory");
  SB();
  STAMP(4);
  const unsigned cch = w >> 1, hf = w & 1, dch = b * 2 + cch, cvb = lds0 + CV_BASE + cch * CV_CH, cvz = lds0 + CV_Z + cch * 1088;
  const unsigned rb2 = 4 * xcd + (jb & 3), cb = jb >> 2;
  u32x4 Wd[14][4];
  Tail t;
  t.hs = mk_rsrc(p.hk + (size_t)dch * S_, (D_ - dch) * S_ * 2);
  t.hvoff = lane * 16 + (2 * hf + 17) * 2; t.hm0 = cvb + 2 * hf * CV_HS; t.zm0 = cvz + 32;
  t.xr = mk_rsrc(ws + WS_KVT + dch * 4096, 4096); t.xvoff = (hf * 128 + lane) * 16; t.xlds = cvb + CV_H + (hf * 128 + lane) * 16;
  t.wsr = wsr; t.wdso = WS_WDT + (cb * 56 + 14 * w) * 4096;
  t.sflags = ws + WS_FS; t.epoch = epoch; t.tmo = ws + WS_TMO;
  {
#pragma unroll 1
    for (unsigned s = 0; s < 5; ++s) {
      DSTAMP(s);
      g1_stage<0>(c, t, Wd, lane, make_iseq<28>{});
      if (s == 0 && threadIdx.x == 0) flag_st(ws + WS_FS + b * 8, epoch);
    }
    DSTAMP(5);
    g1_stage<5>(c, t, Wd, lane, make_iseq<28>{});
    if (lane < 8) *(LAS unsigned*)(cvz + lane * 4) = 0;
    c.dx = G1_XS; c.dw = G1_WS;
    DSTAMP(6);
    g1_stage<6>(c, t, Wd, lane, make_iseq<28>{});
    if (b == 0 && threadIdx.x == 0) flag_st(ws + WS_E, epoch);
    DSTAMP(7);
    g1_stage<7>(c, t, Wd, lane, make_iseq<28>{});
    DSTAMP(8);
  }
  MFMA_DRAIN();
  STAMP(5);
  auto ldWd = [&](int n) { Wd[n >> 2][n & 3] = gld128(wsr, lane * 16 + (n & 3) * 1024, t.wdso + (n >> 2) * 4096); };
  {
    const unsigned grow = 128 * rb + 8 * fc + 2 * w;
    const u32x4 gx0 = *(const u32x4*)(p.q + (size_t)grow * D_ + lane * 8), gx1 = *(const u32x4*)(p.q + (size_t)(grow + 1) * D_ + lane * 8), gw4 = *(const u32x4*)(p.gw + lane * 8);
    SB();
    const int s32 = lane & 31, hh = lane >> 5;
    const unsigned lo = (s32 >> 4) * 57344 + (s32 & 15) * 16 + 8 * hh;
    const unsigned hb = WS_H + rb * 8 * 57344 + fc * 7 * 512;
    const unsigned bA = hb + (2 * mi + ni) * 2 * 57344, bB = hb + (2 * mi + (1 - ni)) * 2 * 57344, t3 = 3 * ni * 512;
    const unsigned sA = bA + t3, sB = bB + t3, s6 = bA + 6 * 512;
    auto put = [&](int n0, int nl, const f32x16& a, unsigned so, int imm) {
      if (nl > 0) ldWd(n0);
      u32x2 o0 = {gelu_mul2(f32x2{a[0], a[1]}, f32x2{a[8], a[9]}), gelu_mul2(f32x2{a[2], a[3]}, f32x2{a[10], a[11]})};
      gst64(o0, wsr, lo + imm, so);
      SB();
      if (nl > 1) ldWd(n0 + 1);
      u32x2 o1 = {gelu_mul2(f32x2{a[4], a[5]}, f32x2{a[12], a[13]}), gelu_mul2(f32x2{a[6], a[7]}, f32x2{a[14], a[15]})};
      if (nl > 2) ldWd(n0 + 2);
      gst64(o1, wsr, lo + imm + 256, so);
      SB();
    };
    put(12, 3, c.acc[0][0], sA, 0); put(15, 3, c.acc[0][1], sB, 0);
    put(18, 2, c.acc[1][0], sA, 512); put(20, 2, c.acc[1][1], sB, 512);
    put(22, 2, c.acc[2][0], sA, 1024); put(24, 2, c.acc[2][1], sB, 1024);
    put(26, 2, c.acc6, s6, 0);
    float gsum[2] = {0.f, 0.f};
#pragma unroll
    for (int j = 0; j < 4; ++j) { gsum[0] = dot2(gx0[j], gw4[j], gsum[0]); gsum[1] = dot2(gx1[j], gw4[j], gsum[1]); }
#pragma unroll
    for (int r = 0; r < 2; ++r) gsum[r] = __builtin_amdgcn_rcpf(1.f + __builtin_amdgcn_exp2f(-1.4426950409f * wave_sum(gsum[r])));
    gst32(__float_as_uint(lane & 1 ? gsum[1] : gsum[0]), wsr, lane < 2 ? WS_G + (grow + lane) * 4 : 0x80000000u, 0);
    SB();
  }
  LGKM_BAR();
  STAMP(6);

  constexpr int PD = 3;
  u32x4 H[PD + 1][4];
  const unsigned hso = WS_H + ((4 * rb2) * 56 + 14 * w) * 1024;
  unsigned hvo[4];
#pragma unroll
  for (int st = 0; st < 4; ++st) hvo[st] = lane * 16 + st * 57344;
  auto ldH1 = [&](int i, int st) { H[i % (PD + 1)][st] = gld128(wsr, hvo[st], hso + i * 1024); };
  auto ldH = [&](int i) { ldH1(i, 0); ldH1(i, 1); ldH1(i, 2); ldH1(i, 3); };
  {
    const int li = lane & 15, gq = lane >> 4;
    u32x4 a0;
#pragma unroll
    for (int e = 0; e < 4; ++e) {
      const unsigned o = cvz + (li + 8 * gq + 2 * e + 1) * 2;
      a0[e] = (unsigned)*(const LAS u16*)o | ((unsigned)*(const LAS u16*)(o + 2) << 16);
    }
    const float Dd = __uint_as_float((unsigned)p.Dv[dch] << 16);
    const unsigned abase = cvb + (li & 3) * CV_HS + (li >> 2) * 8 + gq * 16 + hf * 4 * 64 - 64;
    const unsigned bbase = cvb + CV_H + (254 - 2 * li + gq) * 16 - 64 * 56 + hf * 4 * 64;
    f32x4 acc[8];
    u32x2 alo[2][8], ahi[2][8];
    bf16x8 B[2][8];
#pragma unroll
    for (int i = 0; i < 8; ++i) {
      alo[0][i] = *(const volatile LAS u32x2*)(abase + i * 512);
      ahi[0][i] = *(const volatile LAS u32x2*)(abase + i * 512 + 8);
      B[0][i] = lds_b128(bbase + 64 * (56 - 8 * i));
    }
    SB();
    STAMP(7);
    u64 hflag = 0;
    ConvCtx cx{abase, bbase, 0u - hf, a0, ws + WS_FH + (rb2 >> 1) * 128 + li * 8, t.wdso, lane, wsr};
    DSTAMP(9);
    conv_rho<0>(cx, acc, alo, ahi, B, Wd, hflag, make_iseq<36>{});
    DSTAMP(10);
    WAIT_VM(7); asm volatile("s_barrier" ::: "memory");
    if (threadIdx.x == 0) flag_st(ws + WS_FH + rb * 128 + fc * 8, epoch);
    SB();
    DSTAMP(11);
    conv_rho<1>(cx, acc, alo, ahi, B, Wd, hflag, make_iseq<36>{});
    conv_rho<2>(cx, acc, alo, ahi, B, Wd, hflag, make_iseq<36>{});
    conv_rho<3>(cx, acc, alo, ahi, B, Wd, hflag, make_iseq<36>{});
    DSTAMP(12);
    STAMP(8);
    if (!__all(hflag == epoch)) wait_flags<16>(ws + WS_FH + (rb2 >> 1) * 128, epoch, ws + WS_TMO, lane);
    __builtin_amdgcn_fence(__ATOMIC_ACQUIRE, "wavefront");
    DSTAMP(13);
    ldH(0);
    MFMA_DRAIN();
    const unsigned red = lds0 + CV_RED, hm = 0u - hf;
#pragma unroll
    for (int g = 0; g < 4; ++g) *(LAS f32x4*)(red + w * 4096 + g * 1024 + lane * 16) = selv(hm, acc[g], acc[4 + g]);
    SB();
    ldH(1);
    SB();
    DSTAMP(14);
    LGKM_BAR();
    DSTAMP(15);
    f32x4 oth[4]; u32x2 kvr[4];
#pragma unroll
    for (int g = 0; g < 4; ++g) {
      oth[g] = *(const LAS f32x4*)(red + (w ^ 1) * 4096 + g * 1024 + lane * 16);
      kvr[g] = *(const LAS u32x2*)(cvb + CV_H + (2044 - (256 * (g + 4 * hf) + 16 * li + 4 * gq)) * 2);
    }
    SB();
    ldH(2);
    SB();
#pragma unroll
    for (int g = 0; g < 4; ++g) {
      f32x4 y = selv(hm, acc[4 + g], acc[g]) + oth[g];
      float r0 = y[0] + bfhi(kvr[g][1]) * Dd, r1 = y[1] + bflo(kvr[g][1]) * Dd, r2 = y[2] + bfhi(kvr[g][0]) * Dd, r3 = y[3] + bflo(kvr[g][0]) * Dd;
      gst64(u32x2{cvtpk(r0, r1), cvtpk(r2, r3)}, wsr, (16 * li + 4 * gq) * 2 + g * 512, WS_Y2T + dch * 4096 + hf * 2048);
    }
    SB();
  }
  STAMP(9);

  const int tt = threadIdx.x, cc = tt & 7, sp = tt >> 3;
  const unsigned s0 = rb2 * 64, d0 = cb * 64;
  unsigned yv[8]; u32x2 gt; u32x4 qv[2];
  {
    f32x4 acc[4][4];
    u64 yflag = 0;
    const int li = lane & 15, gq = lane >> 4;
    const unsigned rbase = lds0 + w * 16384 + li * 256;
    G2Ctx g2{wsr, ws + WS_FY + cb * 256, epoch, ws + WS_TMO, lane, (unsigned)(8 * cc * 4096 + sp * 4), WS_Y2T + d0 * 4096 + s0 * 2, (unsigned)(sp * 8), WS_G + s0 * 4,
             p.q + (size_t)(s0 + 2 * sp) * D_ + d0 + cc * 8, hso, rbase, gq, li, ws + WS_FY + (b >> 5) * 256 + (b & 31) * 8};
    g2_loop(g2, acc, Wd, H, hvo, yflag, yv, gt, qv, make_iseq<14>{});
    STAMP(10);
  }
  LGKM_BAR();
  {
#pragma unroll
    for (int r = 0; r < 2; ++r) {
      const int s = 2 * sp + r;
      f32x4 b0 = {0, 0, 0, 0}, b1 = {0, 0, 0, 0};
#pragma unroll
      for (int ww = 0; ww < 4; ++ww) {
        b0 += *(const LAS f32x4*)(lds0 + ww * 16384 + s * 256 + (((2 * cc) ^ (s & 15)) << 4));
        b1 += *(const LAS f32x4*)(lds0 + ww * 16384 + s * 256 + (((2 * cc + 1) ^ (s & 15)) << 4));
      }
      const float g = __uint_as_float(gt[r]);
      u32x4 o;
#pragma unroll
      for (int e = 0; e < 4; ++e) {
        float bl = e < 2 ? b0[2 * e] : b1[2 * e - 4], bh = e < 2 ? b0[2 * e + 1] : b1[2 * e - 3];
        float yl = r ? bfhi(yv[2 * e]) : bflo(yv[2 * e]), yh = r ? bfhi(yv[2 * e + 1]) : bflo(yv[2 * e + 1]);
        float al = bflo(qv[r][e]) * yl, ah = bfhi(qv[r][e]) * yh;
        o[e] = cvtpk(g * al + (1.f - g) * bl, g * ah + (1.f - g) * bh);
      }
      *(u32x4*)(p.out + (size_t)(s0 + s) * D_ + d0 + cc * 8) = o;
    }
  }
  STAMP(11);
  STAMP_FLUSH(12);
}

extern "C" void kernel_launch(void* const* d_in, const int* in_sizes, int n_in, void* d_out, int out_size, void* d_ws, size_t ws_size, hipStream_t stream) {
  Params p;
  p.q = (const u16*)d_in[0]; p.k = (const u16*)d_in[1]; p.v = (const u16*)d_in[2];
  p.shk = (const u16*)d_in[3]; p.hk = (const u16*)d_in[4]; p.Dv = (const u16*)d_in[5];
  p.wg = (const u16*)d_in[7]; p.wu = (const u16*)d_in[8]; p.wd = (const u16*)d_in[9]; p.gw = (const u16*)d_in[10];
  p.out = (u16*)d_out; p.ws = (char*)d_ws;
  k_fused<<<256, 256, 0, stream>>>(p);
}
```

```cpp
#include <hip/hip_runtime.h>
#include <hip/hip_bf16.h>
#include <utility>
#define OUT_AUX 17
#pragma clang diagnostic ignored "-Winline-asm"
#pragma clang diagnostic ignored "-Wint-to-pointer-cast"

#define DI __device__ __forceinline__
#define LAS __attribute__((address_space(3)))
#define GAS __attribute__((address_space(1)))
typedef unsigned short u16;
typedef unsigned long long u64;
typedef float f32x16 __attribute__((ext_vector_type(16)));
typedef float f32x4 __attribute__((ext_vector_type(4)));
typedef float f32x2 __attribute__((ext_vector_type(2)));
typedef __bf16 bf16x8 __attribute__((ext_vector_type(8)));
typedef __bf16 bf16x2 __attribute__((ext_vector_type(2)));
typedef short s16x4 __attribute__((ext_vector_type(4)));
typedef short s16x8 __attribute__((ext_vector_type(8)));
typedef unsigned u32x4 __attribute__((ext_vector_type(4)));
typedef unsigned u32x2 __attribute__((ext_vector_type(2)));
typedef __amdgpu_buffer_rsrc_t rsrc_t;
template <int... Is> using iseq = std::integer_sequence<int, Is...>;
template <int N> using make_iseq = std::make_integer_sequence<int, N>;

constexpr int S_ = 2048, D_ = 512, F_ = 1792;
constexpr unsigned WS_E = 0, WS_FS = 256, WS_FH = 4096, WS_FY = 8192, WS_TMO = 12288;
constexpr unsigned WS_G   = 16384;
constexpr unsigned WS_KVT = 1u << 20;
constexpr unsigned WS_Y2T = WS_KVT + (2u << 20);
constexpr unsigned WS_WDT = WS_Y2T + (2u << 20);
constexpr unsigned WS_H   = WS_WDT + (2u << 20);
constexpr unsigned WS_END = WS_H + 128u * 56 * 1024;

constexpr unsigned G1_XS = 16384, G1_WIMG = 14336 + 128, G1_WS = 28800, G1_XOFF = 0, G1_WOFF = 3 * G1_XS;
constexpr unsigned ST_K = G1_WOFF + 3 * G1_WS, ST_V = ST_K + 9216, ST_SH = ST_V + 8192, ST_WD = ST_SH + 1024, ST_T = 2 * G1_XS;
constexpr unsigned CV_HS = 4160, CV_H = 4 * CV_HS, CV_X = 5120, CV_CH = CV_H + CV_X, CV_BASE = G1_WOFF + 2 * G1_WS, CV_RED = 0;
constexpr unsigned CV_Z = CV_BASE + 2 * CV_CH;
constexpr unsigned LDS_BYTES = ST_WD + 8192;
static_assert(CV_Z + 2 * 1088 <= LDS_BYTES && LDS_BYTES <= 163840, "lds");

#define SB() __builtin_amdgcn_sched_barrier(0)
#define STAMP_DECL
#define STAMP(i)
#define DSTAMP(i)
#define STAMP_FLUSH(n)

DI unsigned rfl(unsigned x) { return __builtin_amdgcn_readfirstlane(x); }
DI rsrc_t mk_rsrc(const void* p, unsigned bytes) { return __builtin_amdgcn_make_buffer_rsrc((void*)p, 0, bytes, 0x00020000); }
DI unsigned cvtpk(float lo, float hi) { f32x2 v = {lo, hi}; bf16x2 b = __builtin_convertvector(v, bf16x2); return __builtin_bit_cast(unsigned, b); }
DI float bflo(unsigned x) { return __uint_as_float(x << 16); }
DI float bfhi(unsigned x) { return __uint_as_float(x & 0xffff0000u); }
DI void dma16(rsrc_t r, unsigned voff, unsigned soff, unsigned ldsaddr) {
  asm volatile("s_mov_b32 m0, %0\n\ts_nop 0\n\tbuffer_load_dwordx4 %1, %2, %3 offen lds" :: "s"(ldsaddr), "v"(voff), "s"(r), "s"(soff) : "m0", "memory");
}
DI void m0_set(unsigned v) { asm volatile("s_mov_b32 m0, %0" :: "s"(v) : "m0", "memory"); }
template <int OFF> DI void dma_go(rsrc_t r, unsigned voff, unsigned soff) {
  asm volatile("buffer_load_dwordx4 %0, %1, %2 offen offset:%3 lds" :: "v"(voff), "s"(r), "s"(soff), "i"(OFF) : "memory");
}
#define WAIT_LGKM0() __builtin_amdgcn_s_waitcnt(0xC07F)
#define WAIT_VM(n) asm volatile("s_waitcnt vmcnt(" #n ")" ::: "memory")
#define WAIT_VM_BAR(n) asm volatile("s_waitcnt vmcnt(" #n ") lgkmcnt(0)\n\ts_barrier" ::: "memory")
#define LGKM_BAR() asm volatile("s_waitcnt lgkmcnt(0)\n\ts_barrier" ::: "memory")
DI bf16x8 lds_b128(unsigned a) { return *(const LAS bf16x8*)a; }
DI u32x4 lds_u128(unsigned a) { return *(const LAS u32x4*)a; }
DI bf16x8 lds_tr2(unsigned a0, unsigned a1) {
  s16x4 lo = __builtin_amdgcn_ds_read_tr16_b64_v4i16((LAS s16x4*)a0);
  s16x4 hi = __builtin_amdgcn_ds_read_tr16_b64_v4i16((LAS s16x4*)a1);
  s16x8 v = __builtin_shufflevector(lo, hi, 0, 1, 2, 3, 4, 5, 6, 7);
  return __builtin_bit_cast(bf16x8, v);
}
DI void mfma_vvv(f32x4& c, const bf16x8& a, const bf16x8& b) { asm volatile("v_mfma_f32_16x16x32_bf16 %0, %1, %2, %0" : "+v"(c) : "v"(a), "v"(b)); }
DI void mfma_vvv0(f32x4& c, const bf16x8& a, const bf16x8& b) { asm volatile("v_mfma_f32_16x16x32_bf16 %0, %1, %2, 0" : "=&v"(c) : "v"(a), "v"(b)); }
DI void mfma32(f32x16& c, const bf16x8& a, const bf16x8& b) { asm volatile("v_mfma_f32_32x32x16_bf16 %0, %1, %2, %0" : "+v"(c) : "v"(a), "v"(b)); }
DI void mfma_vav(f32x4& c, const u32x4& a, const u32x4& b) { asm volatile("v_mfma_f32_16x16x32_bf16 %0, %1, %2, %0" : "+v"(c) : "a"(a), "v"(b)); }
DI void mfma_vav0(f32x4& c, const u32x4& a, const u32x4& b) { asm volatile("v_mfma_f32_16x16x32_bf16 %0, %1, %2, 0" : "=&v"(c) : "a"(a), "v"(b)); }
#define MFMA_DRAIN() do { SB(); asm volatile("s_nop 7\n\ts_nop 4" ::: "memory"); SB(); } while (0)
constexpr int SC1 = 16;
DI u32x4 gld128(rsrc_t r, unsigned voff, unsigned soff) { return __builtin_amdgcn_raw_buffer_load_b128(r, voff, soff, SC1); }
DI unsigned gld32(rsrc_t r, unsigned voff, unsigned soff) { return __builtin_amdgcn_raw_buffer_load_b32(r, voff, soff, SC1); }
DI void gst128(u32x4 v, rsrc_t r, unsigned voff, unsigned soff) { __builtin_amdgcn_raw_buffer_store_b128(v, r, voff, soff, SC1); }
DI void gst64(u32x2 v, rsrc_t r, unsigned voff, unsigned soff) { __builtin_amdgcn_raw_buffer_store_b64(v, r, voff, soff, SC1); }
DI void gst32(unsigned v, rsrc_t r, unsigned voff, unsigned soff) { __builtin_amdgcn_raw_buffer_store_b32(v, r, voff, soff, SC1); }
#define RLX_AGENT __ATOMIC_RELAXED, __HIP_MEMORY_SCOPE_AGENT
DI u64 flag_ld(const char* a) { return __hip_atomic_load((const GAS u64*)a, RLX_AGENT); }
DI void flag_st(char* a, u64 v) { __hip_atomic_store((GAS u64*)a, v, RLX_AGENT); }
template <int N> DI void wait_flags(const char* base, u64 epoch, char* tmo, int lane) {
  for (unsigned spins = 0;; ++spins) {
    bool ok = true;
#pragma unroll
    for (int i = 0; i < (N + 63) / 64; ++i) { const int idx = i * 64 + lane; const u64 v = flag_ld(base + (idx < N ? idx : 0) * 8); ok &= (v == epoch); }
    if (__all(ok)) break;
    if (spins > 30000u) { if (lane == 0) flag_st(tmo, 1); break; }
    __builtin_amdgcn_s_sleep(1);
  }
  __builtin_amdgcn_fence(__ATOMIC_ACQUIRE, "wavefront");
}
DI f32x4 selv(unsigned m, f32x4 a, f32x4 b) {
  f32x4 r;
#pragma unroll
  for (int e = 0; e < 4; ++e) r[e] = __uint_as_float((__float_as_uint(a[e]) & m) | (__float_as_uint(b[e]) & ~m));
  return r;
}
DI unsigned gelu_mul2(f32x2 g, f32x2 u) {
  const f32x2 c1 = {-2.302208198f, -2.302208198f}, c2 = {-0.1029432397f, -0.1029432397f}, one = {1.f, 1.f};
  f32x2 a = g * __builtin_elementwise_fma(g * g, c2, c1);
  f32x2 e = {__builtin_amdgcn_exp2f(a[0]), __builtin_amdgcn_exp2f(a[1])};
  f32x2 d = e + one;
  f32x2 r = {__builtin_amdgcn_rcpf(d[0]), __builtin_amdgcn_rcpf(d[1])};
  f32x2 h = (g * u) * r;
  return cvtpk(h[0], h[1]);
}

DI float wave_sum(float a) {
  a += __builtin_bit_cast(float, __builtin_amdgcn_update_dpp(0, __builtin_bit_cast(int, a), 0xB1, 0xf, 0xf, true));
  a += __builtin_bit_cast(float, __builtin_amdgcn_update_dpp(0, __builtin_bit_cast(int, a), 0x4E, 0xf, 0xf, true));
  a += __builtin_bit_cast(float, __builtin_amdgcn_update_dpp(0, __builtin_bit_cast(int, a), 0x141, 0xf, 0xf, true));
  a += __builtin_bit_cast(float, __builtin_amdgcn_update_dpp(0, __builtin_bit_cast(int, a), 0x140, 0xf, 0xf, true));
  const int x = __builtin_bit_cast(int, a);
  return (__builtin_bit_cast(float, __builtin_amdgcn_readlane(x, 0)) + __builtin_bit_cast(float, __builtin_amdgcn_readlane(x, 16))) +
         (__builtin_bit_cast(float, __builtin_amdgcn_readlane(x, 32)) + __builtin_bit_cast(float, __builtin_amdgcn_readlane(x, 48)));
}
DI float dot2(unsigned a, unsigned b, float c) { return __builtin_amdgcn_fdot2_f32_bf16(__builtin_bit_cast(bf16x2, a), __builtin_bit_cast(bf16x2, b), c, false); }
struct Params { const u16 *q, *k, *v, *shk, *hk, *Dv, *wg, *wu, *wd, *gw; u16* out; char* ws; };

struct G1 {
  rsrc_t xs, wsr;
  unsigned xv[4], wv[7];
  unsigned sx, sw;
  unsigned mx, mw;
  unsigned xm0, wm0, slot;
  unsigned dx, dw;
  unsigned xa[4], xb[4], wt, w6;
  f32x16 acc[3][2], acc6;
  bf16x8 XA[2], XB[2], W[2][4];
};
template <int N> DI void g1_go(G1& c) {
  if constexpr (N < 4) dma_go<N * 1024>(c.xs, c.xv[N], c.sx);
  else dma_go<(N < 8 ? N - 4 : N - 8) * 1024>(c.wsr, c.wv[N - 4], c.sw);
}
template <int N> DI void g1_dma(G1& c, unsigned st, unsigned slot) {
  const unsigned m = N < 4 ? c.xm0 + slot * G1_XS : c.wm0 + slot * G1_WS + (N >= 8 ? 4096 : 0);
  asm volatile("s_mov_b32 m0, %0\n\ts_nop 0" :: "s"(m) : "m0", "memory");
  if constexpr (N < 4) dma_go<N * 1024>(c.xs, c.xv[N], c.sx + st * 128);
  else dma_go<(N < 8 ? N - 4 : N - 8) * 1024>(c.wsr, c.wv[N - 4], c.sw + st * (64 * 3584));
}
template <int... Ns> DI void g1_dma_all(G1& c, unsigned st, unsigned slot, iseq<Ns...>) { (g1_dma<Ns>(c, st, slot), ...); }
template <int KK, int R> DI void g1_read(G1& c) {
  constexpr int set = KK & 1;
  if constexpr (R == 0) c.XA[set] = lds_b128(c.xa[KK]);
  else if constexpr (R == 2) c.XB[set] = lds_b128(c.xb[KK]);
  else {
    constexpr int p = R == 1 ? 0 : R - 2;
    constexpr unsigned o = KK * 16 * 224;
    if constexpr (p < 3) c.W[set][p] = lds_tr2(c.wt + o + p * 32, c.wt + o + p * 32 + 4 * 224);
    else c.W[set][p] = lds_tr2(c.w6 + o, c.w6 + o + 4 * 224);
  }
}
template <int KK, int M> DI void g1_mfma(G1& c) {
  constexpr int set = KK & 1;
  if constexpr (M < 6) mfma32(c.acc[M >> 1][M & 1], c.W[set][M >> 1], (M & 1) ? c.XB[set] : c.XA[set]);
  else mfma32(c.acc6, c.W[set][3], c.XA[set]);
}
struct Tail {
  rsrc_t hs, xr, wsr; unsigned hvoff, hm0, zm0, xvoff, wdso, xlds; const char* sflags; u64 epoch; char* tmo;
  u64 pf[4]; u32x4 xv[3];
};
DI void poll_issue(u64& v, const char* a) { asm volatile("global_load_dwordx2 %0, %1, off sc1" : "=v"(v) : "v"(a) : "memory"); }
template <int I> DI void tail_op(Tail& t, u32x4 (&Wd)[14][4], int lane) {
  if constexpr (I < 4) dma_go<I * 1024>(t.hs, t.hvoff, 0);
  else if constexpr (I == 4) dma_go<0>(t.hs, lane * 16, 0);
  else if constexpr (I < 9) dma_go<(I - 5) * 1024>(t.hs, t.hvoff + 2, 0);
  else if constexpr (I < 11) { }
  else if constexpr (I < 14) t.xv[I - 11] = gld128(t.xr, t.xvoff + (I - 11) * 1024, 0);
  else { constexpr int n = I - 14; Wd[n >> 2][n & 3] = gld128(t.wsr, lane * 16 + (n & 3) * 1024, t.wdso + (n >> 2) * 4096); }
}
constexpr int TAIL_WD = 12;
constexpr int vm_slot(int g) { constexpr int s[11] = {24, 26, 1, 3, 5, 8, 10, 12, 15, 17, 19}; for (int i = 0; i < 11; ++i) if (s[i] == g) return i; return -1; }
template <int MODE, int G> DI void g1_gap(G1& c, Tail& t, u32x4 (&Wd)[14][4], int lane) {
  constexpr int kk = G / 7, m = G % 7, v = vm_slot(G);
  if constexpr (MODE == 5 && G == 0) {
#pragma unroll
    for (int i = 0; i < 4; ++i) poll_issue(t.pf[i], t.sflags + (i * 64 + lane) * 8);
  }
  g1_mfma<kk, m>(c);
  if constexpr (kk < 3) { if constexpr (m < 6) g1_read<kk + 1, m>(c); }
  else if constexpr (MODE != 7) {
    if constexpr (m == 1) {
      SB();
      if constexpr (MODE == 5) asm volatile("s_waitcnt vmcnt(15) lgkmcnt(0)\n\ts_barrier" ::: "memory");
      else if constexpr (MODE == 6) asm volatile("s_waitcnt vmcnt(9) lgkmcnt(0)\n\ts_barrier" : "+v"(t.pf[0]), "+v"(t.pf[1]), "+v"(t.pf[2]), "+v"(t.pf[3]) :: "memory");
      else asm volatile("s_waitcnt vmcnt(11) lgkmcnt(0)\n\ts_barrier" ::: "memory");
      if constexpr (MODE == 6) {
        const bool ok = t.pf[0] == t.epoch && t.pf[1] == t.epoch && t.pf[2] == t.epoch && t.pf[3] == t.epoch;
        if (!__all(ok)) wait_flags<256>(t.sflags, t.epoch, t.tmo, lane);
        __builtin_amdgcn_fence(__ATOMIC_ACQUIRE, "wavefront");
      }
    }
    if constexpr (m == 2) { g1_read<0, 0>(c); g1_read<0, 1>(c); }
    if constexpr (m == 3) { g1_read<0, 2>(c); g1_read<0, 3>(c); }
    if constexpr (m == 4) g1_read<0, 4>(c);
    if constexpr (m == 5) g1_read<0, 5>(c);
  }
  if constexpr (v >= 0) {
    if constexpr (MODE == 0) g1_go<v>(c);
    if constexpr (MODE == 5) { if constexpr (v >= 2) g1_go<v>(c); else tail_op<v>(t, Wd, lane); }
    if constexpr (MODE == 6) { if constexpr (v >= 2) tail_op<v>(t, Wd, lane); else tail_op<11 + v>(t, Wd, lane); }
    if constexpr (MODE == 7) { if constexpr (v >= 2) tail_op<11 + v>(t, Wd, lane); else tail_op<23 + v>(t, Wd, lane); }
  }
  if constexpr (MODE == 7 && G == 22) tail_op<22>(t, Wd, lane);
  if constexpr (MODE == 7 && G == 27) tail_op<25>(t, Wd, lane);
  if constexpr (MODE == 7 && (G == 20 || G == 21 || G == 23)) { constexpr int i = G == 20 ? 0 : G == 21 ? 1 : 2; *(LAS u32x4*)(t.xlds + i * 1024) = t.xv[i]; }
  if constexpr (MODE == 0 || MODE == 5) {
    if constexpr (G == 4) m0_set(c.mw);
    if constexpr (G == 6) c.sx += 128;
    if constexpr (G == 14) m0_set(c.mw + 4096);
    if constexpr (G == 20) c.sw += 64 * 3584;
  }
  if constexpr (MODE == 0 && G == 23) m0_set(c.mx);
  if constexpr (MODE == 5 && G == 23) m0_set(t.hm0);
  if constexpr (MODE == 6 && G == 4) m0_set(t.zm0);
  if constexpr (MODE == 6 && G == 7) m0_set(t.hm0 + CV_HS);
  if constexpr (MODE != 7) {
    if constexpr (G == 9) { c.xa[0] += c.dx; c.xb[0] += c.dx; }
    if constexpr (G == 11) { c.xa[1] += c.dx; c.xb[1] += c.dx; }
    if constexpr (G == 16) { c.xa[2] += c.dx; c.xb[2] += c.dx; }
    if constexpr (G == 21) { c.xa[3] += c.dx; c.xb[3] += c.dx; c.wt += c.dw; c.w6 += c.dw; }
  }
  if constexpr (MODE == 0) {
    if constexpr (G == 25) { c.mw = c.wm0 + c.slot * G1_WS; c.slot = c.slot == 2 ? 0 : c.slot + 1; asm volatile("" : "+s"(c.mw), "+s"(c.slot)); }
    if constexpr (G == 27) {
      c.dx = c.slot == 2 ? 0u - 2 * G1_XS : G1_XS; c.dw = c.slot == 2 ? 0u - 2 * G1_WS : G1_WS; c.mx = c.xm0 + c.slot * G1_XS;
      asm volatile("" : "+s"(c.dx), "+s"(c.dw), "+s"(c.mx));
    }
  }
  SB();
}
template <int MODE, int... Gs> DI void g1_stage(G1& c, Tail& t, u32x4 (&Wd)[14][4], int lane, iseq<Gs...>) { (g1_gap<MODE, Gs>(c, t, Wd, lane), ...); }
template <int... Rs> DI void g1_read0(G1& c, iseq<Rs...>) { (g1_read<0, Rs>(c), ...); }

struct ConvCtx { unsigned abase, bbase, hm; u32x4 a0; const char* hfl; unsigned wdso; int lane; rsrc_t wsr; };
constexpr int cv_al(int m) { int al = 0, n = 8; while (m >= n) { m -= n; --n; ++al; } return al; }
constexpr int cv_be(int m) { int n = 8; while (m >= n) { m -= n; --n; } return m; }
template <int RR, int M> DI void conv_gap(const ConvCtx& cx, f32x4 (&acc)[8], u32x2 (&alo)[2][8], u32x2 (&ahi)[2][8], bf16x8 (&B)[2][8], u32x4 (&Wd)[14][4], u64& hflag) {
  constexpr int set = RR & 1, al = cv_al(M), be = cv_be(M);
  if constexpr (M == 0) WAIT_LGKM0();
  u32x4 av = {alo[set][al][0], alo[set][al][1], ahi[set][al][0], ahi[set][al][1]};
  if constexpr (RR == 0 && al == 0) {
#pragma unroll
    for (int e = 0; e < 4; ++e) av[e] = (av[e] & cx.hm) | (cx.a0[e] & ~cx.hm);
  }
  bf16x8 A = __builtin_bit_cast(bf16x8, av);
  if constexpr (RR == 0 && al == 0) { asm volatile("s_nop 1" : "+v"(A)); mfma_vvv0(acc[be], A, B[set][be]); }
  else mfma_vvv(acc[al + be], A, B[set][be]);
  if constexpr (RR == 3 && M == 1) hflag = flag_ld(cx.hfl);
  if constexpr (M % 5 == 3) {
    constexpr int n = 28 + RR * 7 + M / 5;
    if constexpr (n < 56) Wd[n >> 2][n & 3] = gld128(cx.wsr, cx.lane * 16 + (n & 3) * 1024, cx.wdso + (n >> 2) * 4096);

  } else if constexpr (RR < 3) {
    constexpr int rd = M - (M + 1) / 5;
    if constexpr (rd < 24) {
      constexpr int i = rd / 3, k = rd % 3;
      if constexpr (k == 0) alo[set ^ 1][i] = *(const volatile LAS u32x2*)(cx.abase + (RR + 1) * 64 + i * 512);
      else if constexpr (k == 1) ahi[set ^ 1][i] = *(const volatile LAS u32x2*)(cx.abase + (RR + 1) * 64 + i * 512 + 8);
      else B[set ^ 1][i] = lds_b128(cx.bbase + 64 * (56 - 8 * i) + 64 * (RR + 1));
    }
  }
  SB();
}
template <int RR, int... Ms> DI void conv_rho(const ConvCtx& cx, f32x4 (&acc)[8], u32x2 (&alo)[2][8], u32x2 (&ahi)[2][8], bf16x8 (&B)[2][8], u32x4 (&Wd)[14][4], u64& hflag, iseq<Ms...>) {
  (conv_gap<RR, Ms>(cx, acc, alo, ahi, B, Wd, hflag), ...);
}

struct G2Ctx {
  rsrc_t wsr; const char* yfl; u64 epoch; char* tmo; int lane;
  unsigned yvo, yso, gvo, gso; const u16* qp; unsigned hso, rbase; int gq, li; char* myy;
};
constexpr int G2PD = 3;
template <int I, int M> DI void g2_gap(const G2Ctx& g, f32x4 (&acc)[4][4], u32x4 (&Wd)[14][4], u32x4 (&H)[G2PD + 1][4], const unsigned (&hvo)[4], u64& yflag,
                                       unsigned (&yv)[8], u32x2& gt, u32x4 (&qv)[2]) {
  constexpr int dt = M >> 2, st = M & 3;
  if constexpr (I == 0) mfma_vav0(acc[dt][st], Wd[I][dt], H[I % (G2PD + 1)][st]); else mfma_vav(acc[dt][st], Wd[I][dt], H[I % (G2PD + 1)][st]);
  if constexpr (I + G2PD < 14 && (M & 3) == 1) H[(I + G2PD) % (G2PD + 1)][M >> 2] = gld128(g.wsr, hvo[M >> 2], g.hso + (I + G2PD) * 1024);
  if constexpr (I == 8 && M == 15) yflag = flag_ld(g.yfl + (g.lane & 31) * 8);
  if constexpr (I == 11) {
    if constexpr (M == 0) {
      if (!__all(yflag == g.epoch)) wait_flags<32>(g.yfl, g.epoch, g.tmo, g.lane);
      __builtin_amdgcn_fence(__ATOMIC_ACQUIRE, "wavefront");
    }
    if constexpr ((M & 1) == 1) yv[M >> 1] = gld32(g.wsr, g.yvo + (M >> 1) * 4096, g.yso);
  }
  if constexpr (I == 12) {
    if constexpr (M == 1) gt = __builtin_amdgcn_raw_buffer_load_b64(g.wsr, g.gvo, g.gso, SC1);
    if constexpr (M == 5) qv[0] = *(const u32x4*)g.qp;
    if constexpr (M == 9) qv[1] = *(const u32x4*)(g.qp + D_);
  }
  if constexpr (I == 13 && M >= 9) { constexpr int q = M - 9; *(LAS f32x4*)(g.rbase + (q & 3) * 4096 + (((4 * (q >> 2) + g.gq) ^ g.li) << 4)) = acc[q >> 2][q & 3]; }
  SB();
}
template <int I, int... Ms> DI void g2_step(const G2Ctx& g, f32x4 (&acc)[4][4], u32x4 (&Wd)[14][4], u32x4 (&H)[G2PD + 1][4], const unsigned (&hvo)[4], u64& yflag,
                                            unsigned (&yv)[8], u32x2& gt, u32x4 (&qv)[2], iseq<Ms...>) {
  (g2_gap<I, Ms>(g, acc, Wd, H, hvo, yflag, yv, gt, qv), ...);
  if constexpr (I == 3) {
    WAIT_VM(12); asm volatile("s_barrier" ::: "memory");
    if (threadIdx.x == 0) flag_st(g.myy, g.epoch);
    SB();
  }
  if constexpr (I == 13) {
    MFMA_DRAIN();
#pragma unroll
    for (int q = 7; q < 16; ++q) *(LAS f32x4*)(g.rbase + (q & 3) * 4096 + (((4 * (q >> 2) + g.gq) ^ g.li) << 4)) = acc[q >> 2][q & 3];
  }
}
template <int... Is> DI void g2_loop(const G2Ctx& g, f32x4 (&acc)[4][4], u32x4 (&Wd)[14][4], u32x4 (&H)[G2PD + 1][4], const unsigned (&hvo)[4], u64& yflag,
                                     unsigned (&yv)[8], u32x2& gt, u32x4 (&qv)[2], iseq<Is...>) {
  (g2_step<Is>(g, acc, Wd, H, hvo, yflag, yv, gt, qv, make_iseq<16>{}), ...);
}

__global__ __launch_bounds__(256, 1) void k_fused(Params p) {
  __shared__ __attribute__((aligned(16))) char smem[LDS_BYTES];
  const unsigned lds0 = (unsigned)(size_t)(LAS char*)smem;
  const int lane = threadIdx.x & 63;
  const unsigned w = rfl(threadIdx.x >> 6);
  const unsigned b = blockIdx.x, xcd = b & 7, jb = b >> 3;
  char* const ws = p.ws;
  STAMP_DECL; STAMP(0);
  asm volatile("" :: "s"(p.q), "s"(p.k), "s"(p.v), "s"(p.shk), "s"(p.hk), "s"(p.Dv), "s"(p.wg), "s"(p.wu), "s"(p.wd), "s"(p.gw), "s"(p.out), "s"(p.ws));
  const rsrc_t wsr = mk_rsrc(ws, WS_END);
  u64 eword;
  asm volatile("global_load_dwordx2 %0, %1, off" : "=v"(eword) : "v"(ws + WS_E) : "memory");

  const unsigned sb = b >> 3, db = b & 7, s0p = sb * 64, d0p = db * 64;
  const unsigned kg_w = b >> 2, cq_w = b & 3;
  {
    const unsigned r8 = lane >> 3, c8 = lane & 7;
    const rsrc_t rk = mk_rsrc(p.k, S_ * D_ * 2), rv = mk_rsrc(p.v, S_ * D_ * 2), rw = mk_rsrc(p.wd, F_ * D_ * 2);
    auto dk = [&](int i) { int row = (int)s0p - 3 + 8 * i + (int)r8; unsigned vo = row >= 0 ? (unsigned)row * 1024 + d0p * 2 + c8 * 16 : 0x80000000u; dma16(rk, vo, 0, lds0 + ST_K + i * 1024); };
    auto dv = [&](int i) { dma16(rv, r8 * 1024 + c8 * 16, (s0p + 8 * i) * 1024 + d0p * 2, lds0 + ST_V + i * 1024); };
    auto dw = [&](int i) { dma16(rw, (lane >> 4) * 1024 + (lane & 15) * 16, (kg_w * 32 + 4 * i) * 1024 + cq_w * 256, lds0 + ST_WD + i * 1024); };
    if (w == 0) { dk(0); dk(1); dk(2); dk(3); dk(4); dk(5); dk(6); }
    else if (w == 1) { dk(7); dk(8); dv(0); dv(1); dv(2); dv(3); dv(4); }
    else if (w == 2) { dv(5); dv(6); dv(7); dma16(mk_rsrc(p.shk, D_ * 8), lane * 16, d0p * 8, lds0 + ST_SH); dw(0); dw(1); dw(2); }
    else { dw(3); dw(4); dw(5); dw(6); dw(7); }
  }
  SB();
  const unsigned rb = 8 * (xcd >> 2) + (jb & 7), fc = 4 * (xcd & 3) + (jb >> 3);
  const unsigned mi = w >> 1, ni = w & 1;
  G1 c;
  c.xs = mk_rsrc(p.q, S_ * D_ * 2);
  c.wsr = mk_rsrc((w >> 1) ? p.wu : p.wg, D_ * F_ * 2);
#pragma unroll
  for (int n = 0; n < 4; ++n) c.xv[n] = (lane >> 3) * 1024 + (((lane & 7) ^ ((4 * (n & 1) + (lane >> 4)) & 7)) << 4) + n * (8192 - 1024);
#pragma unroll
  for (int jj = 0; jj < 7; ++jj) {
    const unsigned ci = 64 * (7 * (w & 1) + jj) + lane, k = (ci * 4682u) >> 16, pc = ci - k * 14;
    c.wv[jj] = k * 3584 + pc * 16 - (jj & 3) * 1024;
  }
  c.sx = (rb * 128 + 32 * w) * 1024;
  c.sw = fc * 224;
  c.xm0 = lds0 + G1_XOFF + w * 4096;
  c.wm0 = lds0 + G1_WOFF + (w >> 1) * G1_WIMG + (w & 1) * 7 * 1024;
  g1_dma_all(c, 0, 0, make_iseq<11>{});
  {
    const int r = lane & 31, hh = lane >> 5;
    const unsigned base = r * 128 + ((hh ^ ((r >> 1) & 7)) << 4);
#pragma unroll
    for (int kk = 0; kk < 4; ++kk) {
      c.xa[kk] = lds0 + G1_XOFF + (2 * mi + ni) * 4096 + (base ^ (kk << 5));
      c.xb[kk] = lds0 + G1_XOFF + (2 * mi + (1 - ni)) * 4096 + (base ^ (kk << 5));
    }
    const unsigned wb = lds0 + G1_WOFF + ((lane >> 4) & 1) * G1_WIMG + (8 * hh + ((lane & 15) >> 2)) * 224 + (lane & 3) * 8;
    c.wt = wb + ni * 96; c.w6 = wb + 6 * 32;
  }
  SB();
  STAMP(1);
  asm volatile("s_waitcnt vmcnt(11)\n\ts_barrier" : "+v"(eword) :: "memory");
  const u64 epoch = ((u64)rfl((unsigned)(eword >> 32)) << 32 | rfl((unsigned)eword)) * 6364136223846793005ull + 1442695040888963407ull;
  STAMP(2);
  {
    const int t = threadIdx.x, c4 = t & 15, rg = t >> 4;
    u32x2 kr[7], vr[4]; u32x4 w4[2];
#pragma unroll
    for (int i = 0; i < 7; ++i) kr[i] = *(const LAS u32x2*)(lds0 + ST_K + (4 * rg + i) * 128 + c4 * 8);
#pragma unroll
    for (int i = 0; i < 4; ++i) vr[i] = *(const LAS u32x2*)(lds0 + ST_V + (4 * rg + i) * 128 + c4 * 8);
#pragma unroll
    for (int i = 0; i < 2; ++i) w4[i] = lds_u128(lds0 + ST_SH + c4 * 32 + i * 16);
    const int li = lane & 15, gq = lane >> 4;
    bf16x8 wf[2];
#pragma unroll
    for (int i = 0; i < 2; ++i) { const unsigned a = lds0 + ST_WD + (8 * gq + (li >> 2)) * 256 + (2 * w + i) * 32 + (lane & 3) * 8; wf[i] = lds_tr2(a, a + 4 * 256); }
    g1_dma<0>(c, 1, 1); g1_dma<1>(c, 1, 1);
    SB();
#pragma unroll
    for (int j = 0; j < 4; ++j) {
      const unsigned wa = w4[j >> 1][(j & 1) * 2], wb = w4[j >> 1][(j & 1) * 2 + 1];
      const float w0 = bflo(wa), w1 = bfhi(wa), w2 = bflo(wb), w3 = bfhi(wb);
      float kk[7], o[4];
#pragma unroll
      for (int i = 0; i < 7; ++i) kk[i] = (j & 1) ? bfhi(kr[i][j >> 1]) : bflo(kr[i][j >> 1]);
      if (j == 0) g1_dma<2>(c, 1, 1); if (j == 1) g1_dma<4>(c, 1, 1); if (j == 2) g1_dma<6>(c, 1, 1); if (j == 3) g1_dma<8>(c, 1, 1);
      SB();
#pragma unroll
      for (int i = 0; i < 4; ++i) {
        const float vv = (j & 1) ? bfhi(vr[i][j >> 1]) : bflo(vr[i][j >> 1]);
        o[i] = (kk[i] * w3 + kk[i + 1] * w2 + kk[i + 2] * w1 + kk[i + 3] * w0) * vv;
      }
      const int wd = 30 - 2 * rg, dl = 4 * c4 + j;
      *(LAS u32x2*)(lds0 + ST_T + dl * 128 + ((((wd >> 2) ^ c4) & 7) << 4) + (wd & 3) * 4) = u32x2{cvtpk(o[3], o[2]), cvtpk(o[1], o[0])};
      if (j == 0) g1_dma<3>(c, 1, 1); if (j == 1) g1_dma<5>(c, 1, 1); if (j == 2) g1_dma<7>(c, 1, 1); if (j == 3) g1_dma<9>(c, 1, 1);
      SB();
    }
    g1_dma<10>(c, 1, 1);
    SB();
    LGKM_BAR();
    u32x4 val[2];
#pragma unroll
    for (int i = 0; i < 2; ++i) { const int dl = (w * 2 + i) * 8 + (lane >> 3), ch = lane & 7; val[i] = lds_u128(lds0 + ST_T + dl * 128 + ((ch ^ (dl >> 2)) & 7) * 16); }
#pragma unroll
    for (int i = 0; i < 2; ++i) {
      const unsigned D16 = 8 * cq_w + 2 * w + i;
      gst128(__builtin_bit_cast(u32x4, wf[i]), wsr, kg_w < 56 ? WS_WDT + (((D16 >> 2) * 56 + kg_w) * 4 + (D16 & 3)) * 1024 + lane * 16 : 0x80000000u, 0);
    }
#pragma unroll
    for (int i = 0; i < 2; ++i) {
      const int dl = (w * 2 + i) * 8 + (lane >> 3), ch = lane & 7;
      gst128(val[i], wsr, WS_KVT + (d0p + dl) * 4096 + (1984 - s0p) * 2 + ch * 16, 0);
    }
    SB();
  }
  STAMP(3);
  asm volatile("s_waitcnt vmcnt(15) lgkmcnt(0)\n\ts_barrier" ::: "memory");
  g1_read0(c, make_iseq<6>{});
  g1_dma<0>(c, 2, 2); g1_dma<1>(c, 2, 2);
  c.sx += 2 * 128; c.sw += 2 * (64 * 3584);
  c.slot = 0; c.mx = c.xm0; c.mw = c.wm0 + 2 * G1_WS; c.dx = G1_XS; c.dw = G1_WS;
#pragma unroll
  for (int i = 0; i < 3; ++i) { c.acc[i][0] = f32x16{}; c.acc[i][1] = f32x16{}; }
  c.acc6 = f32x16{};
  SB();
  asm volatile("s_nop 1" ::: "memory");
  SB();
  STAMP(4);
  const unsigned cch = w >> 1, hf = w & 1, dch = b * 2 + cch, cvb = lds0 + CV_BASE + cch * CV_CH, cvz = lds0 + CV_Z + cch * 1088;
  const unsigned rb2 = 4 * xcd + (jb & 3), cb = jb >> 2;
  u32x4 Wd[14][4];
  Tail t;
  t.hs = mk_rsrc(p.hk + (size_t)dch * S_, (D_ - dch) * S_ * 2);
  t.hvoff = lane * 16 + (2 * hf + 17) * 2; t.hm0 = cvb + 2 * hf * CV_HS; t.zm0 = cvz + 32;
  t.xr = mk_rsrc(ws + WS_KVT + dch * 4096, 4096); t.xvoff = (hf * 128 + lane) * 16; t.xlds = cvb + CV_H + (hf * 128 + lane) * 16;
  t.wsr = wsr; t.wdso = WS_WDT + (cb * 56 + 14 * w) * 4096;
  t.sflags = ws + WS_FS; t.epoch = epoch; t.tmo = ws + WS_TMO;
  {
#pragma unroll 1
    for (unsigned s = 0; s < 5; ++s) {
      DSTAMP(s);
      g1_stage<0>(c, t, Wd, lane, make_iseq<28>{});
      if (s == 0 && threadIdx.x == 0) flag_st(ws + WS_FS + b * 8, epoch);
    }
    DSTAMP(5);
    g1_stage<5>(c, t, Wd, lane, make_iseq<28>{});
    if (lane < 8) *(LAS unsigned*)(cvz + lane * 4) = 0;
    c.dx = G1_XS; c.dw = G1_WS;
    DSTAMP(6);
    g1_stage<6>(c, t, Wd, lane, make_iseq<28>{});
    if (b == 0 && threadIdx.x == 0) flag_st(ws + WS_E, epoch);
    DSTAMP(7);
    g1_stage<7>(c, t, Wd, lane, make_iseq<28>{});
    DSTAMP(8);
  }
  MFMA_DRAIN();
  STAMP(5);
  auto ldWd = [&](int n) { Wd[n >> 2][n & 3] = gld128(wsr, lane * 16 + (n & 3) * 1024, t.wdso + (n >> 2) * 4096); };
  {
    const unsigned grow = 128 * rb + 8 * fc + 2 * w;
    const u32x4 gx0 = *(const u32x4*)(p.q + (size_t)grow * D_ + lane * 8), gx1 = *(const u32x4*)(p.q + (size_t)(grow + 1) * D_ + lane * 8), gw4 = *(const u32x4*)(p.gw + lane * 8);
    SB();
    const int s32 = lane & 31, hh = lane >> 5;
    const unsigned lo = (s32 >> 4) * 57344 + (s32 & 15) * 16 + 8 * hh;
    const unsigned hb = WS_H + rb * 8 * 57344 + fc * 7 * 512;
    const unsigned bA = hb + (2 * mi + ni) * 2 * 57344, bB = hb + (2 * mi + (1 - ni)) * 2 * 57344, t3 = 3 * ni * 512;
    const unsigned sA = bA + t3, sB = bB + t3, s6 = bA + 6 * 512;
    auto put = [&](int n0, int nl, const f32x16& a, unsigned so, int imm) {
      if (nl > 0) ldWd(n0);
      u32x2 o0 = {gelu_mul2(f32x2{a[0], a[1]}, f32x2{a[8], a[9]}), gelu_mul2(f32x2{a[2], a[3]}, f32x2{a[10], a[11]})};
      gst64(o0, wsr, lo + imm, so);
      SB();
      if (nl > 1) ldWd(n0 + 1);
      u32x2 o1 = {gelu_mul2(f32x2{a[4], a[5]}, f32x2{a[12], a[13]}), gelu_mul2(f32x2{a[6], a[7]}, f32x2{a[14], a[15]})};
      if (nl > 2) ldWd(n0 + 2);
      gst64(o1, wsr, lo + imm + 256, so);
      SB();
    };
    put(12, 3, c.acc[0][0], sA, 0); put(15, 3, c.acc[0][1], sB, 0);
    put(18, 2, c.acc[1][0], sA, 512); put(20, 2, c.acc[1][1], sB, 512);
    put(22, 2, c.acc[2][0], sA, 1024); put(24, 2, c.acc[2][1], sB, 1024);
    put(26, 2, c.acc6, s6, 0);
    float gsum[2] = {0.f, 0.f};
#pragma unroll
    for (int j = 0; j < 4; ++j) { gsum[0] = dot2(gx0[j], gw4[j], gsum[0]); gsum[1] = dot2(gx1[j], gw4[j], gsum[1]); }
#pragma unroll
    for (int r = 0; r < 2; ++r) gsum[r] = __builtin_amdgcn_rcpf(1.f + __builtin_amdgcn_exp2f(-1.4426950409f * wave_sum(gsum[r])));
    gst32(__float_as_uint(lane & 1 ? gsum[1] : gsum[0]), wsr, lane < 2 ? WS_G + (grow + lane) * 4 : 0x80000000u, 0);
    SB();
  }
  LGKM_BAR();
  STAMP(6);

  constexpr int PD = 3;
  u32x4 H[PD + 1][4];
  const unsigned hso = WS_H + ((4 * rb2) * 56 + 14 * w) * 1024;
  unsigned hvo[4];
#pragma unroll
  for (int st = 0; st < 4; ++st) hvo[st] = lane * 16 + st * 57344;
  auto ldH1 = [&](int i, int st) { H[i % (PD + 1)][st] = gld128(wsr, hvo[st], hso + i * 1024); };
  auto ldH = [&](int i) { ldH1(i, 0); ldH1(i, 1); ldH1(i, 2); ldH1(i, 3); };
  {
    const int li = lane & 15, gq = lane >> 4;
    u32x4 a0;
#pragma unroll
    for (int e = 0; e < 4; ++e) {
      const unsigned o = cvz + (li + 8 * gq + 2 * e + 1) * 2;
      a0[e] = (unsigned)*(const LAS u16*)o | ((unsigned)*(const LAS u16*)(o + 2) << 16);
    }
    const float Dd = __uint_as_float((unsigned)p.Dv[dch] << 16);
    const unsigned abase = cvb + (li & 3) * CV_HS + (li >> 2) * 8 + gq * 16 + hf * 4 * 64 - 64;
    const unsigned bbase = cvb + CV_H + (254 - 2 * li + gq) * 16 - 64 * 56 + hf * 4 * 64;
    f32x4 acc[8];
    u32x2 alo[2][8], ahi[2][8];
    bf16x8 B[2][8];
#pragma unroll
    for (int i = 0; i < 8; ++i) {
      alo[0][i] = *(const volatile LAS u32x2*)(abase + i * 512);
      ahi[0][i] = *(const volatile LAS u32x2*)(abase + i * 512 + 8);
      B[0][i] = lds_b128(bbase + 64 * (56 - 8 * i));
    }
    SB();
    STAMP(7);
    u64 hflag = 0;
    ConvCtx cx{abase, bbase, 0u - hf, a0, ws + WS_FH + (rb2 >> 1) * 128 + li * 8, t.wdso, lane, wsr};
    DSTAMP(9);
    conv_rho<0>(cx, acc, alo, ahi, B, Wd, hflag, make_iseq<36>{});
    DSTAMP(10);
    WAIT_VM(7); asm volatile("s_barrier" ::: "memory");
    if (threadIdx.x == 0) flag_st(ws + WS_FH + rb * 128 + fc * 8, epoch);
    SB();
    DSTAMP(11);
    conv_rho<1>(cx, acc, alo, ahi, B, Wd, hflag, make_iseq<36>{});
    conv_rho<2>(cx, acc, alo, ahi, B, Wd, hflag, make_iseq<36>{});
    conv_rho<3>(cx, acc, alo, ahi, B, Wd, hflag, make_iseq<36>{});
    DSTAMP(12);
    STAMP(8);
    if (!__all(hflag == epoch)) wait_flags<16>(ws + WS_FH + (rb2 >> 1) * 128, epoch, ws + WS_TMO, lane);
    __builtin_amdgcn_fence(__ATOMIC_ACQUIRE, "wavefront");
    DSTAMP(13);
    ldH(0);
    MFMA_DRAIN();
    const unsigned red = lds0 + CV_RED, hm = 0u - hf;
#pragma unroll
    for (int g = 0; g < 4; ++g) *(LAS f32x4*)(red + w * 4096 + g * 1024 + lane * 16) = selv(hm, acc[g], acc[4 + g]);
    SB();
    ldH(1);
    SB();
    DSTAMP(14);
    LGKM_BAR();
    DSTAMP(15);
    f32x4 oth[4]; u32x2 kvr[4];
#pragma unroll
    for (int g = 0; g < 4; ++g) {
      oth[g] = *(const LAS f32x4*)(red + (w ^ 1) * 4096 + g * 1024 + lane * 16);
      kvr[g] = *(const LAS u32x2*)(cvb + CV_H + (2044 - (256 * (g + 4 * hf) + 16 * li + 4 * gq)) * 2);
    }
    SB();
    ldH(2);
    SB();
#pragma unroll
    for (int g = 0; g < 4; ++g) {
      f32x4 y = selv(hm, acc[4 + g], acc[g]) + oth[g];
      float r0 = y[0] + bfhi(kvr[g][1]) * Dd, r1 = y[1] + bflo(kvr[g][1]) * Dd, r2 = y[2] + bfhi(kvr[g][0]) * Dd, r3 = y[3] + bflo(kvr[g][0]) * Dd;
      gst64(u32x2{cvtpk(r0, r1), cvtpk(r2, r3)}, wsr, (16 * li + 4 * gq) * 2 + g * 512, WS_Y2T + dch * 4096 + hf * 2048);
    }
    SB();
  }
  STAMP(9);

  const int tt = threadIdx.x, cc = tt & 7, sp = tt >> 3;
  const unsigned s0 = rb2 * 64, d0 = cb * 64;
  unsigned yv[8]; u32x2 gt; u32x4 qv[2];
  {
    f32x4 acc[4][4];
    u64 yflag = 0;
    const int li = lane & 15, gq = lane >> 4;
    const unsigned rbase = lds0 + w * 16384 + li * 256;
    G2Ctx g2{wsr, ws + WS_FY + cb * 256, epoch, ws + WS_TMO, lane, (unsigned)(8 * cc * 4096 + sp * 4), WS_Y2T + d0 * 4096 + s0 * 2, (unsigned)(sp * 8), WS_G + s0 * 4,
             p.q + (size_t)(s0 + 2 * sp) * D_ + d0 + cc * 8, hso, rbase, gq, li, ws + WS_FY + (b >> 5) * 256 + (b & 31) * 8};
    g2_loop(g2, acc, Wd, H, hvo, yflag, yv, gt, qv, make_iseq<14>{});
    STAMP(10);
  }
  LGKM_BAR();
  {
#pragma unroll
    for (int r = 0; r < 2; ++r) {
      const int s = 2 * sp + r;
      f32x4 b0 = {0, 0, 0, 0}, b1 = {0, 0, 0, 0};
#pragma unroll
      for (int ww = 0; ww < 4; ++ww) {
        b0 += *(const LAS f32x4*)(lds0 + ww * 16384 + s * 256 + (((2 * cc) ^ (s & 15)) << 4));
        b1 += *(const LAS f32x4*)(lds0 + ww * 16384 + s * 256 + (((2 * cc + 1) ^ (s & 15)) << 4));
      }
      const float g = __uint_as_float(gt[r]);
      u32x4 o;
#pragma unroll
      for (int e = 0; e < 4; ++e) {
        float bl = e < 2 ? b0[2 * e] : b1[2 * e - 4], bh = e < 2 ? b0[2 * e + 1] : b1[2 * e - 3];
        float yl = r ? bfhi(yv[2 * e]) : bflo(yv[2 * e]), yh = r ? bfhi(yv[2 * e + 1]) : bflo(yv[2 * e + 1]);
        float al = bflo(qv[r][e]) * yl, ah = bfhi(qv[r][e]) * yh;
        o[e] = cvtpk(g * al + (1.f - g) * bl, g * ah + (1.f - g) * bh);
      }
      __builtin_amdgcn_raw_buffer_store_b128(o, mk_rsrc(p.out, S_ * D_ * 2), ((s0 + s) * D_ + d0 + cc * 8) * 2, 0, OUT_AUX);
    }
  }
  STAMP(11);
  STAMP_FLUSH(12);
}

extern "C" void kernel_launch(void* const* d_in, const int* in_sizes, int n_in, void* d_out, int out_size, void* d_ws, size_t ws_size, hipStream_t stream) {
  Params p;
  p.q = (const u16*)d_in[0]; p.k = (const u16*)d_in[1]; p.v = (const u16*)d_in[2];
  p.shk = (const u16*)d_in[3]; p.hk = (const u16*)d_in[4]; p.Dv = (const u16*)d_in[5];
  p.wg = (const u16*)d_in[7]; p.wu = (const u16*)d_in[8]; p.wd = (const u16*)d_in[9]; p.gw = (const u16*)d_in[10];
  p.out = (u16*)d_out; p.ws = (char*)d_ws;
  k_fused<<<256, 256, 0, stream>>>(p);
}
```

```cpp
#include <hip/hip_runtime.h>
#include <hip/hip_bf16.h>
#include <utility>
#pragma clang diagnostic ignored "-Winline-asm"
#pragma clang diagnostic ignored "-Wint-to-pointer-cast"

#define DI __device__ __forceinline__
#define LAS __attribute__((address_space(3)))
#define GAS __attribute__((address_space(1)))
typedef unsigned short u16;
typedef unsigned long long u64;
typedef float f32x16 __attribute__((ext_vector_type(16)));
typedef float f32x4 __attribute__((ext_vector_type(4)));
typedef float f32x2 __attribute__((ext_vector_type(2)));
typedef __bf16 bf16x8 __attribute__((ext_vector_type(8)));
typedef __bf16 bf16x2 __attribute__((ext_vector_type(2)));
typedef short s16x4 __attribute__((ext_vector_type(4)));
typedef short s16x8 __attribute__((ext_vector_type(8)));
typedef unsigned u32x4 __attribute__((ext_vector_type(4)));
typedef unsigned u32x2 __attribute__((ext_vector_type(2)));
typedef __amdgpu_buffer_rsrc_t rsrc_t;
template <int... Is> using iseq = std::integer_sequence<int, Is...>;
template <int N> using make_iseq = std::make_integer_sequence<int, N>;

constexpr int S_ = 2048, D_ = 512, F_ = 1792;
constexpr unsigned WS_E = 0, WS_FS = 256, WS_FH = 4096, WS_FY = 8192, WS_TMO = 12288;
constexpr unsigned WS_G   = 16384;
constexpr unsigned WS_KVT = 1u << 20;
constexpr unsigned WS_Y2T = WS_KVT + (2u << 20);
constexpr unsigned WS_WDT = WS_Y2T + (2u << 20);
constexpr unsigned WS_H   = WS_WDT + (2u << 20);
constexpr unsigned WS_END = WS_H + 128u * 56 * 1024;

constexpr unsigned G1_XS = 16384, G1_WIMG = 14336 + 128, G1_WS = 28800, G1_XOFF = 0, G1_WOFF = 3 * G1_XS;
constexpr unsigned ST_K = G1_WOFF + 3 * G1_WS, ST_V = ST_K + 9216, ST_SH = ST_V + 8192, ST_WD = ST_SH + 1024, ST_T = 2 * G1_XS;
constexpr unsigned CV_HS = 4160, CV_H = 4 * CV_HS, CV_X = 5120, CV_CH = CV_H + CV_X, CV_BASE = G1_WOFF + 2 * G1_WS, CV_RED = 0;
constexpr unsigned CV_Z = CV_BASE + 2 * CV_CH;
constexpr unsigned LDS_BYTES = ST_WD + 8192;
static_assert(CV_Z + 2 * 1088 <= LDS_BYTES && LDS_BYTES <= 163840, "lds");

#define SB() __builtin_amdgcn_sched_barrier(0)
#define STAMP_DECL
#define STAMP(i)
#define DSTAMP(i)
#define STAMP_FLUSH(n)

DI unsigned rfl(unsigned x) { return __builtin_amdgcn_readfirstlane(x); }
DI rsrc_t mk_rsrc(const void* p, unsigned bytes) { return __builtin_amdgcn_make_buffer_rsrc((void*)p, 0, bytes, 0x00020000); }
DI unsigned cvtpk(float lo, float hi) { f32x2 v = {lo, hi}; bf16x2 b = __builtin_convertvector(v, bf16x2); return __builtin_bit_cast(unsigned, b); }
DI float bflo(unsigned x) { return __uint_as_float(x << 16); }
DI float bfhi(unsigned x) { return __uint_as_float(x & 0xffff0000u); }
DI void dma16(rsrc_t r, unsigned voff, unsigned soff, unsigned ldsaddr) {
  asm volatile("s_mov_b32 m0, %0\n\ts_nop 0\n\tbuffer_load_dwordx4 %1, %2, %3 offen lds" :: "s"(ldsaddr), "v"(voff), "s"(r), "s"(soff) : "m0", "memory");
}
DI void m0_set(unsigned v) { asm volatile("s_mov_b32 m0, %0" :: "s"(v) : "m0", "memory"); }
template <int OFF> DI void dma_go(rsrc_t r, unsigned voff, unsigned soff) {
  asm volatile("buffer_load_dwordx4 %0, %1, %2 offen offset:%3 lds" :: "v"(voff), "s"(r), "s"(soff), "i"(OFF) : "memory");
}
#define WAIT_LGKM0() __builtin_amdgcn_s_waitcnt(0xC07F)
#define WAIT_VM(n) asm volatile("s_waitcnt vmcnt(" #n ")" ::: "memory")
#define WAIT_VM_BAR(n) asm volatile("s_waitcnt vmcnt(" #n ") lgkmcnt(0)\n\ts_barrier" ::: "memory")
#define LGKM_BAR() asm volatile("s_waitcnt lgkmcnt(0)\n\ts_barrier" ::: "memory")
DI bf16x8 lds_b128(unsigned a) { return *(const LAS bf16x8*)a; }
DI u32x4 lds_u128(unsigned a) { return *(const LAS u32x4*)a; }
DI bf16x8 lds_tr2(unsigned a0, unsigned a1) {
  s16x4 lo = __builtin_amdgcn_ds_read_tr16_b64_v4i16((LAS s16x4*)a0);
  s16x4 hi = __builtin_amdgcn_ds_read_tr16_b64_v4i16((LAS s16x4*)a1);
  s16x8 v = __builtin_shufflevector(lo, hi, 0, 1, 2, 3, 4, 5, 6, 7);
  return __builtin_bit_cast(bf16x8, v);
}
DI void mfma_vvv(f32x4& c, const bf16x8& a, const bf16x8& b) { asm volatile("v_mfma_f32_16x16x32_bf16 %0, %1, %2, %0" : "+v"(c) : "v"(a), "v"(b)); }
DI void mfma_vvv0(f32x4& c, const bf16x8& a, const bf16x8& b) { asm volatile("v_mfma_f32_16x16x32_bf16 %0, %1, %2, 0" : "=&v"(c) : "v"(a), "v"(b)); }
DI void mfma32(f32x16& c, const bf16x8& a, const bf16x8& b) { asm volatile("v_mfma_f32_32x32x16_bf16 %0, %1, %2, %0" : "+v"(c) : "v"(a), "v"(b)); }
DI void mfma_vav(f32x4& c, const u32x4& a, const u32x4& b) { asm volatile("v_mfma_f32_16x16x32_bf16 %0, %1, %2, %0" : "+v"(c) : "a"(a), "v"(b)); }
DI void mfma_vav0(f32x4& c, const u32x4& a, const u32x4& b) { asm volatile("v_mfma_f32_16x16x32_bf16 %0, %1, %2, 0" : "=&v"(c) : "a"(a), "v"(b)); }
#define MFMA_DRAIN() do { SB(); asm volatile("s_nop 7\n\ts_nop 4" ::: "memory"); SB(); } while (0)
constexpr int SC1 = 16;
DI u32x4 gld128(rsrc_t r, unsigned voff, unsigned soff) { return __builtin_amdgcn_raw_buffer_load_b128(r, voff, soff, SC1); }
DI unsigned gld32(rsrc_t r, unsigned voff, unsigned soff) { return __builtin_amdgcn_raw_buffer_load_b32(r, voff, soff, SC1); }
DI void gst128(u32x4 v, rsrc_t r, unsigned voff, unsigned soff) { __builtin_amdgcn_raw_buffer_store_b128(v, r, voff, soff, SC1); }
DI void gst64(u32x2 v, rsrc_t r, unsigned voff, unsigned soff) { __builtin_amdgcn_raw_buffer_store_b64(v, r, voff, soff, SC1); }
DI void gst32(unsigned v, rsrc_t r, unsigned voff, unsigned soff) { __builtin_amdgcn_raw_buffer_store_b32(v, r, voff, soff, SC1); }
#define RLX_AGENT __ATOMIC_RELAXED, __HIP_MEMORY_SCOPE_AGENT
DI u64 flag_ld(const char* a) { return __hip_atomic_load((const GAS u64*)a, RLX_AGENT); }
DI void flag_st(char* a, u64 v) { __hip_atomic_store((GAS u64*)a, v, RLX_AGENT); }
template <int N> DI void wait_flags(const char* base, u64 epoch, char* tmo, int lane) {
  for (unsigned spins = 0;; ++spins) {
    bool ok = true;
#pragma unroll
    for (int i = 0; i < (N + 63) / 64; ++i) { const int idx = i * 64 + lane; const u64 v = flag_ld(base + (idx < N ? idx : 0) * 8); ok &= (v == epoch); }
    if (__all(ok)) break;
    if (spins > 30000u) { if (lane == 0) flag_st(tmo, 1); break; }
    __builtin_amdgcn_s_sleep(1);
  }
  __builtin_amdgcn_fence(__ATOMIC_ACQUIRE, "wavefront");
}
DI f32x4 selv(unsigned m, f32x4 a, f32x4 b) {
  f32x4 r;
#pragma unroll
  for (int e = 0; e < 4; ++e) r[e] = __uint_as_float((__float_as_uint(a[e]) & m) | (__float_as_uint(b[e]) & ~m));
  return r;
}
DI unsigned gelu_mul2(f32x2 g, f32x2 u) {
  const f32x2 c1 = {-2.302208198f, -2.302208198f}, c2 = {-0.1029432397f, -0.1029432397f}, one = {1.f, 1.f};
  f32x2 a = g * __builtin_elementwise_fma(g * g, c2, c1);
  f32x2 e = {__builtin_amdgcn_exp2f(a[0]), __builtin_amdgcn_exp2f(a[1])};
  f32x2 d = e + one;
  f32x2 r = {__builtin_amdgcn_rcpf(d[0]), __builtin_amdgcn_rcpf(d[1])};
  f32x2 h = (g * u) * r;
  return cvtpk(h[0], h[1]);
}

DI float wave_sum(float a) {
  a += __builtin_bit_cast(float, __builtin_amdgcn_update_dpp(0, __builtin_bit_cast(int, a), 0xB1, 0xf, 0xf, true));
  a += __builtin_bit_cast(float, __builtin_amdgcn_update_dpp(0, __builtin_bit_cast(int, a), 0x4E, 0xf, 0xf, true));
  a += __builtin_bit_cast(float, __builtin_amdgcn_update_dpp(0, __builtin_bit_cast(int, a), 0x141, 0xf, 0xf, true));
  a += __builtin_bit_cast(float, __builtin_amdgcn_update_dpp(0, __builtin_bit_cast(int, a), 0x140, 0xf, 0xf, true));
  const int x = __builtin_bit_cast(int, a);
  return (__builtin_bit_cast(float, __builtin_amdgcn_readlane(x, 0)) + __builtin_bit_cast(float, __builtin_amdgcn_readlane(x, 16))) +
         (__builtin_bit_cast(float, __builtin_amdgcn_readlane(x, 32)) + __builtin_bit_cast(float, __builtin_amdgcn_readlane(x, 48)));
}
DI float dot2(unsigned a, unsigned b, float c) { return __builtin_amdgcn_fdot2_f32_bf16(__builtin_bit_cast(bf16x2, a), __builtin_bit_cast(bf16x2, b), c, false); }
struct Params { const u16 *q, *k, *v, *shk, *hk, *Dv, *wg, *wu, *wd, *gw; u16* out; char* ws; };

struct G1 {
  rsrc_t xs, wsr;
  unsigned xv[4], wv[7];
  unsigned sx, sw;
  unsigned mx, mw;
  unsigned xm0, wm0, slot;
  unsigned dx, dw;
  bool first;
  unsigned xa[4], xb[4], wt, w6;
  f32x16 acc[3][2], acc6;
  bf16x8 XA[2], XB[2], W[2][4];
};
template <int N> DI void g1_go(G1& c) {
  if constexpr (N < 4) dma_go<N * 1024>(c.xs, c.xv[N], c.sx);
  else dma_go<(N < 8 ? N - 4 : N - 8) * 1024>(c.wsr, c.wv[N - 4], c.sw);
}
template <int N> DI void g1_dma(G1& c, unsigned st, unsigned slot) {
  const unsigned m = N < 4 ? c.xm0 + slot * G1_XS : c.wm0 + slot * G1_WS + (N >= 8 ? 4096 : 0);
  asm volatile("s_mov_b32 m0, %0\n\ts_nop 0" :: "s"(m) : "m0", "memory");
  if constexpr (N < 4) dma_go<N * 1024>(c.xs, c.xv[N], c.sx + st * 128);
  else dma_go<(N < 8 ? N - 4 : N - 8) * 1024>(c.wsr, c.wv[N - 4], c.sw + st * (64 * 3584));
}
template <int... Ns> DI void g1_dma_all(G1& c, unsigned st, unsigned slot, iseq<Ns...>) { (g1_dma<Ns>(c, st, slot), ...); }
template <int KK, int R> DI void g1_read(G1& c) {
  constexpr int set = KK & 1;
  if constexpr (R == 0) c.XA[set] = lds_b128(c.xa[KK]);
  else if constexpr (R == 2) c.XB[set] = lds_b128(c.xb[KK]);
  else {
    constexpr int p = R == 1 ? 0 : R - 2;
    constexpr unsigned o = KK * 16 * 224;
    if constexpr (p < 3) c.W[set][p] = lds_tr2(c.wt + o + p * 32, c.wt + o + p * 32 + 4 * 224);
    else c.W[set][p] = lds_tr2(c.w6 + o, c.w6 + o + 4 * 224);
  }
}
template <int KK, int M> DI void g1_mfma(G1& c) {
  constexpr int set = KK & 1;
  if constexpr (M < 6) mfma32(c.acc[M >> 1][M & 1], c.W[set][M >> 1], (M & 1) ? c.XB[set] : c.XA[set]);
  else mfma32(c.acc6, c.W[set][3], c.XA[set]);
}
struct Tail {
  rsrc_t hs, xr, wsr; unsigned hvoff, hm0, zm0, xvoff, wdso, xlds; const char* sflags; u64 epoch; char* tmo;
  u64 pf[4]; u32x4 xv[3];
};
DI void poll_issue(u64& v, const char* a) { asm volatile("global_load_dwordx2 %0, %1, off sc1" : "=v"(v) : "v"(a) : "memory"); }
template <int I> DI void tail_op(Tail& t, u32x4 (&Wd)[14][4], int lane) {
  if constexpr (I < 4) dma_go<I * 1024>(t.hs, t.hvoff, 0);
  else if constexpr (I == 4) dma_go<0>(t.hs, lane * 16, 0);
  else if constexpr (I < 9) dma_go<(I - 5) * 1024>(t.hs, t.hvoff + 2, 0);
  else if constexpr (I < 11) { }
  else if constexpr (I < 14) t.xv[I - 11] = gld128(t.xr, t.xvoff + (I - 11) * 1024, 0);
  else { constexpr int n = I - 14; Wd[n >> 2][n & 3] = gld128(t.wsr, lane * 16 + (n & 3) * 1024, t.wdso + (n >> 2) * 4096); }
}
constexpr int TAIL_WD = 12;
constexpr int vm_slot(int g) { constexpr int s[11] = {24, 26, 1, 3, 5, 8, 10, 12, 15, 17, 19}; for (int i = 0; i < 11; ++i) if (s[i] == g) return i; return -1; }
template <int MODE, int G> DI void g1_gap(G1& c, Tail& t, u32x4 (&Wd)[14][4], int lane) {
  constexpr int kk = G / 7, m = G % 7, v = vm_slot(G);
  if constexpr (MODE == 5 && G == 0) {
#pragma unroll
    for (int i = 0; i < 4; ++i) poll_issue(t.pf[i], t.sflags + (i * 64 + lane) * 8);
  }
  g1_mfma<kk, m>(c);
  if constexpr (kk < 3) { if constexpr (m < 6) g1_read<kk + 1, m>(c); }
  else if constexpr (MODE != 7) {
    if constexpr (m == 1) {
      SB();
      if constexpr (MODE == 5) asm volatile("s_waitcnt vmcnt(15) lgkmcnt(0)\n\ts_barrier" ::: "memory");
      else if constexpr (MODE == 6) asm volatile("s_waitcnt vmcnt(9) lgkmcnt(0)\n\ts_barrier" : "+v"(t.pf[0]), "+v"(t.pf[1]), "+v"(t.pf[2]), "+v"(t.pf[3]) :: "memory");
      else if constexpr (MODE == 0) { if (c.first) asm volatile("s_waitcnt vmcnt(15)" ::: "memory"); else asm volatile("s_waitcnt vmcnt(11)" ::: "memory"); asm volatile("s_waitcnt lgkmcnt(0)\n\ts_barrier" ::: "memory"); c.first = false; }
      else asm volatile("s_waitcnt vmcnt(11) lgkmcnt(0)\n\ts_barrier" ::: "memory");
      if constexpr (MODE == 6) {
        const bool ok = t.pf[0] == t.epoch && t.pf[1] == t.epoch && t.pf[2] == t.epoch && t.pf[3] == t.epoch;
        if (!__all(ok)) wait_flags<256>(t.sflags, t.epoch, t.tmo, lane);
        __builtin_amdgcn_fence(__ATOMIC_ACQUIRE, "wavefront");
      }
    }
    if constexpr (m == 2) { g1_read<0, 0>(c); g1_read<0, 1>(c); }
    if constexpr (m == 3) { g1_read<0, 2>(c); g1_read<0, 3>(c); }
    if constexpr (m == 4) g1_read<0, 4>(c);
    if constexpr (m == 5) g1_read<0, 5>(c);
  }
  if constexpr (v >= 0) {
    if constexpr (MODE == 0) g1_go<v>(c);
    if constexpr (MODE == 5) { if constexpr (v >= 2) g1_go<v>(c); else tail_op<v>(t, Wd, lane); }
    if constexpr (MODE == 6) { if constexpr (v >= 2) tail_op<v>(t, Wd, lane); else tail_op<11 + v>(t, Wd, lane); }
    if constexpr (MODE == 7) { if constexpr (v >= 2) tail_op<11 + v>(t, Wd, lane); else tail_op<23 + v>(t, Wd, lane); }
  }
  if constexpr (MODE == 7 && G == 22) tail_op<22>(t, Wd, lane);
  if constexpr (MODE == 7 && G == 27) tail_op<25>(t, Wd, lane);
  if constexpr (MODE == 7 && (G == 20 || G == 21 || G == 23)) { constexpr int i = G == 20 ? 0 : G == 21 ? 1 : 2; *(LAS u32x4*)(t.xlds + i * 1024) = t.xv[i]; }
  if constexpr (MODE == 0 || MODE == 5) {
    if constexpr (G == 4) m0_set(c.mw);
    if constexpr (G == 6) c.sx += 128;
    if constexpr (G == 14) m0_set(c.mw + 4096);
    if constexpr (G == 20) c.sw += 64 * 3584;
  }
  if constexpr (MODE == 0 && G == 23) m0_set(c.mx);
  if constexpr (MODE == 5 && G == 23) m0_set(t.hm0);
  if constexpr (MODE == 6 && G == 4) m0_set(t.zm0);
  if constexpr (MODE == 6 && G == 7) m0_set(t.hm0 + CV_HS);
  if constexpr (MODE != 7) {
    if constexpr (G == 9) { c.xa[0] += c.dx; c.xb[0] += c.dx; }
    if constexpr (G == 11) { c.xa[1] += c.dx; c.xb[1] += c.dx; }
    if constexpr (G == 16) { c.xa[2] += c.dx; c.xb[2] += c.dx; }
    if constexpr (G == 21) { c.xa[3] += c.dx; c.xb[3] += c.dx; c.wt += c.dw; c.w6 += c.dw; }
  }
  if constexpr (MODE == 0) {
    if constexpr (G == 25) { c.mw = c.wm0 + c.slot * G1_WS; c.slot = c.slot == 2 ? 0 : c.slot + 1; asm volatile("" : "+s"(c.mw), "+s"(c.slot)); }
    if constexpr (G == 27) {
      c.dx = c.slot == 2 ? 0u - 2 * G1_XS : G1_XS; c.dw = c.slot == 2 ? 0u - 2 * G1_WS : G1_WS; c.mx = c.xm0 + c.slot * G1_XS;
      asm volatile("" : "+s"(c.dx), "+s"(c.dw), "+s"(c.mx));
    }
  }
  SB();
}
template <int MODE, int... Gs> DI void g1_stage(G1& c, Tail& t, u32x4 (&Wd)[14][4], int lane, iseq<Gs...>) { (g1_gap<MODE, Gs>(c, t, Wd, lane), ...); }
template <int... Rs> DI void g1_read0(G1& c, iseq<Rs...>) { (g1_read<0, Rs>(c), ...); }

struct ConvCtx { unsigned abase, bbase, hm; u32x4 a0; const char* hfl; unsigned wdso; int lane; rsrc_t wsr; };
constexpr int cv_al(int m) { int al = 0, n = 8; while (m >= n) { m -= n; --n; ++al; } return al; }
constexpr int cv_be(int m) { int n = 8; while (m >= n) { m -= n; --n; } return m; }
template <int RR, int M> DI void conv_gap(const ConvCtx& cx, f32x4 (&acc)[8], u32x2 (&alo)[2][8], u32x2 (&ahi)[2][8], bf16x8 (&B)[2][8], u32x4 (&Wd)[14][4], u64& hflag) {
  constexpr int set = RR & 1, al = cv_al(M), be = cv_be(M);
  if constexpr (M == 0) WAIT_LGKM0();
  u32x4 av = {alo[set][al][0], alo[set][al][1], ahi[set][al][0], ahi[set][al][1]};
  if constexpr (RR == 0 && al == 0) {
#pragma unroll
    for (int e = 0; e < 4; ++e) av[e] = (av[e] & cx.hm) | (cx.a0[e] & ~cx.hm);
  }
  bf16x8 A = __builtin_bit_cast(bf16x8, av);
  if constexpr (RR == 0 && al == 0) { asm volatile("s_nop 1" : "+v"(A)); mfma_vvv0(acc[be], A, B[set][be]); }
  else mfma_vvv(acc[al + be], A, B[set][be]);
  if constexpr (RR == 3 && M == 1) hflag = flag_ld(cx.hfl);
  if constexpr (M % 5 == 3) {
    constexpr int n = 28 + RR * 7 + M / 5;
    if constexpr (n < 56) Wd[n >> 2][n & 3] = gld128(cx.wsr, cx.lane * 16 + (n & 3) * 1024, cx.wdso + (n >> 2) * 4096);

  } else if constexpr (RR < 3) {
    constexpr int rd = M - (M + 1) / 5;
    if constexpr (rd < 24) {
      constexpr int i = rd / 3, k = rd % 3;
      if constexpr (k == 0) alo[set ^ 1][i] = *(const volatile LAS u32x2*)(cx.abase + (RR + 1) * 64 + i * 512);
      else if constexpr (k == 1) ahi[set ^ 1][i] = *(const volatile LAS u32x2*)(cx.abase + (RR + 1) * 64 + i * 512 + 8);
      else B[set ^ 1][i] = lds_b128(cx.bbase + 64 * (56 - 8 * i) + 64 * (RR + 1));
    }
  }
  SB();
}
template <int RR, int... Ms> DI void conv_rho(const ConvCtx& cx, f32x4 (&acc)[8], u32x2 (&alo)[2][8], u32x2 (&ahi)[2][8], bf16x8 (&B)[2][8], u32x4 (&Wd)[14][4], u64& hflag, iseq<Ms...>) {
  (conv_gap<RR, Ms>(cx, acc, alo, ahi, B, Wd, hflag), ...);
}

struct G2Ctx {
  rsrc_t wsr; const char* yfl; u64 epoch; char* tmo; int lane;
  unsigned yvo, yso, gvo, gso; const u16* qp; unsigned hso, rbase; int gq, li; char* myy;
  struct { char* ws; } p;
};
constexpr int G2R = 16, G2PD = 12;
template <int J> DI void g2_ldh(const G2Ctx& g, u32x4 (&H)[G2R]) { H[J % G2R] = gld128(g.wsr, g.lane * 16 + (J % 14 & 3) * 1024, g.hso + (J / 14) * 57344 + (J % 14 & ~3) * 1024); }
template <int P, int DT> DI void g2_put(const G2Ctx& g, f32x4 (&acc)[4][4]) {
  *(LAS f32x4*)(g.rbase + P * 4096 + (((4 * DT + g.gq) ^ g.li) << 4)) = acc[P][DT];
}
template <int J> DI void g2_step(const G2Ctx& g, f32x4 (&acc)[4][4], u32x4 (&Wd)[14][4], u32x4 (&H)[G2R], u64& yflag, unsigned (&yv)[8], u32x2& gt, u32x4 (&qv)[2]) {
  constexpr int P = J / 14, I = J % 14;
  if constexpr (I == 0) { auto& p = g.p; (void)p; DSTAMP(16 + 3 * P); }
  if constexpr (J == 44) {
    if (!__all(yflag == g.epoch)) wait_flags<32>(g.yfl, g.epoch, g.tmo, g.lane);
    __builtin_amdgcn_fence(__ATOMIC_ACQUIRE, "wavefront");
    SB();
  }
  if constexpr (I == 0) mfma_vav0(acc[P][0], Wd[I][0], H[J % G2R]); else mfma_vav(acc[P][0], Wd[I][0], H[J % G2R]);
  if constexpr (J + G2PD < 56) g2_ldh<J + G2PD>(g, H);
  if constexpr (J == 36) yflag = flag_ld(g.yfl + (g.lane & 31) * 8);
  if constexpr (J >= 44 && J < 52) yv[J - 44] = gld32(g.wsr, g.yvo + (J - 44) * 4096, g.yso);
  if constexpr (J == 52) gt = __builtin_amdgcn_raw_buffer_load_b64(g.wsr, g.gvo, g.gso, SC1);
  if constexpr (J == 53) qv[0] = *(const u32x4*)g.qp;
  if constexpr (J == 54) qv[1] = *(const u32x4*)(g.qp + D_);
  SB();
  if constexpr (I == 0) mfma_vav0(acc[P][1], Wd[I][1], H[J % G2R]); else mfma_vav(acc[P][1], Wd[I][1], H[J % G2R]);
  if constexpr (I == 0) mfma_vav0(acc[P][2], Wd[I][2], H[J % G2R]); else mfma_vav(acc[P][2], Wd[I][2], H[J % G2R]);
  if constexpr (P > 0 && I >= 2 && I < 6) g2_put<(P > 0 ? P - 1 : 0), (I >= 2 && I < 6 ? I - 2 : 0)>(g, acc);
  SB();
  if constexpr (I == 0) mfma_vav0(acc[P][3], Wd[I][3], H[J % G2R]); else mfma_vav(acc[P][3], Wd[I][3], H[J % G2R]);
  SB();
  if constexpr (J == 8) {
    WAIT_VM(12); asm volatile("s_barrier" ::: "memory");
    if (threadIdx.x == 0) flag_st(g.myy, g.epoch);
    SB();
  }
}
template <int... Js> DI void g2_loop(const G2Ctx& g, f32x4 (&acc)[4][4], u32x4 (&Wd)[14][4], u32x4 (&H)[G2R], u64& yflag, unsigned (&yv)[8], u32x2& gt, u32x4 (&qv)[2], iseq<Js...>) {
  (g2_step<Js>(g, acc, Wd, H, yflag, yv, gt, qv), ...);
}
template <int... Js> DI void g2_ldh_range(const G2Ctx& g, u32x4 (&H)[G2R], iseq<Js...>) { (g2_ldh<Js>(g, H), ...); }
template <int B, int... Js> DI void g2_ldh_from(const G2Ctx& g, u32x4 (&H)[G2R], iseq<Js...>) { (g2_ldh<B + Js>(g, H), ...); }

__global__ __launch_bounds__(256, 1) void k_fused(Params p) {
  __shared__ __attribute__((aligned(16))) char smem[LDS_BYTES];
  const unsigned lds0 = (unsigned)(size_t)(LAS char*)smem;
  const int lane = threadIdx.x & 63;
  const unsigned w = rfl(threadIdx.x >> 6);
  const unsigned b = blockIdx.x, xcd = b & 7, jb = b >> 3;
  char* const ws = p.ws;
  STAMP_DECL; STAMP(0);
  asm volatile("" :: "s"(p.q), "s"(p.k), "s"(p.v), "s"(p.shk), "s"(p.hk), "s"(p.Dv), "s"(p.wg), "s"(p.wu), "s"(p.wd), "s"(p.gw), "s"(p.out), "s"(p.ws));
  const rsrc_t wsr = mk_rsrc(ws, WS_END);
  u64 eword;
  asm volatile("global_load_dwordx2 %0, %1, off" : "=v"(eword) : "v"(ws + WS_E) : "memory");

  const unsigned sb = b >> 3, db = b & 7, s0p = sb * 64, d0p = db * 64;
  const unsigned kg_w = b >> 2, cq_w = b & 3;
  {
    const unsigned r8 = lane >> 3, c8 = lane & 7;
    const rsrc_t rk = mk_rsrc(p.k, S_ * D_ * 2), rv = mk_rsrc(p.v, S_ * D_ * 2), rw = mk_rsrc(p.wd, F_ * D_ * 2);
    auto dk = [&](int i) { int row = (int)s0p - 3 + 8 * i + (int)r8; unsigned vo = row >= 0 ? (unsigned)row * 1024 + d0p * 2 + c8 * 16 : 0x80000000u; dma16(rk, vo, 0, lds0 + ST_K + i * 1024); };
    auto dv = [&](int i) { dma16(rv, r8 * 1024 + c8 * 16, (s0p + 8 * i) * 1024 + d0p * 2, lds0 + ST_V + i * 1024); };
    auto dw = [&](int i) { dma16(rw, (lane >> 4) * 1024 + (lane & 15) * 16, (kg_w * 32 + 4 * i) * 1024 + cq_w * 256, lds0 + ST_WD + i * 1024); };
    if (w == 0) { dk(0); dk(1); dk(2); dk(3); dk(4); dk(5); dk(6); }
    else if (w == 1) { dk(7); dk(8); dv(0); dv(1); dv(2); dv(3); dv(4); }
    else if (w == 2) { dv(5); dv(6); dv(7); dma16(mk_rsrc(p.shk, D_ * 8), lane * 16, d0p * 8, lds0 + ST_SH); dw(0); dw(1); dw(2); }
    else { dw(3); dw(4); dw(5); dw(6); dw(7); }
  }
  SB();
  const unsigned rb = 8 * (xcd >> 2) + (jb & 7), fc = 4 * (xcd & 3) + (jb >> 3);
  const unsigned mi = w >> 1, ni = w & 1;
  G1 c;
  c.xs = mk_rsrc(p.q, S_ * D_ * 2);
  c.wsr = mk_rsrc((w >> 1) ? p.wu : p.wg, D_ * F_ * 2);
#pragma unroll
  for (int n = 0; n < 4; ++n) c.xv[n] = (lane >> 3) * 1024 + (((lane & 7) ^ ((4 * (n & 1) + (lane >> 4)) & 7)) << 4) + n * (8192 - 1024);
#pragma unroll
  for (int jj = 0; jj < 7; ++jj) {
    const unsigned ci = 64 * (7 * (w & 1) + jj) + lane, k = (ci * 4682u) >> 16, pc = ci - k * 14;
    c.wv[jj] = k * 3584 + pc * 16 - (jj & 3) * 1024;
  }
  c.sx = (rb * 128 + 32 * w) * 1024;
  c.sw = fc * 224;
  c.xm0 = lds0 + G1_XOFF + w * 4096;
  c.wm0 = lds0 + G1_WOFF + (w >> 1) * G1_WIMG + (w & 1) * 7 * 1024;
  g1_dma_all(c, 0, 0, make_iseq<11>{});
  {
    const int r = lane & 31, hh = lane >> 5;
    const unsigned base = r * 128 + ((hh ^ ((r >> 1) & 7)) << 4);
#pragma unroll
    for (int kk = 0; kk < 4; ++kk) {
      c.xa[kk] = lds0 + G1_XOFF + (2 * mi + ni) * 4096 + (base ^ (kk << 5));
      c.xb[kk] = lds0 + G1_XOFF + (2 * mi + (1 - ni)) * 4096 + (base ^ (kk << 5));
    }
    const unsigned wb = lds0 + G1_WOFF + ((lane >> 4) & 1) * G1_WIMG + (8 * hh + ((lane & 15) >> 2)) * 224 + (lane & 3) * 8;
    c.wt = wb + ni * 96; c.w6 = wb + 6 * 32;
  }
  SB();
  STAMP(1);
  asm volatile("s_waitcnt vmcnt(11)\n\ts_barrier" : "+v"(eword) :: "memory");
  const u64 epoch = ((u64)rfl((unsigned)(eword >> 32)) << 32 | rfl((unsigned)eword)) * 6364136223846793005ull + 1442695040888963407ull;
  STAMP(2);
  {
    const int t = threadIdx.x, c4 = t & 15, rg = t >> 4;
    u32x2 kr[7], vr[4]; u32x4 w4[2];
#pragma unroll
    for (int i = 0; i < 7; ++i) kr[i] = *(const LAS u32x2*)(lds0 + ST_K + (4 * rg + i) * 128 + c4 * 8);
#pragma unroll
    for (int i = 0; i < 4; ++i) vr[i] = *(const LAS u32x2*)(lds0 + ST_V + (4 * rg + i) * 128 + c4 * 8);
#pragma unroll
    for (int i = 0; i < 2; ++i) w4[i] = lds_u128(lds0 + ST_SH + c4 * 32 + i * 16);
    const int li = lane & 15, gq = lane >> 4;
    bf16x8 wf[2];
#pragma unroll
    for (int i = 0; i < 2; ++i) { const unsigned a = lds0 + ST_WD + (8 * gq + (li >> 2)) * 256 + (2 * w + i) * 32 + (lane & 3) * 8; wf[i] = lds_tr2(a, a + 4 * 256); }
    g1_dma<0>(c, 1, 1); g1_dma<1>(c, 1, 1);
    SB();
#pragma unroll
    for (int j = 0; j < 4; ++j) {
      const unsigned wa = w4[j >> 1][(j & 1) * 2], wb = w4[j >> 1][(j & 1) * 2 + 1];
      const float w0 = bflo(wa), w1 = bfhi(wa), w2 = bflo(wb), w3 = bfhi(wb);
      float kk[7], o[4];
#pragma unroll
      for (int i = 0; i < 7; ++i) kk[i] = (j & 1) ? bfhi(kr[i][j >> 1]) : bflo(kr[i][j >> 1]);
      if (j == 0) g1_dma<2>(c, 1, 1); if (j == 1) g1_dma<4>(c, 1, 1); if (j == 2) g1_dma<6>(c, 1, 1); if (j == 3) g1_dma<8>(c, 1, 1);
      SB();
#pragma unroll
      for (int i = 0; i < 4; ++i) {
        const float vv = (j & 1) ? bfhi(vr[i][j >> 1]) : bflo(vr[i][j >> 1]);
        o[i] = (kk[i] * w3 + kk[i + 1] * w2 + kk[i + 2] * w1 + kk[i + 3] * w0) * vv;
      }
      const int wd = 30 - 2 * rg, dl = 4 * c4 + j;
      *(LAS u32x2*)(lds0 + ST_T + dl * 128 + ((((wd >> 2) ^ c4) & 7) << 4) + (wd & 3) * 4) = u32x2{cvtpk(o[3], o[2]), cvtpk(o[1], o[0])};
      if (j == 0) g1_dma<3>(c, 1, 1); if (j == 1) g1_dma<5>(c, 1, 1); if (j == 2) g1_dma<7>(c, 1, 1); if (j == 3) g1_dma<9>(c, 1, 1);
      SB();
    }
    g1_dma<10>(c, 1, 1);
    SB();
    LGKM_BAR();
    u32x4 val[2];
#pragma unroll
    for (int i = 0; i < 2; ++i) { const int dl = (w * 2 + i) * 8 + (lane >> 3), ch = lane & 7; val[i] = lds_u128(lds0 + ST_T + dl * 128 + ((ch ^ (dl >> 2)) & 7) * 16); }
#pragma unroll
    for (int i = 0; i < 2; ++i) {
      const unsigned D16 = 8 * cq_w + 2 * w + i;
      gst128(__builtin_bit_cast(u32x4, wf[i]), wsr, kg_w < 56 ? WS_WDT + (((D16 >> 2) * 56 + kg_w) * 4 + (D16 & 3)) * 1024 + lane * 16 : 0x80000000u, 0);
    }
#pragma unroll
    for (int i = 0; i < 2; ++i) {
      const int dl = (w * 2 + i) * 8 + (lane >> 3), ch = lane & 7;
      gst128(val[i], wsr, WS_KVT + (d0p + dl) * 4096 + (1984 - s0p) * 2 + ch * 16, 0);
    }
    SB();
  }
  STAMP(3);
  asm volatile("s_waitcnt vmcnt(15) lgkmcnt(0)\n\ts_barrier" ::: "memory");
  g1_read0(c, make_iseq<6>{});
  g1_dma<0>(c, 2, 2); g1_dma<1>(c, 2, 2);
  c.sx += 2 * 128; c.sw += 2 * (64 * 3584);
  c.slot = 0; c.mx = c.xm0; c.mw = c.wm0 + 2 * G1_WS; c.dx = G1_XS; c.dw = G1_WS; c.first = true;
#pragma unroll
  for (int i = 0; i < 3; ++i) { c.acc[i][0] = f32x16{}; c.acc[i][1] = f32x16{}; }
  c.acc6 = f32x16{};
  SB();
  asm volatile("s_nop 1" ::: "memory");
  SB();
  STAMP(4);
  const unsigned cch = w >> 1, hf = w & 1, dch = b * 2 + cch, cvb = lds0 + CV_BASE + cch * CV_CH, cvz = lds0 + CV_Z + cch * 1088;
  const unsigned rb2 = 4 * xcd + (jb & 3), cb = jb >> 2;
  u32x4 Wd[14][4];
  Tail t;
  t.hs = mk_rsrc(p.hk + (size_t)dch * S_, (D_ - dch) * S_ * 2);
  t.hvoff = lane * 16 + (2 * hf + 17) * 2; t.hm0 = cvb + 2 * hf * CV_HS; t.zm0 = cvz + 32;
  t.xr = mk_rsrc(ws + WS_KVT + dch * 4096, 4096); t.xvoff = (hf * 128 + lane) * 16; t.xlds = cvb + CV_H + (hf * 128 + lane) * 16;
  t.wsr = wsr; t.wdso = WS_WDT + (cb * 56 + 14 * w) * 4096;
  t.sflags = ws + WS_FS; t.epoch = epoch; t.tmo = ws + WS_TMO;
  {
#pragma unroll 1
    for (unsigned s = 0; s < 5; ++s) {
      DSTAMP(s);
      g1_stage<0>(c, t, Wd, lane, make_iseq<28>{});
      if (s == 1 && threadIdx.x == 0) flag_st(ws + WS_FS + b * 8, epoch);
    }
    DSTAMP(5);
    g1_stage<5>(c, t, Wd, lane, make_iseq<28>{});
    if (lane < 8) *(LAS unsigned*)(cvz + lane * 4) = 0;
    c.dx = G1_XS; c.dw = G1_WS;
    DSTAMP(6);
    g1_stage<6>(c, t, Wd, lane, make_iseq<28>{});
    if (b == 0 && threadIdx.x == 0) flag_st(ws + WS_E, epoch);
    DSTAMP(7);
    g1_stage<7>(c, t, Wd, lane, make_iseq<28>{});
    DSTAMP(8);
  }
  MFMA_DRAIN();
  STAMP(5);
  auto ldWd = [&](int n) { Wd[n >> 2][n & 3] = gld128(wsr, lane * 16 + (n & 3) * 1024, t.wdso + (n >> 2) * 4096); };
  {
    const unsigned grow = 128 * rb + 8 * fc + 2 * w;
    const u32x4 gx0 = *(const u32x4*)(p.q + (size_t)grow * D_ + lane * 8), gx1 = *(const u32x4*)(p.q + (size_t)(grow + 1) * D_ + lane * 8), gw4 = *(const u32x4*)(p.gw + lane * 8);
    SB();
    const int s32 = lane & 31, hh = lane >> 5;
    const unsigned lo = (s32 >> 4) * 57344 + (s32 & 15) * 16 + 8 * hh;
    const unsigned hb = WS_H + rb * 8 * 57344 + fc * 7 * 512;
    const unsigned bA = hb + (2 * mi + ni) * 2 * 57344, bB = hb + (2 * mi + (1 - ni)) * 2 * 57344, t3 = 3 * ni * 512;
    const unsigned sA = bA + t3, sB = bB + t3, s6 = bA + 6 * 512;
    auto put = [&](int n0, int nl, const f32x16& a, unsigned so, int imm) {
      if (nl > 0) ldWd(n0);
      u32x2 o0 = {gelu_mul2(f32x2{a[0], a[1]}, f32x2{a[8], a[9]}), gelu_mul2(f32x2{a[2], a[3]}, f32x2{a[10], a[11]})};
      gst64(o0, wsr, lo + imm, so);
      SB();
      if (nl > 1) ldWd(n0 + 1);
      u32x2 o1 = {gelu_mul2(f32x2{a[4], a[5]}, f32x2{a[12], a[13]}), gelu_mul2(f32x2{a[6], a[7]}, f32x2{a[14], a[15]})};
      if (nl > 2) ldWd(n0 + 2);
      gst64(o1, wsr, lo + imm + 256, so);
      SB();
    };
    put(12, 3, c.acc[0][0], sA, 0); put(15, 3, c.acc[0][1], sB, 0);
    put(18, 2, c.acc[1][0], sA, 512); put(20, 2, c.acc[1][1], sB, 512);
    put(22, 2, c.acc[2][0], sA, 1024); put(24, 2, c.acc[2][1], sB, 1024);
    put(26, 2, c.acc6, s6, 0);
    float gsum[2] = {0.f, 0.f};
#pragma unroll
    for (int j = 0; j < 4; ++j) { gsum[0] = dot2(gx0[j], gw4[j], gsum[0]); gsum[1] = dot2(gx1[j], gw4[j], gsum[1]); }
#pragma unroll
    for (int r = 0; r < 2; ++r) gsum[r] = __builtin_amdgcn_rcpf(1.f + __builtin_amdgcn_exp2f(-1.4426950409f * wave_sum(gsum[r])));
    gst32(__float_as_uint(lane & 1 ? gsum[1] : gsum[0]), wsr, lane < 2 ? WS_G + (grow + lane) * 4 : 0x80000000u, 0);
    SB();
  }
  LGKM_BAR();
  STAMP(6);

  const int tt = threadIdx.x, cc = tt & 7, sp = tt >> 3;
  const unsigned s0 = rb2 * 64, d0 = cb * 64;
  u32x4 H[G2R];
  const G2Ctx g2{wsr, ws + WS_FY + cb * 256, epoch, ws + WS_TMO, lane, (unsigned)(8 * cc * 4096 + sp * 4), WS_Y2T + d0 * 4096 + s0 * 2, (unsigned)(sp * 8), WS_G + s0 * 4,
                 p.q + (size_t)(s0 + 2 * sp) * D_ + d0 + cc * 8, WS_H + ((4 * rb2) * 56 + 14 * w) * 1024, lds0 + w * 16384 + (lane & 15) * 256, lane >> 4, lane & 15,
                 ws + WS_FY + (b >> 5) * 256 + (b & 31) * 8, {ws}};
  {
    const int li = lane & 15, gq = lane >> 4;
    u32x4 a0;
#pragma unroll
    for (int e = 0; e < 4; ++e) {
      const unsigned o = cvz + (li + 8 * gq + 2 * e + 1) * 2;
      a0[e] = (unsigned)*(const LAS u16*)o | ((unsigned)*(const LAS u16*)(o + 2) << 16);
    }
    const float Dd = __uint_as_float((unsigned)p.Dv[dch] << 16);
    const unsigned abase = cvb + (li & 3) * CV_HS + (li >> 2) * 8 + gq * 16 + hf * 4 * 64 - 64;
    const unsigned bbase = cvb + CV_H + (254 - 2 * li + gq) * 16 - 64 * 56 + hf * 4 * 64;
    f32x4 acc[8];
    u32x2 alo[2][8], ahi[2][8];
    bf16x8 B[2][8];
#pragma unroll
    for (int i = 0; i < 8; ++i) {
      alo[0][i] = *(const volatile LAS u32x2*)(abase + i * 512);
      ahi[0][i] = *(const volatile LAS u32x2*)(abase + i * 512 + 8);
      B[0][i] = lds_b128(bbase + 64 * (56 - 8 * i));
    }
    SB();
    STAMP(7);
    u64 hflag = 0;
    ConvCtx cx{abase, bbase, 0u - hf, a0, ws + WS_FH + (rb2 >> 1) * 128 + li * 8, t.wdso, lane, wsr};
    DSTAMP(9);
    conv_rho<0>(cx, acc, alo, ahi, B, Wd, hflag, make_iseq<36>{});
    DSTAMP(10);
    WAIT_VM(7); asm volatile("s_barrier" ::: "memory");
    if (threadIdx.x == 0) flag_st(ws + WS_FH + rb * 128 + fc * 8, epoch);
    SB();
    DSTAMP(11);
    conv_rho<1>(cx, acc, alo, ahi, B, Wd, hflag, make_iseq<36>{});
    conv_rho<2>(cx, acc, alo, ahi, B, Wd, hflag, make_iseq<36>{});
    conv_rho<3>(cx, acc, alo, ahi, B, Wd, hflag, make_iseq<36>{});
    DSTAMP(12);
    STAMP(8);
    if (!__all(hflag == epoch)) wait_flags<16>(ws + WS_FH + (rb2 >> 1) * 128, epoch, ws + WS_TMO, lane);
    __builtin_amdgcn_fence(__ATOMIC_ACQUIRE, "wavefront");
    DSTAMP(13);
    g2_ldh_range(g2, H, make_iseq<4>{});
    MFMA_DRAIN();
    const unsigned red = lds0 + CV_RED, hm = 0u - hf;
#pragma unroll
    for (int g = 0; g < 4; ++g) *(LAS f32x4*)(red + w * 4096 + g * 1024 + lane * 16) = selv(hm, acc[g], acc[4 + g]);
    SB();
    g2_ldh_from<4>(g2, H, make_iseq<4>{});
    SB();
    DSTAMP(14);
    LGKM_BAR();
    DSTAMP(15);
    f32x4 oth[4]; u32x2 kvr[4];
#pragma unroll
    for (int g = 0; g < 4; ++g) {
      oth[g] = *(const LAS f32x4*)(red + (w ^ 1) * 4096 + g * 1024 + lane * 16);
      kvr[g] = *(const LAS u32x2*)(cvb + CV_H + (2044 - (256 * (g + 4 * hf) + 16 * li + 4 * gq)) * 2);
    }
#pragma unroll
    for (int g = 0; g < 4; ++g) {
      f32x4 y = selv(hm, acc[4 + g], acc[g]) + oth[g];
      float r0 = y[0] + bfhi(kvr[g][1]) * Dd, r1 = y[1] + bflo(kvr[g][1]) * Dd, r2 = y[2] + bfhi(kvr[g][0]) * Dd, r3 = y[3] + bflo(kvr[g][0]) * Dd;
      gst64(u32x2{cvtpk(r0, r1), cvtpk(r2, r3)}, wsr, (16 * li + 4 * gq) * 2 + g * 512, WS_Y2T + dch * 4096 + hf * 2048);
    }
    g2_ldh_from<8>(g2, H, make_iseq<4>{});
    SB();
  }
  STAMP(9);

  unsigned yv[8]; u32x2 gt; u32x4 qv[2];
  {
    f32x4 acc[4][4];
    u64 yflag = 0;
    g2_loop(g2, acc, Wd, H, yflag, yv, gt, qv, make_iseq<56>{});
    DSTAMP(28);
    MFMA_DRAIN();
    g2_put<3, 0>(g2, acc); g2_put<3, 1>(g2, acc); g2_put<3, 2>(g2, acc); g2_put<3, 3>(g2, acc);
    DSTAMP(29);
    STAMP(10);
  }
  LGKM_BAR();
  DSTAMP(30);
  {
#pragma unroll
    for (int r = 0; r < 2; ++r) {
      const int s = 2 * sp + r;
      f32x4 b0 = {0, 0, 0, 0}, b1 = {0, 0, 0, 0};
#pragma unroll
      for (int ww = 0; ww < 4; ++ww) {
        b0 += *(const LAS f32x4*)(lds0 + ww * 16384 + s * 256 + (((2 * cc) ^ (s & 15)) << 4));
        b1 += *(const LAS f32x4*)(lds0 + ww * 16384 + s * 256 + (((2 * cc + 1) ^ (s & 15)) << 4));
      }
      const float g = __uint_as_float(gt[r]);
      u32x4 o;
#pragma unroll
      for (int e = 0; e < 4; ++e) {
        float bl = e < 2 ? b0[2 * e] : b1[2 * e - 4], bh = e < 2 ? b0[2 * e + 1] : b1[2 * e - 3];
        float yl = r ? bfhi(yv[2 * e]) : bflo(yv[2 * e]), yh = r ? bfhi(yv[2 * e + 1]) : bflo(yv[2 * e + 1]);
        float al = bflo(qv[r][e]) * yl, ah = bfhi(qv[r][e]) * yh;
        o[e] = cvtpk(g * al + (1.f - g) * bl, g * ah + (1.f - g) * bh);
      }
      *(u32x4*)(p.out + (size_t)(s0 + s) * D_ + d0 + cc * 8) = o;
    }
  }
  STAMP(11);
  STAMP_FLUSH(12);
}

extern "C" void kernel_launch(void* const* d_in, const int* in_sizes, int n_in, void* d_out, int out_size, void* d_ws, size_t ws_size, hipStream_t stream) {
  Params p;
  p.q = (const u16*)d_in[0]; p.k = (const u16*)d_in[1]; p.v = (const u16*)d_in[2];
  p.shk = (const u16*)d_in[3]; p.hk = (const u16*)d_in[4]; p.Dv = (const u16*)d_in[5];
  p.wg = (const u16*)d_in[7]; p.wu = (const u16*)d_in[8]; p.wd = (const u16*)d_in[9]; p.gw = (const u16*)d_in[10];
  p.out = (u16*)d_out; p.ws = (char*)d_ws + (128u << 20);
  k_fused<<<256, 256, 0, stream>>>(p);
}
```

```cpp
#include <hip/hip_runtime.h>
#include <hip/hip_bf16.h>
#include <utility>
#define ROT 2
#pragma clang diagnostic ignored "-Winline-asm"
#pragma clang diagnostic ignored "-Wint-to-pointer-cast"

#define DI __device__ __forceinline__
#define LAS __attribute__((address_space(3)))
#define GAS __attribute__((address_space(1)))
typedef unsigned short u16;
typedef unsigned long long u64;
typedef float f32x16 __attribute__((ext_vector_type(16)));
typedef float f32x4 __attribute__((ext_vector_type(4)));
typedef float f32x2 __attribute__((ext_vector_type(2)));
typedef __bf16 bf16x8 __attribute__((ext_vector_type(8)));
typedef __bf16 bf16x2 __attribute__((ext_vector_type(2)));
typedef short s16x4 __attribute__((ext_vector_type(4)));
typedef short s16x8 __attribute__((ext_vector_type(8)));
typedef unsigned u32x4 __attribute__((ext_vector_type(4)));
typedef unsigned u32x2 __attribute__((ext_vector_type(2)));
typedef __amdgpu_buffer_rsrc_t rsrc_t;
template <int... Is> using iseq = std::integer_sequence<int, Is...>;
template <int N> using make_iseq = std::make_integer_sequence<int, N>;

constexpr int S_ = 2048, D_ = 512, F_ = 1792;
constexpr unsigned WS_E = 0, WS_FS = 256, WS_FH = 4096, WS_FY = 8192, WS_TMO = 12288;
constexpr unsigned WS_G   = 16384;
constexpr unsigned WS_KVT = 1u << 20;
constexpr unsigned WS_Y2T = WS_KVT + (2u << 20);
constexpr unsigned WS_WDT = WS_Y2T + (2u << 20);
constexpr unsigned WS_H   = WS_WDT + (2u << 20);
constexpr unsigned WS_END = WS_H + 128u * 56 * 1024;

constexpr unsigned G1_XS = 16384, G1_WIMG = 14336 + 128, G1_WS = 28800, G1_XOFF = 0, G1_WOFF = 3 * G1_XS;
constexpr unsigned ST_K = G1_WOFF + 3 * G1_WS, ST_V = ST_K + 9216, ST_SH = ST_V + 8192, ST_WD = ST_SH + 1024, ST_T = 2 * G1_XS;
constexpr unsigned CV_HS = 4160, CV_H = 4 * CV_HS, CV_X = 5120, CV_CH = CV_H + CV_X, CV_BASE = G1_WOFF + 2 * G1_WS, CV_RED = 0;
constexpr unsigned CV_Z = CV_BASE + 2 * CV_CH;
constexpr unsigned LDS_BYTES = ST_WD + 8192;
static_assert(CV_Z + 2 * 1088 <= LDS_BYTES && LDS_BYTES <= 163840, "lds");

#define SB() __builtin_amdgcn_sched_barrier(0)
#define STAMP_DECL
#define STAMP(i)
#define DSTAMP(i)
#define STAMP_FLUSH(n)

DI unsigned rfl(unsigned x) { return __builtin_amdgcn_readfirstlane(x); }
DI rsrc_t mk_rsrc(const void* p, unsigned bytes) { return __builtin_amdgcn_make_buffer_rsrc((void*)p, 0, bytes, 0x00020000); }
DI unsigned cvtpk(float lo, float hi) { f32x2 v = {lo, hi}; bf16x2 b = __builtin_convertvector(v, bf16x2); return __builtin_bit_cast(unsigned, b); }
DI float bflo(unsigned x) { return __uint_as_float(x << 16); }
DI float bfhi(unsigned x) { return __uint_as_float(x & 0xffff0000u); }
DI void dma16(rsrc_t r, unsigned voff, unsigned soff, unsigned ldsaddr) {
  asm volatile("s_mov_b32 m0, %0\n\ts_nop 0\n\tbuffer_load_dwordx4 %1, %2, %3 offen lds" :: "s"(ldsaddr), "v"(voff), "s"(r), "s"(soff) : "m0", "memory");
}
DI void m0_set(unsigned v) { asm volatile("s_mov_b32 m0, %0" :: "s"(v) : "m0", "memory"); }
template <int OFF> DI void dma_go(rsrc_t r, unsigned voff, unsigned soff) {
  asm volatile("buffer_load_dwordx4 %0, %1, %2 offen offset:%3 lds" :: "v"(voff), "s"(r), "s"(soff), "i"(OFF) : "memory");
}
#define WAIT_LGKM0() __builtin_amdgcn_s_waitcnt(0xC07F)
#define WAIT_VM(n) asm volatile("s_waitcnt vmcnt(" #n ")" ::: "memory")
#define WAIT_VM_BAR(n) asm volatile("s_waitcnt vmcnt(" #n ") lgkmcnt(0)\n\ts_barrier" ::: "memory")
#define LGKM_BAR() asm volatile("s_waitcnt lgkmcnt(0)\n\ts_barrier" ::: "memory")
DI bf16x8 lds_b128(unsigned a) { return *(const LAS bf16x8*)a; }
DI u32x4 lds_u128(unsigned a) { return *(const LAS u32x4*)a; }
DI bf16x8 lds_tr2(unsigned a0, unsigned a1) {
  s16x4 lo = __builtin_amdgcn_ds_read_tr16_b64_v4i16((LAS s16x4*)a0);
  s16x4 hi = __builtin_amdgcn_ds_read_tr16_b64_v4i16((LAS s16x4*)a1);
  s16x8 v = __builtin_shufflevector(lo, hi, 0, 1, 2, 3, 4, 5, 6, 7);
  return __builtin_bit_cast(bf16x8, v);
}
DI void mfma_vvv(f32x4& c, const bf16x8& a, const bf16x8& b) { asm volatile("v_mfma_f32_16x16x32_bf16 %0, %1, %2, %0" : "+v"(c) : "v"(a), "v"(b)); }
DI void mfma_vvv0(f32x4& c, const bf16x8& a, const bf16x8& b) { asm volatile("v_mfma_f32_16x16x32_bf16 %0, %1, %2, 0" : "=&v"(c) : "v"(a), "v"(b)); }
DI void mfma32(f32x16& c, const bf16x8& a, const bf16x8& b) { asm volatile("v_mfma_f32_32x32x16_bf16 %0, %1, %2, %0" : "+v"(c) : "v"(a), "v"(b)); }
DI void mfma_vav(f32x4& c, const u32x4& a, const u32x4& b) { asm volatile("v_mfma_f32_16x16x32_bf16 %0, %1, %2, %0" : "+v"(c) : "a"(a), "v"(b)); }
DI void mfma_vav0(f32x4& c, const u32x4& a, const u32x4& b) { asm volatile("v_mfma_f32_16x16x32_bf16 %0, %1, %2, 0" : "=&v"(c) : "a"(a), "v"(b)); }
#define MFMA_DRAIN() do { SB(); asm volatile("s_nop 7\n\ts_nop 4" ::: "memory"); SB(); } while (0)
constexpr int SC1 = 16;
DI u32x4 gld128(rsrc_t r, unsigned voff, unsigned soff) { return __builtin_amdgcn_raw_buffer_load_b128(r, voff, soff, SC1); }
DI unsigned gld32(rsrc_t r, unsigned voff, unsigned soff) { return __builtin_amdgcn_raw_buffer_load_b32(r, voff, soff, SC1); }
DI void gst128(u32x4 v, rsrc_t r, unsigned voff, unsigned soff) { __builtin_amdgcn_raw_buffer_store_b128(v, r, voff, soff, SC1); }
DI void gst64(u32x2 v, rsrc_t r, unsigned voff, unsigned soff) { __builtin_amdgcn_raw_buffer_store_b64(v, r, voff, soff, SC1); }
DI void gst32(unsigned v, rsrc_t r, unsigned voff, unsigned soff) { __builtin_amdgcn_raw_buffer_store_b32(v, r, voff, soff, SC1); }
#define RLX_AGENT __ATOMIC_RELAXED, __HIP_MEMORY_SCOPE_AGENT
DI u64 flag_ld(const char* a) { return __hip_atomic_load((const GAS u64*)a, RLX_AGENT); }
DI void flag_st(char* a, u64 v) { __hip_atomic_store((GAS u64*)a, v, RLX_AGENT); }
template <int N> DI void wait_flags(const char* base, u64 epoch, char* tmo, int lane) {
  for (unsigned spins = 0;; ++spins) {
    bool ok = true;
#pragma unroll
    for (int i = 0; i < (N + 63) / 64; ++i) { const int idx = i * 64 + lane; const u64 v = flag_ld(base + (idx < N ? idx : 0) * 8); ok &= (v == epoch); }
    if (__all(ok)) break;
    if (spins > 30000u) { if (lane == 0) flag_st(tmo, 1); break; }
    __builtin_amdgcn_s_sleep(1);
  }
  __builtin_amdgcn_fence(__ATOMIC_ACQUIRE, "wavefront");
}
DI f32x4 selv(unsigned m, f32x4 a, f32x4 b) {
  f32x4 r;
#pragma unroll
  for (int e = 0; e < 4; ++e) r[e] = __uint_as_float((__float_as_uint(a[e]) & m) | (__float_as_uint(b[e]) & ~m));
  return r;
}
DI unsigned gelu_mul2(f32x2 g, f32x2 u) {
  const f32x2 c1 = {-2.302208198f, -2.302208198f}, c2 = {-0.1029432397f, -0.1029432397f}, one = {1.f, 1.f};
  f32x2 a = g * __builtin_elementwise_fma(g * g, c2, c1);
  f32x2 e = {__builtin_amdgcn_exp2f(a[0]), __builtin_amdgcn_exp2f(a[1])};
  f32x2 d = e + one;
  f32x2 r = {__builtin_amdgcn_rcpf(d[0]), __builtin_amdgcn_rcpf(d[1])};
  f32x2 h = (g * u) * r;
  return cvtpk(h[0], h[1]);
}

DI float wave_sum(float a) {
  a += __builtin_bit_cast(float, __builtin_amdgcn_update_dpp(0, __builtin_bit_cast(int, a), 0xB1, 0xf, 0xf, true));
  a += __builtin_bit_cast(float, __builtin_amdgcn_update_dpp(0, __builtin_bit_cast(int, a), 0x4E, 0xf, 0xf, true));
  a += __builtin_bit_cast(float, __builtin_amdgcn_update_dpp(0, __builtin_bit_cast(int, a), 0x141, 0xf, 0xf, true));
  a += __builtin_bit_cast(float, __builtin_amdgcn_update_dpp(0, __builtin_bit_cast(int, a), 0x140, 0xf, 0xf, true));
  const int x = __builtin_bit_cast(int, a);
  return (__builtin_bit_cast(float, __builtin_amdgcn_readlane(x, 0)) + __builtin_bit_cast(float, __builtin_amdgcn_readlane(x, 16))) +
         (__builtin_bit_cast(float, __builtin_amdgcn_readlane(x, 32)) + __builtin_bit_cast(float, __builtin_amdgcn_readlane(x, 48)));
}
DI float dot2(unsigned a, unsigned b, float c) { return __builtin_amdgcn_fdot2_f32_bf16(__builtin_bit_cast(bf16x2, a), __builtin_bit_cast(bf16x2, b), c, false); }
struct Params { const u16 *q, *k, *v, *shk, *hk, *Dv, *wg, *wu, *wd, *gw; u16* out; char* ws; };

struct G1 {
  rsrc_t xs, wsr;
  unsigned xv[4], wv[7];
  unsigned sx, sw;
  unsigned sx0, sw0, ix, iw;
  unsigned mx, mw;
  unsigned xm0, wm0, slot;
  unsigned dx, dw;
  bool first;
  unsigned xa[4], xb[4], wt, w6;
  f32x16 acc[3][2], acc6;
  bf16x8 XA[2], XB[2], W[2][4];
};
template <int N> DI void g1_go(G1& c) {
  if constexpr (N < 4) dma_go<N * 1024>(c.xs, c.xv[N], c.sx);
  else dma_go<(N < 8 ? N - 4 : N - 8) * 1024>(c.wsr, c.wv[N - 4], c.sw);
}
template <int N> DI void g1_dma(G1& c, unsigned st, unsigned slot) {
  const unsigned m = N < 4 ? c.xm0 + slot * G1_XS : c.wm0 + slot * G1_WS + (N >= 8 ? 4096 : 0), ks = (c.ix + st) & 7;
  asm volatile("s_mov_b32 m0, %0\n\ts_nop 0" :: "s"(m) : "m0", "memory");
  if constexpr (N < 4) dma_go<N * 1024>(c.xs, c.xv[N], c.sx0 + ks * 128);
  else dma_go<(N < 8 ? N - 4 : N - 8) * 1024>(c.wsr, c.wv[N - 4], c.sw0 + ks * (64 * 3584));
}
template <int... Ns> DI void g1_dma_all(G1& c, unsigned st, unsigned slot, iseq<Ns...>) { (g1_dma<Ns>(c, st, slot), ...); }
template <int KK, int R> DI void g1_read(G1& c) {
  constexpr int set = KK & 1;
  if constexpr (R == 0) c.XA[set] = lds_b128(c.xa[KK]);
  else if constexpr (R == 2) c.XB[set] = lds_b128(c.xb[KK]);
  else {
    constexpr int p = R == 1 ? 0 : R - 2;
    constexpr unsigned o = KK * 16 * 224;
    if constexpr (p < 3) c.W[set][p] = lds_tr2(c.wt + o + p * 32, c.wt + o + p * 32 + 4 * 224);
    else c.W[set][p] = lds_tr2(c.w6 + o, c.w6 + o + 4 * 224);
  }
}
template <int KK, int M> DI void g1_mfma(G1& c) {
  constexpr int set = KK & 1;
  if constexpr (M < 6) mfma32(c.acc[M >> 1][M & 1], c.W[set][M >> 1], (M & 1) ? c.XB[set] : c.XA[set]);
  else mfma32(c.acc6, c.W[set][3], c.XA[set]);
}
struct Tail {
  rsrc_t hs, xr, wsr; unsigned hvoff, hm0, zm0, xvoff, wdso, xlds; const char* sflags; u64 epoch; char* tmo;
  u64 pf[4]; u32x4 xv[3];
};
DI void poll_issue(u64& v, const char* a) { asm volatile("global_load_dwordx2 %0, %1, off sc1" : "=v"(v) : "v"(a) : "memory"); }
template <int I> DI void tail_op(Tail& t, u32x4 (&Wd)[14][4], int lane) {
  if constexpr (I < 4) dma_go<I * 1024>(t.hs, t.hvoff, 0);
  else if constexpr (I == 4) dma_go<0>(t.hs, lane * 16, 0);
  else if constexpr (I < 9) dma_go<(I - 5) * 1024>(t.hs, t.hvoff + 2, 0);
  else if constexpr (I < 11) { }
  else if constexpr (I < 14) t.xv[I - 11] = gld128(t.xr, t.xvoff + (I - 11) * 1024, 0);
  else { constexpr int n = I - 14; Wd[n >> 2][n & 3] = gld128(t.wsr, lane * 16 + (n & 3) * 1024, t.wdso + (n >> 2) * 4096); }
}
constexpr int TAIL_WD = 12;
constexpr int vm_slot(int g) { constexpr int s[11] = {24, 26, 1, 3, 5, 8, 10, 12, 15, 17, 19}; for (int i = 0; i < 11; ++i) if (s[i] == g) return i; return -1; }
template <int MODE, int G> DI void g1_gap(G1& c, Tail& t, u32x4 (&Wd)[14][4], int lane) {
  constexpr int kk = G / 7, m = G % 7, v = vm_slot(G);
  if constexpr (MODE == 5 && G == 0) {
#pragma unroll
    for (int i = 0; i < 4; ++i) poll_issue(t.pf[i], t.sflags + (i * 64 + lane) * 8);
  }
  g1_mfma<kk, m>(c);
  if constexpr (kk < 3) { if constexpr (m < 6) g1_read<kk + 1, m>(c); }
  else if constexpr (MODE != 7) {
    if constexpr (m == 1) {
      SB();
      if constexpr (MODE == 5) asm volatile("s_waitcnt vmcnt(15) lgkmcnt(0)\n\ts_barrier" ::: "memory");
      else if constexpr (MODE == 6) asm volatile("s_waitcnt vmcnt(9) lgkmcnt(0)\n\ts_barrier" : "+v"(t.pf[0]), "+v"(t.pf[1]), "+v"(t.pf[2]), "+v"(t.pf[3]) :: "memory");
      else if constexpr (MODE == 0) { if (c.first) asm volatile("s_waitcnt vmcnt(15)" ::: "memory"); else asm volatile("s_waitcnt vmcnt(11)" ::: "memory"); asm volatile("s_waitcnt lgkmcnt(0)\n\ts_barrier" ::: "memory"); c.first = false; }
      else asm volatile("s_waitcnt vmcnt(11) lgkmcnt(0)\n\ts_barrier" ::: "memory");
      if constexpr (MODE == 6) {
        const bool ok = t.pf[0] == t.epoch && t.pf[1] == t.epoch && t.pf[2] == t.epoch && t.pf[3] == t.epoch;
        if (!__all(ok)) wait_flags<256>(t.sflags, t.epoch, t.tmo, lane);
        __builtin_amdgcn_fence(__ATOMIC_ACQUIRE, "wavefront");
      }
    }
    if constexpr (m == 2) { g1_read<0, 0>(c); g1_read<0, 1>(c); }
    if constexpr (m == 3) { g1_read<0, 2>(c); g1_read<0, 3>(c); }
    if constexpr (m == 4) g1_read<0, 4>(c);
    if constexpr (m == 5) g1_read<0, 5>(c);
  }
  if constexpr (v >= 0) {
    if constexpr (MODE == 0) g1_go<v>(c);
    if constexpr (MODE == 5) { if constexpr (v >= 2) g1_go<v>(c); else tail_op<v>(t, Wd, lane); }
    if constexpr (MODE == 6) { if constexpr (v >= 2) tail_op<v>(t, Wd, lane); else tail_op<11 + v>(t, Wd, lane); }
    if constexpr (MODE == 7) { if constexpr (v >= 2) tail_op<11 + v>(t, Wd, lane); else tail_op<23 + v>(t, Wd, lane); }
  }
  if constexpr (MODE == 7 && G == 22) tail_op<22>(t, Wd, lane);
  if constexpr (MODE == 7 && G == 27) tail_op<25>(t, Wd, lane);
  if constexpr (MODE == 7 && (G == 20 || G == 21 || G == 23)) { constexpr int i = G == 20 ? 0 : G == 21 ? 1 : 2; *(LAS u32x4*)(t.xlds + i * 1024) = t.xv[i]; }
  if constexpr (MODE == 0 || MODE == 5) {
    if constexpr (G == 4) m0_set(c.mw);
    if constexpr (G == 6) { c.ix = (c.ix + 1) & 7; c.sx = c.sx0 + c.ix * 128; asm volatile("" : "+s"(c.ix), "+s"(c.sx)); }
    if constexpr (G == 14) m0_set(c.mw + 4096);
    if constexpr (G == 20) { c.iw = (c.iw + 1) & 7; c.sw = c.sw0 + c.iw * (64 * 3584); asm volatile("" : "+s"(c.iw), "+s"(c.sw)); }
  }
  if constexpr (MODE == 0 && G == 23) m0_set(c.mx);
  if constexpr (MODE == 5 && G == 23) m0_set(t.hm0);
  if constexpr (MODE == 6 && G == 4) m0_set(t.zm0);
  if constexpr (MODE == 6 && G == 7) m0_set(t.hm0 + CV_HS);
  if constexpr (MODE != 7) {
    if constexpr (G == 9) { c.xa[0] += c.dx; c.xb[0] += c.dx; }
    if constexpr (G == 11) { c.xa[1] += c.dx; c.xb[1] += c.dx; }
    if constexpr (G == 16) { c.xa[2] += c.dx; c.xb[2] += c.dx; }
    if constexpr (G == 21) { c.xa[3] += c.dx; c.xb[3] += c.dx; c.wt += c.dw; c.w6 += c.dw; }
  }
  if constexpr (MODE == 0) {
    if constexpr (G == 25) { c.mw = c.wm0 + c.slot * G1_WS; c.slot = c.slot == 2 ? 0 : c.slot + 1; asm volatile("" : "+s"(c.mw), "+s"(c.slot)); }
    if constexpr (G == 27) {
      c.dx = c.slot == 2 ? 0u - 2 * G1_XS : G1_XS; c.dw = c.slot == 2 ? 0u - 2 * G1_WS : G1_WS; c.mx = c.xm0 + c.slot * G1_XS;
      asm volatile("" : "+s"(c.dx), "+s"(c.dw), "+s"(c.mx));
    }
  }
  SB();
}
template <int MODE, int... Gs> DI void g1_stage(G1& c, Tail& t, u32x4 (&Wd)[14][4], int lane, iseq<Gs...>) { (g1_gap<MODE, Gs>(c, t, Wd, lane), ...); }
template <int... Rs> DI void g1_read0(G1& c, iseq<Rs...>) { (g1_read<0, Rs>(c), ...); }

struct ConvCtx { unsigned abase, bbase, hm; u32x4 a0; const char* hfl; unsigned wdso; int lane; rsrc_t wsr; };
constexpr int cv_al(int m) { int al = 0, n = 8; while (m >= n) { m -= n; --n; ++al; } return al; }
constexpr int cv_be(int m) { int n = 8; while (m >= n) { m -= n; --n; } return m; }
template <int RR, int M> DI void conv_gap(const ConvCtx& cx, f32x4 (&acc)[8], u32x2 (&alo)[2][8], u32x2 (&ahi)[2][8], bf16x8 (&B)[2][8], u32x4 (&Wd)[14][4], u64& hflag) {
  constexpr int set = RR & 1, al = cv_al(M), be = cv_be(M);
  if constexpr (M == 0) WAIT_LGKM0();
  u32x4 av = {alo[set][al][0], alo[set][al][1], ahi[set][al][0], ahi[set][al][1]};
  if constexpr (RR == 0 && al == 0) {
#pragma unroll
    for (int e = 0; e < 4; ++e) av[e] = (av[e] & cx.hm) | (cx.a0[e] & ~cx.hm);
  }
  bf16x8 A = __builtin_bit_cast(bf16x8, av);
  if constexpr (RR == 0 && al == 0) { asm volatile("s_nop 1" : "+v"(A)); mfma_vvv0(acc[be], A, B[set][be]); }
  else mfma_vvv(acc[al + be], A, B[set][be]);
  if constexpr (RR == 3 && M == 1) hflag = flag_ld(cx.hfl);
  if constexpr (M % 5 == 3) {
    constexpr int n = 28 + RR * 7 + M / 5;
    if constexpr (n < 56) Wd[n >> 2][n & 3] = gld128(cx.wsr, cx.lane * 16 + (n & 3) * 1024, cx.wdso + (n >> 2) * 4096);

  } else if constexpr (RR < 3) {
    constexpr int rd = M - (M + 1) / 5;
    if constexpr (rd < 24) {
      constexpr int i = rd / 3, k = rd % 3;
      if constexpr (k == 0) alo[set ^ 1][i] = *(const volatile LAS u32x2*)(cx.abase + (RR + 1) * 64 + i * 512);
      else if constexpr (k == 1) ahi[set ^ 1][i] = *(const volatile LAS u32x2*)(cx.abase + (RR + 1) * 64 + i * 512 + 8);
      else B[set ^ 1][i] = lds_b128(cx.bbase + 64 * (56 - 8 * i) + 64 * (RR + 1));
    }
  }
  SB();
}
template <int RR, int... Ms> DI void conv_rho(const ConvCtx& cx, f32x4 (&acc)[8], u32x2 (&alo)[2][8], u32x2 (&ahi)[2][8], bf16x8 (&B)[2][8], u32x4 (&Wd)[14][4], u64& hflag, iseq<Ms...>) {
  (conv_gap<RR, Ms>(cx, acc, alo, ahi, B, Wd, hflag), ...);
}

struct G2Ctx {
  rsrc_t wsr; const char* yfl; u64 epoch; char* tmo; int lane;
  unsigned yvo, yso, gvo, gso; const u16* qp; unsigned hso, rbase; int gq, li; char* myy;
  struct { char* ws; } p;
};
constexpr int G2R = 16, G2PD = 12;
template <int J> DI void g2_ldh(const G2Ctx& g, u32x4 (&H)[G2R]) { H[J % G2R] = gld128(g.wsr, g.lane * 16 + (J % 14 & 3) * 1024, g.hso + (J / 14) * 57344 + (J % 14 & ~3) * 1024); }
template <int P, int DT> DI void g2_put(const G2Ctx& g, f32x4 (&acc)[4][4]) {
  *(LAS f32x4*)(g.rbase + P * 4096 + (((4 * DT + g.gq) ^ g.li) << 4)) = acc[P][DT];
}
template <int J> DI void g2_step(const G2Ctx& g, f32x4 (&acc)[4][4], u32x4 (&Wd)[14][4], u32x4 (&H)[G2R], u64& yflag, unsigned (&yv)[8], u32x2& gt, u32x4 (&qv)[2]) {
  constexpr int P = J / 14, I = J % 14;
  if constexpr (I == 0) { auto& p = g.p; (void)p; DSTAMP(16 + 3 * P); }
  if constexpr (J == 44) {
    if (!__all(yflag == g.epoch)) wait_flags<32>(g.yfl, g.epoch, g.tmo, g.lane);
    __builtin_amdgcn_fence(__ATOMIC_ACQUIRE, "wavefront");
    SB();
  }
  if constexpr (I == 0) mfma_vav0(acc[P][0], Wd[I][0], H[J % G2R]); else mfma_vav(acc[P][0], Wd[I][0], H[J % G2R]);
  if constexpr (J + G2PD < 56) g2_ldh<J + G2PD>(g, H);
  if constexpr (J == 36) yflag = flag_ld(g.yfl + (g.lane & 31) * 8);
  if constexpr (J >= 44 && J < 52) yv[J - 44] = gld32(g.wsr, g.yvo + (J - 44) * 4096, g.yso);
  if constexpr (J == 52) gt = __builtin_amdgcn_raw_buffer_load_b64(g.wsr, g.gvo, g.gso, SC1);
  if constexpr (J == 53) qv[0] = *(const u32x4*)g.qp;
  if constexpr (J == 54) qv[1] = *(const u32x4*)(g.qp + D_);
  SB();
  if constexpr (I == 0) mfma_vav0(acc[P][1], Wd[I][1], H[J % G2R]); else mfma_vav(acc[P][1], Wd[I][1], H[J % G2R]);
  if constexpr (I == 0) mfma_vav0(acc[P][2], Wd[I][2], H[J % G2R]); else mfma_vav(acc[P][2], Wd[I][2], H[J % G2R]);
  if constexpr (P > 0 && I >= 2 && I < 6) g2_put<(P > 0 ? P - 1 : 0), (I >= 2 && I < 6 ? I - 2 : 0)>(g, acc);
  SB();
  if constexpr (I == 0) mfma_vav0(acc[P][3], Wd[I][3], H[J % G2R]); else mfma_vav(acc[P][3], Wd[I][3], H[J % G2R]);
  SB();
  if constexpr (J == 8) {
    WAIT_VM(12); asm volatile("s_barrier" ::: "memory");
    if (threadIdx.x == 0) flag_st(g.myy, g.epoch);
    SB();
  }
}
template <int... Js> DI void g2_loop(const G2Ctx& g, f32x4 (&acc)[4][4], u32x4 (&Wd)[14][4], u32x4 (&H)[G2R], u64& yflag, unsigned (&yv)[8], u32x2& gt, u32x4 (&qv)[2], iseq<Js...>) {
  (g2_step<Js>(g, acc, Wd, H, yflag, yv, gt, qv), ...);
}
template <int... Js> DI void g2_ldh_range(const G2Ctx& g, u32x4 (&H)[G2R], iseq<Js...>) { (g2_ldh<Js>(g, H), ...); }
template <int B, int... Js> DI void g2_ldh_from(const G2Ctx& g, u32x4 (&H)[G2R], iseq<Js...>) { (g2_ldh<B + Js>(g, H), ...); }

__global__ __launch_bounds__(256, 1) void k_fused(Params p) {
  __shared__ __attribute__((aligned(16))) char smem[LDS_BYTES];
  const unsigned lds0 = (unsigned)(size_t)(LAS char*)smem;
  const int lane = threadIdx.x & 63;
  const unsigned w = rfl(threadIdx.x >> 6);
  const unsigned b = blockIdx.x, xcd = b & 7, jb = b >> 3;
  char* const ws = p.ws;
  STAMP_DECL; STAMP(0);
  asm volatile("" :: "s"(p.q), "s"(p.k), "s"(p.v), "s"(p.shk), "s"(p.hk), "s"(p.Dv), "s"(p.wg), "s"(p.wu), "s"(p.wd), "s"(p.gw), "s"(p.out), "s"(p.ws));
  const rsrc_t wsr = mk_rsrc(ws, WS_END);
  u64 eword;
  asm volatile("global_load_dwordx2 %0, %1, off" : "=v"(eword) : "v"(ws + WS_E) : "memory");

  const unsigned sb = b >> 3, db = b & 7, s0p = sb * 64, d0p = db * 64;
  const unsigned kg_w = b >> 2, cq_w = b & 3;
  {
    const unsigned r8 = lane >> 3, c8 = lane & 7;
    const rsrc_t rk = mk_rsrc(p.k, S_ * D_ * 2), rv = mk_rsrc(p.v, S_ * D_ * 2), rw = mk_rsrc(p.wd, F_ * D_ * 2);
    auto dk = [&](int i) { int row = (int)s0p - 3 + 8 * i + (int)r8; unsigned vo = row >= 0 ? (unsigned)row * 1024 + d0p * 2 + c8 * 16 : 0x80000000u; dma16(rk, vo, 0, lds0 + ST_K + i * 1024); };
    auto dv = [&](int i) { dma16(rv, r8 * 1024 + c8 * 16, (s0p + 8 * i) * 1024 + d0p * 2, lds0 + ST_V + i * 1024); };
    auto dw = [&](int i) { dma16(rw, (lane >> 4) * 1024 + (lane & 15) * 16, (kg_w * 32 + 4 * i) * 1024 + cq_w * 256, lds0 + ST_WD + i * 1024); };
    if (w == 0) { dk(0); dk(1); dk(2); dk(3); dk(4); dk(5); dk(6); }
    else if (w == 1) { dk(7); dk(8); dv(0); dv(1); dv(2); dv(3); dv(4); }
    else if (w == 2) { dv(5); dv(6); dv(7); dma16(mk_rsrc(p.shk, D_ * 8), lane * 16, d0p * 8, lds0 + ST_SH); dw(0); dw(1); dw(2); }
    else { dw(3); dw(4); dw(5); dw(6); dw(7); }
  }
  SB();
  const unsigned rb = 8 * (xcd >> 2) + (jb & 7), fc = 4 * (xcd & 3) + (jb >> 3);
  const unsigned mi = w >> 1, ni = w & 1;
  G1 c;
  c.xs = mk_rsrc(p.q, S_ * D_ * 2);
  c.wsr = mk_rsrc((w >> 1) ? p.wu : p.wg, D_ * F_ * 2);
#pragma unroll
  for (int n = 0; n < 4; ++n) c.xv[n] = (lane >> 3) * 1024 + (((lane & 7) ^ ((4 * (n & 1) + (lane >> 4)) & 7)) << 4) + n * (8192 - 1024);
#pragma unroll
  for (int jj = 0; jj < 7; ++jj) {
    const unsigned ci = 64 * (7 * (w & 1) + jj) + lane, k = (ci * 4682u) >> 16, pc = ci - k * 14;
    c.wv[jj] = k * 3584 + pc * 16 - (jj & 3) * 1024;
  }
  c.sx0 = (rb * 128 + 32 * w) * 1024;
  c.sw0 = fc * 224;
  c.ix = ROT == 0 ? 0 : ROT == 1 ? (rb + 2 * (fc & 3)) & 7 : ROT == 2 ? (rb & 7) : (2 * (fc & 3)) & 7;
  c.xm0 = lds0 + G1_XOFF + w * 4096;
  c.wm0 = lds0 + G1_WOFF + (w >> 1) * G1_WIMG + (w & 1) * 7 * 1024;
  g1_dma_all(c, 0, 0, make_iseq<11>{});
  {
    const int r = lane & 31, hh = lane >> 5;
    const unsigned base = r * 128 + ((hh ^ ((r >> 1) & 7)) << 4);
#pragma unroll
    for (int kk = 0; kk < 4; ++kk) {
      c.xa[kk] = lds0 + G1_XOFF + (2 * mi + ni) * 4096 + (base ^ (kk << 5));
      c.xb[kk] = lds0 + G1_XOFF + (2 * mi + (1 - ni)) * 4096 + (base ^ (kk << 5));
    }
    const unsigned wb = lds0 + G1_WOFF + ((lane >> 4) & 1) * G1_WIMG + (8 * hh + ((lane & 15) >> 2)) * 224 + (lane & 3) * 8;
    c.wt = wb + ni * 96; c.w6 = wb + 6 * 32;
  }
  SB();
  STAMP(1);
  asm volatile("s_waitcnt vmcnt(11)\n\ts_barrier" : "+v"(eword) :: "memory");
  const u64 epoch = ((u64)rfl((unsigned)(eword >> 32)) << 32 | rfl((unsigned)eword)) * 6364136223846793005ull + 1442695040888963407ull;
  STAMP(2);
  {
    const int t = threadIdx.x, c4 = t & 15, rg = t >> 4;
    u32x2 kr[7], vr[4]; u32x4 w4[2];
#pragma unroll
    for (int i = 0; i < 7; ++i) kr[i] = *(const LAS u32x2*)(lds0 + ST_K + (4 * rg + i) * 128 + c4 * 8);
#pragma unroll
    for (int i = 0; i < 4; ++i) vr[i] = *(const LAS u32x2*)(lds0 + ST_V + (4 * rg + i) * 128 + c4 * 8);
#pragma unroll
    for (int i = 0; i < 2; ++i) w4[i] = lds_u128(lds0 + ST_SH + c4 * 32 + i * 16);
    const int li = lane & 15, gq = lane >> 4;
    bf16x8 wf[2];
#pragma unroll
    for (int i = 0; i < 2; ++i) { const unsigned a = lds0 + ST_WD + (8 * gq + (li >> 2)) * 256 + (2 * w + i) * 32 + (lane & 3) * 8; wf[i] = lds_tr2(a, a + 4 * 256); }
    g1_dma<0>(c, 1, 1); g1_dma<1>(c, 1, 1);
    SB();
#pragma unroll
    for (int j = 0; j < 4; ++j) {
      const unsigned wa = w4[j >> 1][(j & 1) * 2], wb = w4[j >> 1][(j & 1) * 2 + 1];
      const float w0 = bflo(wa), w1 = bfhi(wa), w2 = bflo(wb), w3 = bfhi(wb);
      float kk[7], o[4];
#pragma unroll
      for (int i = 0; i < 7; ++i) kk[i] = (j & 1) ? bfhi(kr[i][j >> 1]) : bflo(kr[i][j >> 1]);
      if (j == 0) g1_dma<2>(c, 1, 1); if (j == 1) g1_dma<4>(c, 1, 1); if (j == 2) g1_dma<6>(c, 1, 1); if (j == 3) g1_dma<8>(c, 1, 1);
      SB();
#pragma unroll
      for (int i = 0; i < 4; ++i) {
        const float vv = (j & 1) ? bfhi(vr[i][j >> 1]) : bflo(vr[i][j >> 1]);
        o[i] = (kk[i] * w3 + kk[i + 1] * w2 + kk[i + 2] * w1 + kk[i + 3] * w0) * vv;
      }
      const int wd = 30 - 2 * rg, dl = 4 * c4 + j;
      *(LAS u32x2*)(lds0 + ST_T + dl * 128 + ((((wd >> 2) ^ c4) & 7) << 4) + (wd & 3) * 4) = u32x2{cvtpk(o[3], o[2]), cvtpk(o[1], o[0])};
      if (j == 0) g1_dma<3>(c, 1, 1); if (j == 1) g1_dma<5>(c, 1, 1); if (j == 2) g1_dma<7>(c, 1, 1); if (j == 3) g1_dma<9>(c, 1, 1);
      SB();
    }
    g1_dma<10>(c, 1, 1);
    SB();
    LGKM_BAR();
    u32x4 val[2];
#pragma unroll
    for (int i = 0; i < 2; ++i) { const int dl = (w * 2 + i) * 8 + (lane >> 3), ch = lane & 7; val[i] = lds_u128(lds0 + ST_T + dl * 128 + ((ch ^ (dl >> 2)) & 7) * 16); }
#pragma unroll
    for (int i = 0; i < 2; ++i) {
      const unsigned D16 = 8 * cq_w + 2 * w + i;
      gst128(__builtin_bit_cast(u32x4, wf[i]), wsr, kg_w < 56 ? WS_WDT + (((D16 >> 2) * 56 + kg_w) * 4 + (D16 & 3)) * 1024 + lane * 16 : 0x80000000u, 0);
    }
#pragma unroll
    for (int i = 0; i < 2; ++i) {
      const int dl = (w * 2 + i) * 8 + (lane >> 3), ch = lane & 7;
      gst128(val[i], wsr, WS_KVT + (d0p + dl) * 4096 + (1984 - s0p) * 2 + ch * 16, 0);
    }
    SB();
  }
  STAMP(3);
  asm volatile("s_waitcnt vmcnt(15) lgkmcnt(0)\n\ts_barrier" ::: "memory");
  g1_read0(c, make_iseq<6>{});
  g1_dma<0>(c, 2, 2); g1_dma<1>(c, 2, 2);
  c.ix = (c.ix + 2) & 7; c.iw = c.ix; c.sx = c.sx0 + c.ix * 128; c.sw = c.sw0 + c.iw * (64 * 3584);
  c.slot = 0; c.mx = c.xm0; c.mw = c.wm0 + 2 * G1_WS; c.dx = G1_XS; c.dw = G1_WS; c.first = true;
#pragma unroll
  for (int i = 0; i < 3; ++i) { c.acc[i][0] = f32x16{}; c.acc[i][1] = f32x16{}; }
  c.acc6 = f32x16{};
  SB();
  asm volatile("s_nop 1" ::: "memory");
  SB();
  STAMP(4);
  const unsigned cch = w >> 1, hf = w & 1, dch = b * 2 + cch, cvb = lds0 + CV_BASE + cch * CV_CH, cvz = lds0 + CV_Z + cch * 1088;
  const unsigned rb2 = 4 * xcd + (jb & 3), cb = jb >> 2;
  u32x4 Wd[14][4];
  Tail t;
  t.hs = mk_rsrc(p.hk + (size_t)dch * S_, (D_ - dch) * S_ * 2);
  t.hvoff = lane * 16 + (2 * hf + 17) * 2; t.hm0 = cvb + 2 * hf * CV_HS; t.zm0 = cvz + 32;
  t.xr = mk_rsrc(ws + WS_KVT + dch * 4096, 4096); t.xvoff = (hf * 128 + lane) * 16; t.xlds = cvb + CV_H + (hf * 128 + lane) * 16;
  t.wsr = wsr; t.wdso = WS_WDT + (cb * 56 + 14 * w) * 4096;
  t.sflags = ws + WS_FS; t.epoch = epoch; t.tmo = ws + WS_TMO;
  {
#pragma unroll 1
    for (unsigned s = 0; s < 5; ++s) {
      DSTAMP(s);
      g1_stage<0>(c, t, Wd, lane, make_iseq<28>{});
      if (s == 1 && threadIdx.x == 0) flag_st(ws + WS_FS + b * 8, epoch);
    }
    DSTAMP(5);
    g1_stage<5>(c, t, Wd, lane, make_iseq<28>{});
    if (lane < 8) *(LAS unsigned*)(cvz + lane * 4) = 0;
    c.dx = G1_XS; c.dw = G1_WS;
    DSTAMP(6);
    g1_stage<6>(c, t, Wd, lane, make_iseq<28>{});
    if (b == 0 && threadIdx.x == 0) flag_st(ws + WS_E, epoch);
    DSTAMP(7);
    g1_stage<7>(c, t, Wd, lane, make_iseq<28>{});
    DSTAMP(8);
  }
  MFMA_DRAIN();
  STAMP(5);
  auto ldWd = [&](int n) { Wd[n >> 2][n & 3] = gld128(wsr, lane * 16 + (n & 3) * 1024, t.wdso + (n >> 2) * 4096); };
  {
    const unsigned grow = 128 * rb + 8 * fc + 2 * w;
    const u32x4 gx0 = *(const u32x4*)(p.q + (size_t)grow * D_ + lane * 8), gx1 = *(const u32x4*)(p.q + (size_t)(grow + 1) * D_ + lane * 8), gw4 = *(const u32x4*)(p.gw + lane * 8);
    SB();
    const int s32 = lane & 31, hh = lane >> 5;
    const unsigned lo = (s32 >> 4) * 57344 + (s32 & 15) * 16 + 8 * hh;
    const unsigned hb = WS_H + rb * 8 * 57344 + fc * 7 * 512;
    const unsigned bA = hb + (2 * mi + ni) * 2 * 57344, bB = hb + (2 * mi + (1 - ni)) * 2 * 57344, t3 = 3 * ni * 512;
    const unsigned sA = bA + t3, sB = bB + t3, s6 = bA + 6 * 512;
    auto put = [&](int n0, int nl, const f32x16& a, unsigned so, int imm) {
      if (nl > 0) ldWd(n0);
      u32x2 o0 = {gelu_mul2(f32x2{a[0], a[1]}, f32x2{a[8], a[9]}), gelu_mul2(f32x2{a[2], a[3]}, f32x2{a[10], a[11]})};
      gst64(o0, wsr, lo + imm, so);
      SB();
      if (nl > 1) ldWd(n0 + 1);
      u32x2 o1 = {gelu_mul2(f32x2{a[4], a[5]}, f32x2{a[12], a[13]}), gelu_mul2(f32x2{a[6], a[7]}, f32x2{a[14], a[15]})};
      if (nl > 2) ldWd(n0 + 2);
      gst64(o1, wsr, lo + imm + 256, so);
      SB();
    };
    put(12, 3, c.acc[0][0], sA, 0); put(15, 3, c.acc[0][1], sB, 0);
    put(18, 2, c.acc[1][0], sA, 512); put(20, 2, c.acc[1][1], sB, 512);
    put(22, 2, c.acc[2][0], sA, 1024); put(24, 2, c.acc[2][1], sB, 1024);
    put(26, 2, c.acc6, s6, 0);
    float gsum[2] = {0.f, 0.f};
#pragma unroll
    for (int j = 0; j < 4; ++j) { gsum[0] = dot2(gx0[j], gw4[j], gsum[0]); gsum[1] = dot2(gx1[j], gw4[j], gsum[1]); }
#pragma unroll
    for (int r = 0; r < 2; ++r) gsum[r] = __builtin_amdgcn_rcpf(1.f + __builtin_amdgcn_exp2f(-1.4426950409f * wave_sum(gsum[r])));
    gst32(__float_as_uint(lane & 1 ? gsum[1] : gsum[0]), wsr, lane < 2 ? WS_G + (grow + lane) * 4 : 0x80000000u, 0);
    SB();
  }
  LGKM_BAR();
  STAMP(6);

  const int tt = threadIdx.x, cc = tt & 7, sp = tt >> 3;
  const unsigned s0 = rb2 * 64, d0 = cb * 64;
  u32x4 H[G2R];
  const G2Ctx g2{wsr, ws + WS_FY + cb * 256, epoch, ws + WS_TMO, lane, (unsigned)(8 * cc * 4096 + sp * 4), WS_Y2T + d0 * 4096 + s0 * 2, (unsigned)(sp * 8), WS_G + s0 * 4,
                 p.q + (size_t)(s0 + 2 * sp) * D_ + d0 + cc * 8, WS_H + ((4 * rb2) * 56 + 14 * w) * 1024, lds0 + w * 16384 + (lane & 15) * 256, lane >> 4, lane & 15,
                 ws + WS_FY + (b >> 5) * 256 + (b & 31) * 8, {ws}};
  {
    const int li = lane & 15, gq = lane >> 4;
    u32x4 a0;
#pragma unroll
    for (int e = 0; e < 4; ++e) {
      const unsigned o = cvz + (li + 8 * gq + 2 * e + 1) * 2;
      a0[e] = (unsigned)*(const LAS u16*)o | ((unsigned)*(const LAS u16*)(o + 2) << 16);
    }
    const float Dd = __uint_as_float((unsigned)p.Dv[dch] << 16);
    const unsigned abase = cvb + (li & 3) * CV_HS + (li >> 2) * 8 + gq * 16 + hf * 4 * 64 - 64;
    const unsigned bbase = cvb + CV_H + (254 - 2 * li + gq) * 16 - 64 * 56 + hf * 4 * 64;
    f32x4 acc[8];
    u32x2 alo[2][8], ahi[2][8];
    bf16x8 B[2][8];
#pragma unroll
    for (int i = 0; i < 8; ++i) {
      alo[0][i] = *(const volatile LAS u32x2*)(abase + i * 512);
      ahi[0][i] = *(const volatile LAS u32x2*)(abase + i * 512 + 8);
      B[0][i] = lds_b128(bbase + 64 * (56 - 8 * i));
    }
    SB();
    STAMP(7);
    u64 hflag = 0;
    ConvCtx cx{abase, bbase, 0u - hf, a0, ws + WS_FH + (rb2 >> 1) * 128 + li * 8, t.wdso, lane, wsr};
    DSTAMP(9);
    conv_rho<0>(cx, acc, alo, ahi, B, Wd, hflag, make_iseq<36>{});
    DSTAMP(10);
    WAIT_VM(7); asm volatile("s_barrier" ::: "memory");
    if (threadIdx.x == 0) flag_st(ws + WS_FH + rb * 128 + fc * 8, epoch);
    SB();
    DSTAMP(11);
    conv_rho<1>(cx, acc, alo, ahi, B, Wd, hflag, make_iseq<36>{});
    conv_rho<2>(cx, acc, alo, ahi, B, Wd, hflag, make_iseq<36>{});
    conv_rho<3>(cx, acc, alo, ahi, B, Wd, hflag, make_iseq<36>{});
    DSTAMP(12);
    STAMP(8);
    if (!__all(hflag == epoch)) wait_flags<16>(ws + WS_FH + (rb2 >> 1) * 128, epoch, ws + WS_TMO, lane);
    __builtin_amdgcn_fence(__ATOMIC_ACQUIRE, "wavefront");
    DSTAMP(13);
    g2_ldh_range(g2, H, make_iseq<4>{});
    MFMA_DRAIN();
    const unsigned red = lds0 + CV_RED, hm = 0u - hf;
#pragma unroll
    for (int g = 0; g < 4; ++g) *(LAS f32x4*)(red + w * 4096 + g * 1024 + lane * 16) = selv(hm, acc[g], acc[4 + g]);
    SB();
    g2_ldh_from<4>(g2, H, make_iseq<4>{});
    SB();
    DSTAMP(14);
    LGKM_BAR();
    DSTAMP(15);
    f32x4 oth[4]; u32x2 kvr[4];
#pragma unroll
    for (int g = 0; g < 4; ++g) {
      oth[g] = *(const LAS f32x4*)(red + (w ^ 1) * 4096 + g * 1024 + lane * 16);
      kvr[g] = *(const LAS u32x2*)(cvb + CV_H + (2044 - (256 * (g + 4 * hf) + 16 * li + 4 * gq)) * 2);
    }
#pragma unroll
    for (int g = 0; g < 4; ++g) {
      f32x4 y = selv(hm, acc[4 + g], acc[g]) + oth[g];
      float r0 = y[0] + bfhi(kvr[g][1]) * Dd, r1 = y[1] + bflo(kvr[g][1]) * Dd, r2 = y[2] + bfhi(kvr[g][0]) * Dd, r3 = y[3] + bflo(kvr[g][0]) * Dd;
      gst64(u32x2{cvtpk(r0, r1), cvtpk(r2, r3)}, wsr, (16 * li + 4 * gq) * 2 + g * 512, WS_Y2T + dch * 4096 + hf * 2048);
    }
    g2_ldh_from<8>(g2, H, make_iseq<4>{});
    SB();
  }
  STAMP(9);

  unsigned yv[8]; u32x2 gt; u32x4 qv[2];
  {
    f32x4 acc[4][4];
    u64 yflag = 0;
    g2_loop(g2, acc, Wd, H, yflag, yv, gt, qv, make_iseq<56>{});
    DSTAMP(28);
    MFMA_DRAIN();
    g2_put<3, 0>(g2, acc); g2_put<3, 1>(g2, acc); g2_put<3, 2>(g2, acc); g2_put<3, 3>(g2, acc);
    DSTAMP(29);
    STAMP(10);
  }
  LGKM_BAR();
  DSTAMP(30);
  {
#pragma unroll
    for (int r = 0; r < 2; ++r) {
      const int s = 2 * sp + r;
      f32x4 b0 = {0, 0, 0, 0}, b1 = {0, 0, 0, 0};
#pragma unroll
      for (int ww = 0; ww < 4; ++ww) {
        b0 += *(const LAS f32x4*)(lds0 + ww * 16384 + s * 256 + (((2 * cc) ^ (s & 15)) << 4));
        b1 += *(const LAS f32x4*)(lds0 + ww * 16384 + s * 256 + (((2 * cc + 1) ^ (s & 15)) << 4));
      }
      const float g = __uint_as_float(gt[r]);
      u32x4 o;
#pragma unroll
      for (int e = 0; e < 4; ++e) {
        float bl = e < 2 ? b0[2 * e] : b1[2 * e - 4], bh = e < 2 ? b0[2 * e + 1] : b1[2 * e - 3];
        float yl = r ? bfhi(yv[2 * e]) : bflo(yv[2 * e]), yh = r ? bfhi(yv[2 * e + 1]) : bflo(yv[2 * e + 1]);
        float al = bflo(qv[r][e]) * yl, ah = bfhi(qv[r][e]) * yh;
        o[e] = cvtpk(g * al + (1.f - g) * bl, g * ah + (1.f - g) * bh);
      }
      *(u32x4*)(p.out + (size_t)(s0 + s) * D_ + d0 + cc * 8) = o;
    }
  }
  STAMP(11);
  STAMP_FLUSH(12);
}

extern "C" void kernel_launch(void* const* d_in, const int* in_sizes, int n_in, void* d_out, int out_size, void* d_ws, size_t ws_size, hipStream_t stream) {
  Params p;
  p.q = (const u16*)d_in[0]; p.k = (const u16*)d_in[1]; p.v = (const u16*)d_in[2];
  p.shk = (const u16*)d_in[3]; p.hk = (const u16*)d_in[4]; p.Dv = (const u16*)d_in[5];
  p.wg = (const u16*)d_in[7]; p.wu = (const u16*)d_in[8]; p.wd = (const u16*)d_in[9]; p.gw = (const u16*)d_in[10];
  p.out = (u16*)d_out; p.ws = (char*)d_ws;
  k_fused<<<256, 256, 0, stream>>>(p);
}
```

```cpp
#include <hip/hip_runtime.h>
#include <hip/hip_bf16.h>
#include <utility>
#pragma clang diagnostic ignored "-Winline-asm"
#pragma clang diagnostic ignored "-Wint-to-pointer-cast"

#define DI __device__ __forceinline__
#define LAS __attribute__((address_space(3)))
#define GAS __attribute__((address_space(1)))
typedef unsigned short u16;
typedef unsigned long long u64;
typedef float f32x16 __attribute__((ext_vector_type(16)));
typedef float f32x4 __attribute__((ext_vector_type(4)));
typedef float f32x2 __attribute__((ext_vector_type(2)));
typedef __bf16 bf16x8 __attribute__((ext_vector_type(8)));
typedef __bf16 bf16x2 __attribute__((ext_vector_type(2)));
typedef short s16x4 __attribute__((ext_vector_type(4)));
typedef short s16x8 __attribute__((ext_vector_type(8)));
typedef unsigned u32x4 __attribute__((ext_vector_type(4)));
typedef unsigned u32x2 __attribute__((ext_vector_type(2)));
typedef __amdgpu_buffer_rsrc_t rsrc_t;
template <int... Is> using iseq = std::integer_sequence<int, Is...>;
template <int N> using make_iseq = std::make_integer_sequence<int, N>;

constexpr int S_ = 2048, D_ = 512, F_ = 1792;
constexpr unsigned WS_E = 0, WS_FA = 256, WS_FW = 2560, WS_FH = 4096, WS_FY = 8192, WS_TMO = 12288, WS_FK = 13312, WS_FG = 15360;
constexpr unsigned WS_G   = 16384;
constexpr unsigned WS_KVT = 1u << 20;
constexpr unsigned WS_Y2T = WS_KVT + (2u << 20);
constexpr unsigned WS_WDT = WS_Y2T + (2u << 20);
constexpr unsigned WS_H   = WS_WDT + (2u << 20);
constexpr unsigned WS_END = WS_H + 128u * 56 * 1024;

constexpr unsigned G1_XS = 16384, G1_WS = 32768, G1_XOFF = 0, G1_WOFF = 3 * G1_XS;
constexpr unsigned CV_HS = 4160, CV_H = 4 * CV_HS, CV_X = 5120, CV_CH = CV_H + CV_X, CV_BASE = G1_WOFF + 2 * G1_WS, CV_RED = 0;
constexpr unsigned CV_Z = CV_BASE + 2 * CV_CH;
constexpr unsigned ST_K = G1_WOFF + 2 * G1_WS, ST_V = ST_K + 9216, ST_SH = ST_V + 8192, ST_T = 2 * G1_XS;
constexpr unsigned HP_WD = G1_WOFF;
constexpr unsigned LDS_BYTES = CV_Z + 2 * 1088;
static_assert(HP_WD + 4 * 16384 <= ST_K && ST_SH + 1024 <= LDS_BYTES && LDS_BYTES <= 163840, "lds");

#define SB() __builtin_amdgcn_sched_barrier(0)
#define STAMP_DECL
#define STAMP(i)
#define DSTAMP(i)
#define STAMP_FLUSH(n)

DI unsigned rfl(unsigned x) { return __builtin_amdgcn_readfirstlane(x); }
DI rsrc_t mk_rsrc(const void* p, unsigned bytes) { return __builtin_amdgcn_make_buffer_rsrc((void*)p, 0, bytes, 0x00020000); }
DI unsigned cvtpk(float lo, float hi) { f32x2 v = {lo, hi}; bf16x2 b = __builtin_convertvector(v, bf16x2); return __builtin_bit_cast(unsigned, b); }
DI float bflo(unsigned x) { return __uint_as_float(x << 16); }
DI float bfhi(unsigned x) { return __uint_as_float(x & 0xffff0000u); }
DI void dma16(rsrc_t r, unsigned voff, unsigned soff, unsigned ldsaddr) {
  asm volatile("s_mov_b32 m0, %0\n\ts_nop 0\n\tbuffer_load_dwordx4 %1, %2, %3 offen lds" :: "s"(ldsaddr), "v"(voff), "s"(r), "s"(soff) : "m0", "memory");
}
DI void m0_set(unsigned v) { asm volatile("s_mov_b32 m0, %0" :: "s"(v) : "m0", "memory"); }
template <int OFF> DI void dma_go(rsrc_t r, unsigned voff, unsigned soff) {
  asm volatile("buffer_load_dwordx4 %0, %1, %2 offen offset:%3 lds" :: "v"(voff), "s"(r), "s"(soff), "i"(OFF) : "memory");
}
#define WAIT_LGKM0() __builtin_amdgcn_s_waitcnt(0xC07F)
#define WAIT_VM(n) asm volatile("s_waitcnt vmcnt(" #n ")" ::: "memory")
#define WAIT_VM_BAR(n) asm volatile("s_waitcnt vmcnt(" #n ") lgkmcnt(0)\n\ts_barrier" ::: "memory")
#define LGKM_BAR() asm volatile("s_waitcnt lgkmcnt(0)\n\ts_barrier" ::: "memory")
DI bf16x8 lds_b128(unsigned a) { return *(const LAS bf16x8*)a; }
DI u32x4 lds_u128(unsigned a) { return *(const LAS u32x4*)a; }
DI bf16x8 lds_tr2(unsigned a0, unsigned a1) {
  s16x4 lo = __builtin_amdgcn_ds_read_tr16_b64_v4i16((LAS s16x4*)a0);
  s16x4 hi = __builtin_amdgcn_ds_read_tr16_b64_v4i16((LAS s16x4*)a1);
  s16x8 v = __builtin_shufflevector(lo, hi, 0, 1, 2, 3, 4, 5, 6, 7);
  return __builtin_bit_cast(bf16x8, v);
}
DI void mfma_vvv(f32x4& c, const bf16x8& a, const bf16x8& b) { asm volatile("v_mfma_f32_16x16x32_bf16 %0, %1, %2, %0" : "+v"(c) : "v"(a), "v"(b)); }
DI void mfma_vvv0(f32x4& c, const bf16x8& a, const bf16x8& b) { asm volatile("v_mfma_f32_16x16x32_bf16 %0, %1, %2, 0" : "=&v"(c) : "v"(a), "v"(b)); }
DI void mfma32(f32x16& c, const bf16x8& a, const bf16x8& b) { asm volatile("v_mfma_f32_32x32x16_bf16 %0, %1, %2, %0" : "+v"(c) : "v"(a), "v"(b)); }
DI void mfma_vav(f32x4& c, const u32x4& a, const u32x4& b) { asm volatile("v_mfma_f32_16x16x32_bf16 %0, %1, %2, %0" : "+v"(c) : "a"(a), "v"(b)); }
DI void mfma_vav0(f32x4& c, const u32x4& a, const u32x4& b) { asm volatile("v_mfma_f32_16x16x32_bf16 %0, %1, %2, 0" : "=&v"(c) : "a"(a), "v"(b)); }
#define MFMA_DRAIN() do { SB(); asm volatile("s_nop 7\n\ts_nop 4" ::: "memory"); SB(); } while (0)
constexpr int SC1 = 16;
DI u32x4 gld128(rsrc_t r, unsigned voff, unsigned soff) { return __builtin_amdgcn_raw_buffer_load_b128(r, voff, soff, SC1); }
DI unsigned gld32(rsrc_t r, unsigned voff, unsigned soff) { return __builtin_amdgcn_raw_buffer_load_b32(r, voff, soff, SC1); }
DI void gst128(u32x4 v, rsrc_t r, unsigned voff, unsigned soff) { __builtin_amdgcn_raw_buffer_store_b128(v, r, voff, soff, SC1); asm volatile("s_nop 1" : "+v"(v)); }
DI void gst64(u32x2 v, rsrc_t r, unsigned voff, unsigned soff) { __builtin_amdgcn_raw_buffer_store_b64(v, r, voff, soff, SC1); }
DI void gst32(unsigned v, rsrc_t r, unsigned voff, unsigned soff) { __builtin_amdgcn_raw_buffer_store_b32(v, r, voff, soff, SC1); }
#define RLX_AGENT __ATOMIC_RELAXED, __HIP_MEMORY_SCOPE_AGENT
DI u64 flag_ld(const char* a) { return __hip_atomic_load((const GAS u64*)a, RLX_AGENT); }
DI void flag_st(char* a, u64 v) { __hip_atomic_store((GAS u64*)a, v, RLX_AGENT); }
template <int N, int STRIDE = 8, int SLEEP = 4> DI void wait_flags(const char* base, u64 epoch, char* tmo, int lane) {
  for (unsigned spins = 0;; ++spins) {
    bool ok = true;
#pragma unroll
    for (int i = 0; i < (N + 63) / 64; ++i) { const int idx = i * 64 + lane; const u64 v = flag_ld(base + (idx < N ? idx : 0) * STRIDE); ok &= (v == epoch); }
    if (__all(ok)) break;
    if (spins > 20000u) { if (lane == 0) flag_st(tmo, 1); break; }
    __builtin_amdgcn_s_sleep(SLEEP);
  }
  __builtin_amdgcn_fence(__ATOMIC_ACQUIRE, "wavefront");
}
DI f32x4 selv(unsigned m, f32x4 a, f32x4 b) {
  f32x4 r;
#pragma unroll
  for (int e = 0; e < 4; ++e) r[e] = __uint_as_float((__float_as_uint(a[e]) & m) | (__float_as_uint(b[e]) & ~m));
  return r;
}
DI unsigned gelu_mul2(f32x2 g, f32x2 u) {
  const f32x2 c1 = {-2.302208198f, -2.302208198f}, c2 = {-0.1029432397f, -0.1029432397f}, one = {1.f, 1.f};
  f32x2 a = g * __builtin_elementwise_fma(g * g, c2, c1);
  f32x2 e = {__builtin_amdgcn_exp2f(a[0]), __builtin_amdgcn_exp2f(a[1])};
  f32x2 d = e + one;
  f32x2 r = {__builtin_amdgcn_rcpf(d[0]), __builtin_amdgcn_rcpf(d[1])};
  f32x2 h = (g * u) * r;
  return cvtpk(h[0], h[1]);
}

DI float wave_sum(float a) {
  a += __builtin_bit_cast(float, __builtin_amdgcn_update_dpp(0, __builtin_bit_cast(int, a), 0xB1, 0xf, 0xf, true));
  a += __builtin_bit_cast(float, __builtin_amdgcn_update_dpp(0, __builtin_bit_cast(int, a), 0x4E, 0xf, 0xf, true));
  a += __builtin_bit_cast(float, __builtin_amdgcn_update_dpp(0, __builtin_bit_cast(int, a), 0x141, 0xf, 0xf, true));
  a += __builtin_bit_cast(float, __builtin_amdgcn_update_dpp(0, __builtin_bit_cast(int, a), 0x140, 0xf, 0xf, true));
  const int x = __builtin_bit_cast(int, a);
  return (__builtin_bit_cast(float, __builtin_amdgcn_readlane(x, 0)) + __builtin_bit_cast(float, __builtin_amdgcn_readlane(x, 16))) +
         (__builtin_bit_cast(float, __builtin_amdgcn_readlane(x, 32)) + __builtin_bit_cast(float, __builtin_amdgcn_readlane(x, 48)));
}
DI float dot2(unsigned a, unsigned b, float c) { return __builtin_amdgcn_fdot2_f32_bf16(__builtin_bit_cast(bf16x2, a), __builtin_bit_cast(bf16x2, b), c, false); }
struct Params { const u16 *q, *k, *v, *shk, *hk, *Dv, *wg, *wu, *wd, *gw; u16* out; char* ws; };

struct G1 {
  rsrc_t xs, wsr;
  unsigned xv[4], wv[8];
  unsigned sx, sw;
  unsigned sx0, sw0, ix, iw;
  unsigned mx, mw;
  unsigned xm0, wm0, slot;
  unsigned dx, dw;
  unsigned xa[4], xb[4], wt[4];
  f32x16 acc[4][2];
  bf16x8 XA[2], XB[2], W[2][4];
};
template <int N> DI void g1_go(G1& c) {
  if constexpr (N < 4) dma_go<N * 1024>(c.xs, c.xv[N], c.sx);
  else dma_go<(N < 8 ? N - 4 : N - 8) * 1024>(c.wsr, c.wv[N - 4], c.sw);
}
template <int N> DI void g1_dma(G1& c, unsigned st, unsigned slot) {
  const unsigned m = N < 4 ? c.xm0 + slot * G1_XS : c.wm0 + slot * G1_WS + (N >= 8 ? 4096 : 0), ks = (c.ix + st) & 7;
  if constexpr (N == 0 || N == 4 || N == 8) asm volatile("s_mov_b32 m0, %0\n\ts_nop 0" :: "s"(m) : "m0", "memory");
  if constexpr (N < 4) dma_go<N * 1024>(c.xs, c.xv[N], c.sx0 + ks * 128);
  else dma_go<(N < 8 ? N - 4 : N - 8) * 1024>(c.wsr, c.wv[N - 4], c.sw0 + ks * (64 * 3584));
}
template <int... Ns> DI void g1_dma_all(G1& c, unsigned st, unsigned slot, iseq<Ns...>) { (g1_dma<Ns>(c, st, slot), ...); }
template <int KK, int R> DI void g1_read(G1& c) {
  constexpr int set = KK & 1;
  if constexpr (R == 0) c.XA[set] = lds_b128(c.xa[KK]);
  else if constexpr (R == 2) c.XB[set] = lds_b128(c.xb[KK]);
  else { constexpr int p = R == 1 ? 0 : R - 2; c.W[set][p] = lds_tr2(c.wt[p] + KK * 4096, c.wt[p] + KK * 4096 + 1024); }
}
template <int KK, int M> DI void g1_mfma(G1& c) { mfma32(c.acc[M >> 1][M & 1], c.W[KK & 1][M >> 1], (M & 1) ? c.XB[KK & 1] : c.XA[KK & 1]); }
struct Tail {
  rsrc_t hs, wsr, xr; unsigned hvoff, hm0, zm0, wdso, xvoff; const char *fw, *fk; u64 epoch; char* tmo;
  u64 pw, pk;
};
DI void poll_issue(u64& v, const char* a) { asm volatile("global_load_dwordx2 %0, %1, off sc1" : "=v"(v) : "v"(a) : "memory"); }
template <int I> DI void tail_op(Tail& t, u32x4 (&Wd)[14][4], u32x4 (&xv)[3], int lane) {
  if constexpr (I < 4) dma_go<I * 1024>(t.hs, t.hvoff, 0);
  else if constexpr (I == 4) dma_go<0>(t.hs, lane * 16, 0);
  else if constexpr (I < 9) dma_go<(I - 5) * 1024>(t.hs, t.hvoff + 2, 0);
  else if constexpr (I < 12) { }
  else if constexpr (I < 15) xv[I - 12] = gld128(t.xr, t.xvoff + (I - 12) * 1024, 0);
  else { constexpr int n = I - 15; Wd[n >> 2][n & 3] = gld128(t.wsr, lane * 16 + (n & 3) * 1024, t.wdso + (n >> 2) * 4096); }
}
constexpr int TAIL_WD = 12;
constexpr int vm_slot(int g) { constexpr int s[12] = {27, 30, 1, 3, 6, 9, 11, 14, 17, 19, 22, 24}; for (int i = 0; i < 12; ++i) if (s[i] == g) return i; return -1; }
template <int MODE, int G> DI void g1_gap(G1& c, Tail& t, u32x4 (&Wd)[14][4], u32x4 (&xv)[3], int lane) {
  constexpr int kk = G / 8, m = G % 8, v = vm_slot(G);
  if constexpr (MODE == 5 && G == 0) { poll_issue(t.pw, t.fw + (lane & 31) * 8); poll_issue(t.pk, t.fk + (lane & 31) * 8); }
  g1_mfma<kk, m>(c);
  if constexpr (kk < 3) { if constexpr (m < 6) g1_read<kk + 1, m>(c); }
  else if constexpr (MODE != 7) {
    if constexpr (m == 1) {
      SB();
      if constexpr (MODE == 5) asm volatile("s_waitcnt vmcnt(14) lgkmcnt(0)\n\ts_barrier" ::: "memory");
      else if constexpr (MODE == 6) asm volatile("s_waitcnt vmcnt(9) lgkmcnt(0)\n\ts_barrier" : "+v"(t.pw), "+v"(t.pk) :: "memory");
      else asm volatile("s_waitcnt vmcnt(12) lgkmcnt(0)\n\ts_barrier" ::: "memory");
      if constexpr (MODE == 6) {
        if (!__all(t.pw == t.epoch)) wait_flags<32>(t.fw, t.epoch, t.tmo, lane);
        if (!__all(t.pk == t.epoch)) wait_flags<32>(t.fk, t.epoch, t.tmo, lane);
        __builtin_amdgcn_fence(__ATOMIC_ACQUIRE, "wavefront");
      }
    }
    if constexpr (m == 2) { g1_read<0, 0>(c); g1_read<0, 1>(c); }
    if constexpr (m == 3) { g1_read<0, 2>(c); g1_read<0, 3>(c); }
    if constexpr (m == 4) g1_read<0, 4>(c);
    if constexpr (m == 5) g1_read<0, 5>(c);
  }
  if constexpr (v >= 0) {
    if constexpr (MODE == 0) g1_go<v>(c);
    if constexpr (MODE == 5) { if constexpr (v >= 2) g1_go<v>(c); else tail_op<v>(t, Wd, xv, lane); }
    if constexpr (MODE == 6) { if constexpr (v >= 2) tail_op<v>(t, Wd, xv, lane); else tail_op<12 + v>(t, Wd, xv, lane); }
    if constexpr (MODE == 7) { if constexpr (v >= 2) tail_op<12 + v>(t, Wd, xv, lane); else tail_op<24 + v>(t, Wd, xv, lane); }
  }
  if constexpr (MODE == 7 && G == 26) tail_op<26>(t, Wd, xv, lane);
  if constexpr (MODE == 0 || MODE == 5) {
    if constexpr (G == 4) { c.ix = (c.ix + 1) & 7; c.sx = c.sx0 + c.ix * 128; asm volatile("" : "+s"(c.ix), "+s"(c.sx)); }
    if constexpr (G == 5) m0_set(c.mw);
    if constexpr (G == 16) m0_set(c.mw + 4096);
    if constexpr (G == 25) { c.iw = (c.iw + 1) & 7; c.sw = c.sw0 + c.iw * (64 * 3584); asm volatile("" : "+s"(c.iw), "+s"(c.sw)); }
  }
  if constexpr (MODE == 0 && G == 26) m0_set(c.mx);
  if constexpr (MODE == 5 && G == 26) m0_set(t.hm0);
  if constexpr (MODE == 6 && G == 5) m0_set(t.zm0);
  if constexpr (MODE == 6 && G == 8) m0_set(t.hm0 + CV_HS);
  if constexpr (MODE != 7) {
    if constexpr (G == 7) { c.xa[0] += c.dx; c.xb[0] += c.dx; }
    if constexpr (G == 8) { c.xa[1] += c.dx; c.xb[1] += c.dx; }
    if constexpr (G == 15) { c.xa[2] += c.dx; c.xb[2] += c.dx; }
    if constexpr (G == 23) { c.xa[3] += c.dx; c.xb[3] += c.dx; }
    if constexpr (G == 24) { c.wt[0] += c.dw; c.wt[1] += c.dw; }
    if constexpr (G == 25) { c.wt[2] += c.dw; c.wt[3] += c.dw; }
  }
  if constexpr (MODE == 0) {
    if constexpr (G == 28) { c.mw = c.wm0 + c.slot * G1_WS; c.slot = c.slot == 2 ? 0 : c.slot + 1; asm volatile("" : "+s"(c.mw), "+s"(c.slot)); }
    if constexpr (G == 31) {
      c.dx = c.slot == 2 ? 0u - 2 * G1_XS : G1_XS; c.dw = c.slot == 2 ? 0u - 2 * G1_WS : G1_WS; c.mx = c.xm0 + c.slot * G1_XS;
      asm volatile("" : "+s"(c.dx), "+s"(c.dw), "+s"(c.mx));
    }
  }
  SB();
}
template <int MODE, int... Gs> DI void g1_stage(G1& c, Tail& t, u32x4 (&Wd)[14][4], u32x4 (&xv)[3], int lane, iseq<Gs...>) { (g1_gap<MODE, Gs>(c, t, Wd, xv, lane), ...); }
template <int... Rs> DI void g1_read0(G1& c, iseq<Rs...>) { (g1_read<0, Rs>(c), ...); }

struct ConvCtx { unsigned abase, bbase, hm; u32x4 a0; const char* hfl; unsigned wdso; int lane; rsrc_t wsr; };
constexpr int cv_al(int m) { int al = 0, n = 8; while (m >= n) { m -= n; --n; ++al; } return al; }
constexpr int cv_be(int m) { int n = 8; while (m >= n) { m -= n; --n; } return m; }
template <int RR, int M> DI void conv_gap(const ConvCtx& cx, f32x4 (&acc)[8], u32x2 (&alo)[2][8], u32x2 (&ahi)[2][8], bf16x8 (&B)[2][8], u32x4 (&Wd)[14][4], u64& hflag) {
  constexpr int set = RR & 1, al = cv_al(M), be = cv_be(M);
  if constexpr (M == 0) WAIT_LGKM0();
  u32x4 av = {alo[set][al][0], alo[set][al][1], ahi[set][al][0], ahi[set][al][1]};
  if constexpr (RR == 0 && al == 0) {
#pragma unroll
    for (int e = 0; e < 4; ++e) av[e] = (av[e] & cx.hm) | (cx.a0[e] & ~cx.hm);
  }
  bf16x8 A = __builtin_bit_cast(bf16x8, av);
  if constexpr (RR == 0 && al == 0) { asm volatile("s_nop 1" : "+v"(A)); mfma_vvv0(acc[be], A, B[set][be]); }
  else mfma_vvv(acc[al + be], A, B[set][be]);
  if constexpr (RR == 3 && M == 1) hflag = flag_ld(cx.hfl);
  if constexpr (M % 5 == 3) {
    constexpr int n = 28 + RR * 7 + M / 5;
    if constexpr (n < 56) Wd[n >> 2][n & 3] = gld128(cx.wsr, cx.lane * 16 + (n & 3) * 1024, cx.wdso + (n >> 2) * 4096);

  } else if constexpr (RR < 3) {
    constexpr int rd = M - (M + 1) / 5;
    if constexpr (rd < 24) {
      constexpr int i = rd / 3, k = rd % 3;
      if constexpr (k == 0) alo[set ^ 1][i] = *(const volatile LAS u32x2*)(cx.abase + (RR + 1) * 64 + i * 512);
      else if constexpr (k == 1) ahi[set ^ 1][i] = *(const volatile LAS u32x2*)(cx.abase + (RR + 1) * 64 + i * 512 + 8);
      else B[set ^ 1][i] = lds_b128(cx.bbase + 64 * (56 - 8 * i) + 64 * (RR + 1));
    }
  }
  SB();
}
template <int RR, int... Ms> DI void conv_rho(const ConvCtx& cx, f32x4 (&acc)[8], u32x2 (&alo)[2][8], u32x2 (&ahi)[2][8], bf16x8 (&B)[2][8], u32x4 (&Wd)[14][4], u64& hflag, iseq<Ms...>) {
  (conv_gap<RR, Ms>(cx, acc, alo, ahi, B, Wd, hflag), ...);
}

struct G2Ctx {
  rsrc_t wsr; const char *yfl, *gfl; u64 epoch; char* tmo; int lane;
  unsigned yvo, yso, gvo, gso; const u16* qp; unsigned hso, rbase; int gq, li; char* myy;
  struct { char* ws; } p;
};
constexpr int G2R = 16, G2PD = 12;
template <int J> DI void g2_ldh(const G2Ctx& g, u32x4 (&H)[G2R]) { H[J % G2R] = gld128(g.wsr, g.lane * 16 + (J % 14 & 3) * 1024, g.hso + (J / 14) * 57344 + (J % 14 & ~3) * 1024); }
template <int P, int DT> DI void g2_put(const G2Ctx& g, f32x4 (&acc)[4][4]) {
  *(LAS f32x4*)(g.rbase + P * 4096 + (((4 * DT + g.gq) ^ g.li) << 4)) = acc[P][DT];
}
template <int J> DI void g2_step(const G2Ctx& g, f32x4 (&acc)[4][4], u32x4 (&Wd)[14][4], u32x4 (&H)[G2R], u64& yflag, unsigned (&yv)[8], u32x2& gt, u32x4 (&qv)[2]) {
  constexpr int P = J / 14, I = J % 14;
  if constexpr (I == 0) { auto& p = g.p; (void)p; DSTAMP(16 + 3 * P); }
  if constexpr (J == 44) {
    if (!__all(yflag == g.epoch)) { wait_flags<32>(g.yfl, g.epoch, g.tmo, g.lane); wait_flags<1>(g.gfl, g.epoch, g.tmo, g.lane); }
    __builtin_amdgcn_fence(__ATOMIC_ACQUIRE, "wavefront");
    SB();
  }
  if constexpr (I == 0) mfma_vav0(acc[P][0], Wd[I][0], H[J % G2R]); else mfma_vav(acc[P][0], Wd[I][0], H[J % G2R]);
  if constexpr (J + G2PD < 56) g2_ldh<J + G2PD>(g, H);
  if constexpr (J == 36) yflag = flag_ld(g.lane < 32 ? g.yfl + g.lane * 8 : g.gfl);
  if constexpr (J >= 44 && J < 52) yv[J - 44] = gld32(g.wsr, g.yvo + (J - 44) * 4096, g.yso);
  if constexpr (J == 52) gt = __builtin_amdgcn_raw_buffer_load_b64(g.wsr, g.gvo, g.gso, SC1);
  if constexpr (J == 53) qv[0] = *(const u32x4*)g.qp;
  if constexpr (J == 54) qv[1] = *(const u32x4*)(g.qp + D_);
  SB();
  if constexpr (I == 0) mfma_vav0(acc[P][1], Wd[I][1], H[J % G2R]); else mfma_vav(acc[P][1], Wd[I][1], H[J % G2R]);
  if constexpr (I == 0) mfma_vav0(acc[P][2], Wd[I][2], H[J % G2R]); else mfma_vav(acc[P][2], Wd[I][2], H[J % G2R]);
  if constexpr (P > 0 && I >= 2 && I < 6) g2_put<(P > 0 ? P - 1 : 0), (I >= 2 && I < 6 ? I - 2 : 0)>(g, acc);
  SB();
  if constexpr (I == 0) mfma_vav0(acc[P][3], Wd[I][3], H[J % G2R]); else mfma_vav(acc[P][3], Wd[I][3], H[J % G2R]);
  SB();
  if constexpr (J == 8) {
    WAIT_VM(12); asm volatile("s_barrier" ::: "memory");
    if (threadIdx.x == 0) flag_st(g.myy, g.epoch);
    SB();
  }
}
template <int... Js> DI void g2_loop(const G2Ctx& g, f32x4 (&acc)[4][4], u32x4 (&Wd)[14][4], u32x4 (&H)[G2R], u64& yflag, unsigned (&yv)[8], u32x2& gt, u32x4 (&qv)[2], iseq<Js...>) {
  (g2_step<Js>(g, acc, Wd, H, yflag, yv, gt, qv), ...);
}
template <int... Js> DI void g2_ldh_range(const G2Ctx& g, u32x4 (&H)[G2R], iseq<Js...>) { (g2_ldh<Js>(g, H), ...); }
template <int B, int... Js> DI void g2_ldh_from(const G2Ctx& g, u32x4 (&H)[G2R], iseq<Js...>) { (g2_ldh<B + Js>(g, H), ...); }

__global__ __launch_bounds__(256, 1) void k_fused(Params p) {
  __shared__ __attribute__((aligned(16))) char smem[LDS_BYTES];
  const unsigned lds0 = (unsigned)(size_t)(LAS char*)smem;
  const int lane = threadIdx.x & 63;
  const unsigned w = rfl(threadIdx.x >> 6);
  const unsigned b = blockIdx.x, xcd = b & 7, jb = b >> 3;
  char* const ws = p.ws;
  STAMP_DECL; STAMP(0);
  asm volatile("" :: "s"(p.q), "s"(p.k), "s"(p.v), "s"(p.shk), "s"(p.hk), "s"(p.Dv), "s"(p.wg), "s"(p.wu), "s"(p.wd), "s"(p.gw), "s"(p.out), "s"(p.ws));
  const rsrc_t wsr = mk_rsrc(ws, WS_END);
  u64 eword;
  asm volatile("global_load_dwordx2 %0, %1, off" : "=v"(eword) : "v"(ws + WS_E) : "memory");

  const unsigned cch = w >> 1, hf = w & 1, dch = b * 2 + cch, cvb = lds0 + CV_BASE + cch * CV_CH, cvz = lds0 + CV_Z + cch * 1088;
  const unsigned rb2 = 4 * xcd + (jb & 3), cb = jb >> 2;
  u32x4 Wd[14][4], xv[3];
  Tail t;
  t.hs = mk_rsrc(p.hk + (size_t)dch * S_, (D_ - dch) * S_ * 2);
  t.hvoff = lane * 16 + (2 * hf + 17) * 2; t.hm0 = cvb + 2 * hf * CV_HS; t.zm0 = cvz + 32;
  t.wsr = wsr; t.wdso = WS_WDT + (cb * 56 + 14 * w) * 4096;
  t.fw = ws + WS_FW; t.fk = ws + WS_FK + (b >> 5) * 256; t.tmo = ws + WS_TMO;
  const rsrc_t xr = mk_rsrc(ws + WS_KVT + dch * 4096, 4096);
  auto ldWd = [&](int n) { Wd[n >> 2][n & 3] = gld128(wsr, lane * 16 + (n & 3) * 1024, t.wdso + (n >> 2) * 4096); };
  auto ldX = [&]() {
#pragma unroll
    for (int i = 0; i < 3; ++i) xv[i] = gld128(xr, (hf * 128 + lane) * 16 + i * 1024, 0);
  };
  t.xr = xr; t.xvoff = (hf * 128 + lane) * 16;
  const unsigned cg = xcd & 3;
  const bool g1wg = !(cg & 1) || jb < 24;
  const unsigned rb = 8 * (xcd >> 2) + (jb & 7), fc = (cg >> 1) * 7 + (cg & 1) * 4 + (jb >> 3);
  const unsigned hj = (xcd >> 1) * 8 + (jb - 24);
  const unsigned mi = w >> 1, ni = w & 1;

  const unsigned sb = b >> 3, db = b & 7, s0p = sb * 64, d0p = db * 64;
  {
    const unsigned r8 = lane >> 3, c8 = lane & 7;
    const rsrc_t rk = mk_rsrc(p.k, S_ * D_ * 2), rv = mk_rsrc(p.v, S_ * D_ * 2);
    auto dk = [&](int i) { int row = (int)s0p - 3 + 8 * i + (int)r8; dma16(rk, row >= 0 ? (unsigned)row * 1024 + d0p * 2 + c8 * 16 : 0x80000000u, 0, lds0 + ST_K + i * 1024); };
    auto dv = [&](int i) { dma16(rv, r8 * 1024 + c8 * 16, (s0p + 8 * i) * 1024 + d0p * 2, lds0 + ST_V + i * 1024); };
    if (w == 0) { dk(0); dk(1); dk(2); dk(3); dk(4); }
    else if (w == 1) { dk(5); dk(6); dk(7); dk(8); }
    else if (w == 2) { dv(0); dv(1); dv(2); dv(3); dv(4); }
    else { dv(5); dv(6); dv(7); dma16(mk_rsrc(p.shk, D_ * 8), lane * 16, d0p * 8, lds0 + ST_SH); }
  }
  SB();
  G1 c;
  const unsigned hb = lds0 + HP_WD + w * 16384;
  c.xs = mk_rsrc(p.q, S_ * D_ * 2);
  c.wsr = mk_rsrc((w >> 1) ? p.wu : p.wg, D_ * F_ * 2);
#pragma unroll
  for (int n = 0; n < 4; ++n) c.xv[n] = (lane >> 3) * 1024 + (((lane & 7) ^ ((4 * (n & 1) + (lane >> 4)) & 7)) << 4) + n * (8192 - 1024);
  {
    const unsigned kr = lane >> 4, c16 = lane & 15, tl = (c16 >> 1) ^ (2 * kr + (w >> 1));
#pragma unroll
    for (int jj = 0; jj < 8; ++jj) c.wv[jj] = (32 * (w & 1) + 4 * jj + kr) * 3584 + (tl * 2 + (c16 & 1)) * 16 - (jj & 3) * 1024;
  }
  c.sx0 = (rb * 128 + 32 * w) * 1024;
  c.sw0 = fc * 256;
  c.ix = 0;
  c.xm0 = lds0 + G1_XOFF + w * 4096;
  c.wm0 = lds0 + G1_WOFF + (w >> 1) * 16384 + (w & 1) * 8192;
  if (g1wg) {
    g1_dma_all(c, 0, 0, make_iseq<12>{});
  } else {
    const rsrc_t rw = mk_rsrc(p.wd, F_ * D_ * 2);
    const unsigned rl = lane >> 4, c16 = lane & 15;
#pragma unroll
    for (int n = 0; n < 16; ++n) {
      const unsigned jn = n >> 3, i = n & 7, ji = w + 4 * jn, job = 7 * hj + ji, row = 4 * i + rl, tl = (c16 >> 1) ^ ((row & 3) | ((row >> 1) & 4));
      dma16(rw, ji < 7 ? rl * 1024 + (tl * 2 + (c16 & 1)) * 16 : 0x80000000u, ((job >> 2) * 32 + 4 * i) * 1024 + (job & 3) * 256, hb + n * 1024);
    }
  }
  {
    const int r = lane & 31, hh = lane >> 5;
    const unsigned base = r * 128 + ((hh ^ ((r >> 1) & 7)) << 4);
#pragma unroll
    for (int kk = 0; kk < 4; ++kk) {
      c.xa[kk] = lds0 + G1_XOFF + (2 * mi) * 4096 + (base ^ (kk << 5));
      c.xb[kk] = lds0 + G1_XOFF + (2 * mi + 1) * 4096 + (base ^ (kk << 5));
    }
    const unsigned gu = (lane >> 4) & 1, q = (lane & 15) >> 2;
#pragma unroll
    for (int pp = 0; pp < 4; ++pp) c.wt[pp] = lds0 + G1_WOFF + gu * 16384 + (8 * hh + q) * 256 + (((4 * ni + pp) ^ (2 * q + gu)) << 5) + (lane & 3) * 8;
#pragma unroll
    for (int i = 0; i < 4; ++i) { c.acc[i][0] = f32x16{}; c.acc[i][1] = f32x16{}; }
    asm volatile("" : "+v"(c.acc[0][0]), "+v"(c.acc[0][1]), "+v"(c.acc[1][0]), "+v"(c.acc[1][1]), "+v"(c.acc[2][0]), "+v"(c.acc[2][1]), "+v"(c.acc[3][0]), "+v"(c.acc[3][1]));
  }
  SB();
  STAMP(1);
  asm volatile("s_waitcnt vmcnt(12)\n\ts_barrier" : "+v"(eword) :: "memory");
  const u64 epoch = ((u64)rfl((unsigned)(eword >> 32)) << 32 | rfl((unsigned)eword)) * 6364136223846793005ull + 1442695040888963407ull;
  t.epoch = epoch;
  STAMP(2);
  u32x4 kvo[2];
  {
    const int th = threadIdx.x, c4 = th & 15, rg = th >> 4;
    u32x2 kr[7], vr[4];
#pragma unroll
    for (int i = 0; i < 7; ++i) kr[i] = *(const LAS u32x2*)(lds0 + ST_K + (4 * rg + i) * 128 + c4 * 8);
#pragma unroll
    for (int i = 0; i < 4; ++i) vr[i] = *(const LAS u32x2*)(lds0 + ST_V + (4 * rg + i) * 128 + c4 * 8);
    const u32x4 w4[2] = {lds_u128(lds0 + ST_SH + c4 * 32), lds_u128(lds0 + ST_SH + c4 * 32 + 16)};
    if (threadIdx.x == 0) flag_st(ws + WS_FA + b * 8, epoch);
    if (g1wg) { g1_dma<0>(c, 1, 1); g1_dma<1>(c, 1, 1); g1_dma<2>(c, 1, 1); g1_dma<3>(c, 1, 1); }
    SB();
    const int wd = 30 - 2 * rg;
    const unsigned wr = lds0 + ST_T + 4 * c4 * 128 + ((((wd >> 2) ^ c4) & 7) << 4) + (wd & 3) * 4;
#pragma unroll
    for (int pp = 0; pp < 2; ++pp) {
      const f32x2 w0 = {bflo(w4[pp][0]), bflo(w4[pp][2])}, w1 = {bfhi(w4[pp][0]), bfhi(w4[pp][2])}, w2 = {bflo(w4[pp][1]), bflo(w4[pp][3])}, w3 = {bfhi(w4[pp][1]), bfhi(w4[pp][3])};
      f32x2 kk[7], o[4];
#pragma unroll
      for (int i = 0; i < 7; ++i) kk[i] = f32x2{bflo(kr[i][pp]), bfhi(kr[i][pp])};
#pragma unroll
      for (int i = 0; i < 4; ++i) o[i] = (kk[i] * w3 + kk[i + 1] * w2 + kk[i + 2] * w1 + kk[i + 3] * w0) * f32x2{bflo(vr[i][pp]), bfhi(vr[i][pp])};
#pragma unroll
      for (int e = 0; e < 2; ++e) *(LAS u32x2*)(wr + (2 * pp + e) * 128) = u32x2{cvtpk(o[3][e], o[2][e]), cvtpk(o[1][e], o[0][e])};
      if (g1wg) { if (pp == 0) { g1_dma<4>(c, 1, 1); g1_dma<5>(c, 1, 1); g1_dma<6>(c, 1, 1); g1_dma<7>(c, 1, 1); } else { g1_dma<8>(c, 1, 1); g1_dma<9>(c, 1, 1); g1_dma<10>(c, 1, 1); g1_dma<11>(c, 1, 1); } }
      SB();
    }
    LGKM_BAR();
#pragma unroll
    for (int i = 0; i < 2; ++i) { const int dl = (w * 2 + i) * 8 + (lane >> 3), ch = lane & 7; kvo[i] = lds_u128(lds0 + ST_T + dl * 128 + ((ch ^ (dl >> 2)) & 7) * 16); }
    SB();
  }
  auto kv_store = [&]() {
#pragma unroll
    for (int i = 0; i < 2; ++i) gst128(kvo[i], wsr, (d0p + (w * 2 + i) * 8 + (lane >> 3)) * 4096 + (lane & 7) * 16, WS_KVT + (1984 - s0p) * 2);
  };
  char* const fk_mine = ws + WS_FK + db * 256 + sb * 8;
  STAMP(3);

  if (g1wg) {
    asm volatile("s_waitcnt vmcnt(12) lgkmcnt(0)\n\ts_barrier" ::: "memory");
    g1_read0(c, make_iseq<6>{});
    g1_dma<0>(c, 2, 2); g1_dma<1>(c, 2, 2);
    kv_store();
    c.ix = (c.ix + 2) & 7; c.iw = c.ix; c.sx = c.sx0 + c.ix * 128; c.sw = c.sw0 + c.iw * (64 * 3584);
    c.slot = 0; c.mx = c.xm0; c.mw = c.wm0 + 2 * G1_WS; c.dx = G1_XS; c.dw = G1_WS;
    SB();
    STAMP(4);
#pragma unroll 1
    for (unsigned s = 0; s < 2; ++s) { DSTAMP(s); g1_stage<0>(c, t, Wd, xv, lane, make_iseq<32>{}); }
    if (threadIdx.x == 0) flag_st(fk_mine, epoch);
#pragma unroll 1
    for (unsigned s = 2; s < 5; ++s) { DSTAMP(s); g1_stage<0>(c, t, Wd, xv, lane, make_iseq<32>{}); }
    DSTAMP(5);
    g1_stage<5>(c, t, Wd, xv, lane, make_iseq<32>{});
    if (lane < 8) *(LAS unsigned*)(cvz + lane * 4) = 0;
    c.dx = G1_XS; c.dw = G1_WS;
    DSTAMP(6);
    g1_stage<6>(c, t, Wd, xv, lane, make_iseq<32>{});
    DSTAMP(7);
    g1_stage<7>(c, t, Wd, xv, lane, make_iseq<32>{});
    DSTAMP(8);
    MFMA_DRAIN();
    STAMP(5);
    {
      const int s32 = lane & 31, hh = lane >> 5;
      const unsigned lo = (s32 >> 4) * 57344 + (s32 & 15) * 16 + hh * 256;
      const unsigned sA = WS_H + (rb * 8 + 4 * mi) * 57344 + (fc * 8 + 4 * ni) * 512, sB = sA + 2 * 57344;
      auto put = [&](int n0, const f32x16& a, unsigned so, int imm) {
        ldWd(n0); ldWd(n0 + 1);
        SB();
        unsigned x0 = gelu_mul2(f32x2{a[0], a[1]}, f32x2{a[8], a[9]}), y0 = gelu_mul2(f32x2{a[2], a[3]}, f32x2{a[10], a[11]});
        unsigned x1 = gelu_mul2(f32x2{a[4], a[5]}, f32x2{a[12], a[13]}), y1 = gelu_mul2(f32x2{a[6], a[7]}, f32x2{a[14], a[15]});
        const auto sx = __builtin_amdgcn_permlane32_swap(x0, x1, false, false), sy = __builtin_amdgcn_permlane32_swap(y0, y1, false, false);
        gst128(u32x4{sx[0], sy[0], sx[1], sy[1]}, wsr, lo + imm, so);
        SB();
      };
      put(12, c.acc[0][0], sA, 0); put(14, c.acc[0][1], sB, 0); put(16, c.acc[1][0], sA, 512); put(18, c.acc[1][1], sB, 512);
      put(20, c.acc[2][0], sA, 1024); put(22, c.acc[2][1], sB, 1024); put(24, c.acc[3][0], sA, 1536); put(26, c.acc[3][1], sB, 1536);
    }
    SB();
  } else {
    const int li = lane & 15, gq = lane >> 4;
    kv_store();
    asm volatile("s_waitcnt vmcnt(2)" ::: "memory");
    {
      const unsigned g = (li >> 2) | ((gq & 1) << 2);
#pragma unroll
      for (int jn = 0; jn < 2; ++jn) {
        const unsigned ji = w + 4 * jn, job = 7 * hj + ji, kg = job >> 2, cq = job & 3;
        bf16x8 f[8];
#pragma unroll
        for (int i = 0; i < 8; ++i) { const unsigned a = hb + jn * 8192 + (8 * gq + (li >> 2)) * 256 + ((i ^ g) << 5) + (lane & 3) * 8; f[i] = lds_tr2(a, a + 4 * 256); }
#pragma unroll
        for (int i = 0; i < 8; ++i) {
          const unsigned D16 = 8 * cq + i;
          gst128(__builtin_bit_cast(u32x4, f[i]), wsr, ji < 7 ? lane * 16 : 0x80000000u, WS_WDT + (((D16 >> 2) * 56 + kg) * 4 + (D16 & 3)) * 1024);
        }
      }
    }
    asm volatile("s_waitcnt vmcnt(0) lgkmcnt(0)\n\ts_barrier" ::: "memory");
    if (threadIdx.x == 0) { flag_st(ws + WS_FW + hj * 8, epoch); flag_st(fk_mine, epoch); }
    STAMP(4);
    {
      const unsigned grow = 64 * hj + 16 * w;
      const u32x4 gw4 = *(const u32x4*)(p.gw + lane * 8);
      float gv = 0.f;
#pragma unroll
      for (int r0 = 0; r0 < 16; r0 += 4) {
        u32x4 gx[4];
#pragma unroll
        for (int r = 0; r < 4; ++r) gx[r] = *(const u32x4*)(p.q + (size_t)(grow + r0 + r) * D_ + lane * 8);
#pragma unroll
        for (int r = 0; r < 4; ++r) {
          float a = 0.f;
#pragma unroll
          for (int j = 0; j < 4; ++j) a = dot2(gx[r][j], gw4[j], a);
          const float sg = __builtin_amdgcn_rcpf(1.f + __builtin_amdgcn_exp2f(-1.4426950409f * wave_sum(a)));
          gv = lane == r0 + r ? sg : gv;
        }
      }
      gst32(__float_as_uint(gv), wsr, lane < 16 ? WS_G + (grow + lane) * 4 : 0x80000000u, 0);
      asm volatile("s_waitcnt vmcnt(0)\n\ts_barrier" ::: "memory");
      if (threadIdx.x == 0) flag_st(ws + WS_FG + hj * 8, epoch);
    }
    if (hj == 0 && w == 0) {
      wait_flags<256>(ws + WS_FA, epoch, t.tmo, lane);
      if (lane == 0) flag_st(ws + WS_E, epoch);
    }
#pragma unroll
    for (int i = 0; i < 4; ++i) { dma16(t.hs, t.hvoff, i * 1024, t.hm0 + i * 1024); dma16(t.hs, t.hvoff + 2, i * 1024, t.hm0 + CV_HS + i * 1024); }
    dma16(t.hs, lane * 16, 0, t.zm0);
    if (lane < 8) *(LAS unsigned*)(cvz + lane * 4) = 0;
    if (w == 0) wait_flags<32, 8, 16>(t.fw, epoch, t.tmo, lane);
    asm volatile("s_barrier" ::: "memory");
#pragma unroll
    for (int n = 0; n < 28; ++n) ldWd(n);
    if (w == 0) wait_flags<32, 8, 32>(t.fk, epoch, t.tmo, lane);
    asm volatile("s_barrier" ::: "memory");
    ldX();
    STAMP(5);
  }
#pragma unroll
  for (int i = 0; i < 3; ++i) *(LAS u32x4*)(cvb + CV_H + (hf * 128 + lane) * 16 + i * 1024) = xv[i];
  LGKM_BAR();
  STAMP(6);

  const int tt = threadIdx.x, cc = tt & 7, sp = tt >> 3;
  const unsigned s0 = rb2 * 64, d0 = cb * 64;
  u32x4 H[G2R];
  const G2Ctx g2{wsr, ws + WS_FY + cb * 256, ws + WS_FG + rb2 * 8, epoch, ws + WS_TMO, lane, (unsigned)(8 * cc * 4096 + sp * 4), WS_Y2T + d0 * 4096 + s0 * 2, (unsigned)(sp * 8), WS_G + s0 * 4,
                 p.q + (size_t)(s0 + 2 * sp) * D_ + d0 + cc * 8, WS_H + ((4 * rb2) * 56 + 14 * w) * 1024, lds0 + w * 16384 + (lane & 15) * 256, lane >> 4, lane & 15,
                 ws + WS_FY + (b >> 5) * 256 + (b & 31) * 8, {ws}};
  {
    const int li = lane & 15, gq = lane >> 4;
    u32x4 a0;
#pragma unroll
    for (int e = 0; e < 4; ++e) {
      const unsigned o = cvz + (li + 8 * gq + 2 * e + 1) * 2;
      a0[e] = (unsigned)*(const LAS u16*)o | ((unsigned)*(const LAS u16*)(o + 2) << 16);
    }
    const float Dd = __uint_as_float((unsigned)p.Dv[dch] << 16);
    const unsigned abase = cvb + (li & 3) * CV_HS + (li >> 2) * 8 + gq * 16 + hf * 4 * 64 - 64;
    const unsigned bbase = cvb + CV_H + (254 - 2 * li + gq) * 16 - 64 * 56 + hf * 4 * 64;
    f32x4 acc[8];
    u32x2 alo[2][8], ahi[2][8];
    bf16x8 B[2][8];
#pragma unroll
    for (int i = 0; i < 8; ++i) {
      alo[0][i] = *(const volatile LAS u32x2*)(abase + i * 512);
      ahi[0][i] = *(const volatile LAS u32x2*)(abase + i * 512 + 8);
      B[0][i] = lds_b128(bbase + 64 * (56 - 8 * i));
    }
    SB();
    STAMP(7);
    u64 hflag = 0;
    ConvCtx cx{abase, bbase, 0u - hf, a0, ws + WS_FH + (rb2 >> 1) * 128 + (li < 14 ? li : 0) * 8, t.wdso, lane, wsr};
    DSTAMP(9);
    conv_rho<0>(cx, acc, alo, ahi, B, Wd, hflag, make_iseq<36>{});
    DSTAMP(10);
    WAIT_VM(7); asm volatile("s_barrier" ::: "memory");
    if (g1wg && threadIdx.x == 0) flag_st(ws + WS_FH + rb * 128 + fc * 8, epoch);
    SB();
    DSTAMP(11);
    conv_rho<1>(cx, acc, alo, ahi, B, Wd, hflag, make_iseq<36>{});
    conv_rho<2>(cx, acc, alo, ahi, B, Wd, hflag, make_iseq<36>{});
    conv_rho<3>(cx, acc, alo, ahi, B, Wd, hflag, make_iseq<36>{});
    DSTAMP(12);
    STAMP(8);
    if (!__all(hflag == epoch)) wait_flags<14>(ws + WS_FH + (rb2 >> 1) * 128, epoch, ws + WS_TMO, lane);
    __builtin_amdgcn_fence(__ATOMIC_ACQUIRE, "wavefront");
    DSTAMP(13);
    g2_ldh_range(g2, H, make_iseq<4>{});
    MFMA_DRAIN();
    const unsigned red = lds0 + CV_RED, hm = 0u - hf;
#pragma unroll
    for (int g = 0; g < 4; ++g) *(LAS f32x4*)(red + w * 4096 + g * 1024 + lane * 16) = selv(hm, acc[g], acc[4 + g]);
    SB();
    g2_ldh_from<4>(g2, H, make_iseq<4>{});
    SB();
    DSTAMP(14);
    LGKM_BAR();
    DSTAMP(15);
    f32x4 oth[4]; u32x2 kvr[4];
#pragma unroll
    for (int g = 0; g < 4; ++g) {
      oth[g] = *(const LAS f32x4*)(red + (w ^ 1) * 4096 + g * 1024 + lane * 16);
      kvr[g] = *(const LAS u32x2*)(cvb + CV_H + (2044 - (256 * (g + 4 * hf) + 16 * li + 4 * gq)) * 2);
    }
#pragma unroll
    for (int g = 0; g < 4; ++g) {
      f32x4 y = selv(hm, acc[4 + g], acc[g]) + oth[g];
      float r0 = y[0] + bfhi(kvr[g][1]) * Dd, r1 = y[1] + bflo(kvr[g][1]) * Dd, r2 = y[2] + bfhi(kvr[g][0]) * Dd, r3 = y[3] + bflo(kvr[g][0]) * Dd;
      gst64(u32x2{cvtpk(r0, r1), cvtpk(r2, r3)}, wsr, (16 * li + 4 * gq) * 2 + g * 512, WS_Y2T + dch * 4096 + hf * 2048);
    }
    g2_ldh_from<8>(g2, H, make_iseq<4>{});
    SB();
  }
  STAMP(9);

  unsigned yv[8]; u32x2 gt; u32x4 qv[2];
  {
    f32x4 acc[4][4];
    u64 yflag = 0;
    g2_loop(g2, acc, Wd, H, yflag, yv, gt, qv, make_iseq<56>{});
    DSTAMP(28);
    MFMA_DRAIN();
    g2_put<3, 0>(g2, acc); g2_put<3, 1>(g2, acc); g2_put<3, 2>(g2, acc); g2_put<3, 3>(g2, acc);
    DSTAMP(29);
    STAMP(10);
  }
  float at[2][8], omg[2];
#pragma unroll
  for (int r = 0; r < 2; ++r) {
    const float g = __uint_as_float(gt[r]);
    omg[r] = 1.f - g;
#pragma unroll
    for (int e = 0; e < 4; ++e) {
      const float yl = r ? bfhi(yv[2 * e]) : bflo(yv[2 * e]), yh = r ? bfhi(yv[2 * e + 1]) : bflo(yv[2 * e + 1]);
      at[r][2 * e] = g * (bflo(qv[r][e]) * yl); at[r][2 * e + 1] = g * (bfhi(qv[r][e]) * yh);
    }
  }
  SB();
  LGKM_BAR();
  DSTAMP(30);
  {
#pragma unroll
    for (int r = 0; r < 2; ++r) {
      const int s = 2 * sp + r;
      f32x4 p0[4], p1[4];
#pragma unroll
      for (int ww = 0; ww < 4; ++ww) {
        p0[ww] = *(const LAS f32x4*)(lds0 + ww * 16384 + s * 256 + (((2 * cc) ^ (s & 15)) << 4));
        p1[ww] = *(const LAS f32x4*)(lds0 + ww * 16384 + s * 256 + (((2 * cc + 1) ^ (s & 15)) << 4));
      }
      const f32x4 b0 = (p0[0] + p0[1]) + (p0[2] + p0[3]), b1 = (p1[0] + p1[1]) + (p1[2] + p1[3]);
      u32x4 o;
#pragma unroll
      for (int e = 0; e < 4; ++e) {
        const float bl = e < 2 ? b0[2 * e] : b1[2 * e - 4], bh = e < 2 ? b0[2 * e + 1] : b1[2 * e - 3];
        o[e] = cvtpk(__builtin_fmaf(omg[r], bl, at[r][2 * e]), __builtin_fmaf(omg[r], bh, at[r][2 * e + 1]));
      }
      *(u32x4*)(p.out + (size_t)(s0 + s) * D_ + d0 + cc * 8) = o;
    }
  }
  STAMP(11);
  STAMP_FLUSH(12);
}

extern "C" void kernel_launch(void* const* d_in, const int* in_sizes, int n_in, void* d_out, int out_size, void* d_ws, size_t ws_size, hipStream_t stream) {
  Params p;
  p.q = (const u16*)d_in[0]; p.k = (const u16*)d_in[1]; p.v = (const u16*)d_in[2];
  p.shk = (const u16*)d_in[3]; p.hk = (const u16*)d_in[4]; p.Dv = (const u16*)d_in[5];
  p.wg = (const u16*)d_in[7]; p.wu = (const u16*)d_in[8]; p.wd = (const u16*)d_in[9]; p.gw = (const u16*)d_in[10];
  p.out = (u16*)d_out; p.ws = (char*)d_ws;
  k_fused<<<256, 256, 0, stream>>>(p);
}
```
